```python
import torch

def _softmax(x, axis=-1):
    return torch.softmax(x, dim=axis)
def _gelu(x):
    return torch.nn.functional.gelu(x, approximate="tanh")
def _silu(x):
    return torch.nn.functional.silu(x)
def _sigmoid(x):
    return torch.sigmoid(x)
def _layernorm(x, axis=-1, eps=1e-5):
    m = x.mean(dim=axis, keepdim=True)
    v = x.var(dim=axis, keepdim=True, unbiased=False)
    return (x - m) / torch.sqrt(v + eps)
def _scatter_add(tgt, idx, src, axis=0):
    return tgt.clone().index_add_(axis, idx, src)
def _segment_sum(src, seg, n):
    out = torch.zeros((n,) + src.shape[1:], dtype=src.dtype)
    return out.index_add_(0, seg, src)
def _pad(x, pw):
    flat = tuple(v for pair in reversed(pw) for v in pair)
    return torch.nn.functional.pad(x, flat)

kernel_name = 'gated_mix__linear_attn_block__dense_activation_mish__prompt'

def setup_inputs():

    _w = (lambda a: (lambda t: t.to(torch.bfloat16) if t.is_floating_point() else t)(torch.as_tensor(a)))
    def _A_setup():
        B, S, D = 1, 2048, 576
        return {
            "x": _w((lambda *s: torch.randn(*s, dtype=torch.bfloat16) * 0.05)(B, S, D)),
            "wqkv": _w((lambda *s: torch.randn(*s, dtype=torch.bfloat16) * 0.8)(D, 3 * D)),
            "wo": _w((lambda *s: torch.randn(*s, dtype=torch.bfloat16) * 0.05)(D, D)),
        }
    _A_in = _A_setup()

    def _B_setup():
        B, S, D, F = 1, 2048, 576, 4608
        return {
            "x": _w((lambda *s: torch.randn(*s, dtype=torch.bfloat16) * 0.05)(B, S, D)),
            "w1": _w((lambda *s: torch.randn(*s, dtype=torch.bfloat16) * 12.8)(D, F) * (D ** -0.5)),
            "b1": _w((lambda *s: torch.randn(*s, dtype=torch.bfloat16) * 0.2)(F)),
            "w2": _w((lambda *s: torch.randn(*s, dtype=torch.bfloat16) * 0.05)(F, D) * (F ** -0.5)),
        }
    _B_in = _B_setup()

    return {
        "A_x": _A_in["x"],
        "A_wqkv": _A_in["wqkv"],
        "A_wo": _A_in["wo"],
        "B_x": _B_in["x"],
        "B_w1": _B_in["w1"],
        "B_b1": _B_in["b1"],
        "B_w2": _B_in["w2"],
        "gate_w": _w((lambda *s: torch.randn(*s, dtype=torch.bfloat16) * 0.05)(576) * 16.0),
    }

def reference(A_x, A_wqkv, A_wo, B_x, B_w1, B_b1, B_w2, gate_w):
    def _A_ref(x, wqkv, wo):
        B, S, D = x.shape
        H = 32; Dh = D // H
        qkv = (lambda x, *d: x.permute(*d))((x @ wqkv).reshape(B, S, 3, H, Dh), 2, 0, 3, 1, 4)
        q, k, v = qkv[0], qkv[1], qkv[2]
        phi = lambda t: torch.where(t > 0, t + 1, torch.exp(t)) + 1e-6
        pq, pk = phi(q), phi(k)
        kv = torch.einsum("bhsd,bhse->bhde", pk, v)
        z = torch.einsum("bhsd->bhd", pk)
        num = torch.einsum("bhqd,bhde->bhqe", pq, kv)
        den = torch.einsum("bhqd,bhd->bhq", pq, z)[..., None] + 1e-6
        o = num / den
        return (lambda x, *d: x.permute(*d))(o, 0, 2, 1, 3).reshape(B, S, D) @ wo

    def _B_ref(x, w1, b1, w2):
        h = torch.einsum("bsd,df->bsf", x, w1) + b1
        sp = torch.log1p(torch.exp(-torch.abs(h))) + torch.where(h > 0, h, torch.zeros_like(h))
        act = h * torch.tanh(sp)
        return torch.einsum("bsf,fd->bsd", act, w2)

    x = A_x
    g = _sigmoid((x * gate_w).sum(-1, keepdims=True))
    a = _A_ref(x, A_wqkv, A_wo)
    b = _B_ref(x, B_w1, B_b1, B_w2)
    return g * a + (1 - g) * b
```

```cpp
#include <hip/hip_runtime.h>
#include <hip/hip_bf16.h>
typedef __hip_bfloat16 bf16;
#define S 2048
#define D 576
#define F 4608
#define H 32
#define DH 18
__device__ __forceinline__ float b2f(bf16 x){return __bfloat162float(x);}
__global__ void gemm_naive(const bf16* A, const bf16* B, float* C, int N, int K){
  __shared__ float As[32][33]; __shared__ float Bs[32][33];
  int tx=threadIdx.x&31, ty=threadIdx.x>>5; int row0=blockIdx.y*32, col0=blockIdx.x*32;
  float acc[4]={0,0,0,0};
  for(int k0=0;k0<K;k0+=32){
    for(int i=0;i<4;i++){int r=ty+8*i; As[r][tx]=b2f(A[(size_t)(row0+r)*K+k0+tx]); Bs[r][tx]=b2f(B[(size_t)(k0+r)*N+col0+tx]);}
    __syncthreads();
    for(int k=0;k<32;k++){float b=Bs[k][tx]; for(int i=0;i<4;i++) acc[i]+=As[ty+8*i][k]*b;}
    __syncthreads();
  }
  for(int i=0;i<4;i++) C[(size_t)(row0+ty+8*i)*N+col0+tx]=acc[i];
}
__device__ __forceinline__ float phi(float t){return (t>0.f? t+1.f : __expf(t))+1e-6f;}
__global__ void kv_kernel(const float* qkv, float* kv){
  int h=blockIdx.x; int t=threadIdx.x; if(t>=DH*(DH+1)) return; int d=t/(DH+1), e=t%(DH+1);
  float acc=0; for(int s=0;s<S;s++){float pk=phi(qkv[(size_t)s*3*D+D+h*DH+d]); float v= e<DH? qkv[(size_t)s*3*D+2*D+h*DH+e]:1.f; acc+=pk*v;}
  kv[h*DH*(DH+1)+t]=acc;
}
__global__ void o_kernel(const float* qkv,const float* kv, bf16* o){
  int s=blockIdx.x,c=threadIdx.x,h=c/DH,e=c%DH; float num=0,den=0;
  for(int d=0;d<DH;d++){float pq=phi(qkv[(size_t)s*3*D+h*DH+d]); num+=pq*kv[h*DH*(DH+1)+d*(DH+1)+e]; den+=pq*kv[h*DH*(DH+1)+d*(DH+1)+DH];}
  o[(size_t)s*D+c]=__float2bfloat16(num/(den+1e-6f));
}
__global__ void act_kernel(const float* hh,const bf16* b1,bf16* act){ size_t i=(size_t)blockIdx.x*256+threadIdx.x; int f=i%F;
  float h=hh[i]+b2f(b1[f]); float sp=log1pf(__expf(-fabsf(h)))+(h>0?h:0.f); act[i]=__float2bfloat16(h*tanhf(sp)); }
__global__ void fin_kernel(const bf16* x,const bf16* gw,const float* a,const float* b,bf16* out){
  __shared__ float red[16]; int s=blockIdx.x,c=threadIdx.x; float p=b2f(x[(size_t)s*D+c])*b2f(gw[c]);
  for(int o=32;o>0;o>>=1)p+=__shfl_xor(p,o); if((c&63)==0)red[c>>6]=p; __syncthreads(); float t=0; for(int i=0;i<9;i++)t+=red[i];
  float g=1.f/(1.f+__expf(-t)); out[(size_t)s*D+c]=__float2bfloat16(g*a[(size_t)s*D+c]+(1.f-g)*b[(size_t)s*D+c]); }
extern "C" void kernel_launch(void* const* d_in, const int* in_sizes, int n_in, void* d_out, int out_size, void* d_ws, size_t ws_size, hipStream_t stream) {
  const bf16* x=(const bf16*)d_in[0]; const bf16* wqkv=(const bf16*)d_in[1]; const bf16* wo=(const bf16*)d_in[2];
  const bf16* w1=(const bf16*)d_in[4]; const bf16* b1=(const bf16*)d_in[5]; const bf16* w2=(const bf16*)d_in[6]; const bf16* gw=(const bf16*)d_in[7];
  char* ws=(char*)d_ws; float* qkv=(float*)ws; ws+=(size_t)S*3*D*4; float* hh=(float*)ws; ws+=(size_t)S*F*4; float* kv=(float*)ws; ws+=65536*4;
  bf16* o=(bf16*)ws; ws+=(size_t)S*D*2; bf16* act=(bf16*)ws; ws+=(size_t)S*F*2; float* a=(float*)ws; ws+=(size_t)S*D*4; float* b=(float*)ws;
  gemm_naive<<<dim3(3*D/32,S/32),256,0,stream>>>(x,wqkv,qkv,3*D,D);
  gemm_naive<<<dim3(F/32,S/32),256,0,stream>>>(x,w1,hh,F,D);
  kv_kernel<<<H,384,0,stream>>>(qkv,kv);
  o_kernel<<<S,D,0,stream>>>(qkv,kv,o);
  act_kernel<<<S*F/256,256,0,stream>>>(hh,b1,act);
  gemm_naive<<<dim3(D/32,S/32),256,0,stream>>>(o,wo,a,D,D);
  gemm_naive<<<dim3(D/32,S/32),256,0,stream>>>(act,w2,b,D,F);
  fin_kernel<<<S,D,0,stream>>>(x,gw,a,b,(bf16*)d_out);
}
```

```cpp
#include <hip/hip_runtime.h>
#include <hip/hip_bf16.h>

struct Args { const void *x, *wqkv, *wo, *w1, *b1, *w2, *gw; void *out, *ws; };

__global__ __launch_bounds__(512, 2) void fused_kernel(Args a) {
  unsigned tid = threadIdx.x;
  asm volatile(
    "Lstart:\n"
    "s_load_dwordx16 s[32:47], %1, 0\n"
    "s_load_dwordx2 s[48:49], %1, 64\n"
    "v_and_b32 v1, 63, v0\n"
    "v_lshrrev_b32 v2, 6, v0\n"
    "s_and_b32 s78, %2, 7\n"
    "s_lshr_b32 s53, s78, 1\n"
    "v_readfirstlane_b32 s50, v2\n"
    "s_and_b32 s78, %2, 1\n"
    "s_lshl_b32 s78, s78, 3\n"
    "s_lshr_b32 s54, %2, 5\n"
    "s_add_u32 s54, s54, s78\n"
    "s_lshr_b32 s55, %2, 3\n"
    "s_and_b32 s55, s55, 3\n"
    "s_and_b32 s51, s50, 3\n"
    "s_lshr_b32 s52, s50, 2\n"
    "v_lshlrev_b32 v2, 4, v1\n"
    "v_lshrrev_b32 v8, 3, v1\n"
    "v_and_b32 v9, 7, v1\n"
    "v_lshrrev_b32 v10, 4, v1\n"
    "v_xor_b32 v11, v9, v10\n"
    "v_lshlrev_b32 v11, 4, v11\n"
    "v_mul_u32_u24 v12, 1152, v8\n"
    "v_add_u32 v224, v12, v11\n"
    "v_xor_b32 v11, 64, v11\n"
    "v_add_u32 v12, 9216, v12\n"
    "v_add_u32 v225, v12, v11\n"
    "s_mul_i32 s56, s54, 147456\n"
    "s_mul_i32 s78, s50, 18432\n"
    "s_add_u32 s56, s56, s78\n"
    "s_lshl_b32 s67, s50, 11\n"
    "s_mov_b32 s70, 0x3fb8aa3b\n"
    "s_mov_b32 s90, s52\n"
    "s_mov_b32 s91, 0\n"
    "s_waitcnt lgkmcnt(0)\n"
    "s_mov_b32 s20, s48\n"
    "s_and_b32 s21, s49, 0xffff\n"
    "s_mov_b32 s22, 32768000\n"
    "s_mov_b32 s23, 0x20000\n"
    "s_mov_b32 s8, s32\n"
    "s_and_b32 s9, s33, 0xffff\n"
    "s_mov_b32 s10, 2359296\n"
    "s_mov_b32 s11, 0x20000\n"
    "s_lshl_b32 s78, s54, 8\n"
    "buffer_load_dwordx2 v[4:5], off, s[20:23], s78 sc1\n"
    "s_cmp_eq_u32 s55, 3\n"
    "s_cbranch_scc1 Latt_1\n"
    "s_mov_b32 s12, s38\n"
    "s_and_b32 s13, s39, 0xffff\n"
    "s_mov_b32 s14, 5308416\n"
    "s_mov_b32 s15, 0x20000\n"
    "s_mov_b32 s28, s40\n"
    "s_and_b32 s29, s41, 0xffff\n"
    "s_mov_b32 s30, 9216\n"
    "s_mov_b32 s31, 0x20000\n"
    "s_mul_i32 s79, s53, 2304\n"
    "s_mul_i32 s80, s55, 768\n"
    "s_add_u32 s79, s79, s80\n"
    "v_min_u32 v216, 191, v0\n"
    "v_lshlrev_b32 v216, 2, v216\n"
    "buffer_load_dword v217, v216, s[28:31], s79 offen\n"
    "s_mul_i32 s80, s50, 73728\n"
    "s_add_u32 s57, s79, s80\n"
    "s_mul_i32 s80, s50, 221184\n"
    "s_add_u32 s58, s79, s80\n"
    "s_mul_i32 s69, s50, 3072\n"
    "v_and_b32 v11, 15, v1\n"
    "v_lshlrev_b32 v12, 2, v10\n"
    "v_xor_b32 v11, v11, v12\n"
    "v_lshlrev_b32 v11, 4, v11\n"
    "v_mul_u32_u24 v204, 9216, v10\n"
    "v_add_u32 v204, v204, v11\n"
    "s_add_u32 m0, s67, 0\n"
    "s_nop 0\n"
    "buffer_load_dwordx4 v224, s[8:11], s56 offen lds\n"
    "s_add_u32 m0, s67, 1024\n"
    "s_nop 0\n"
    "buffer_load_dwordx4 v225, s[8:11], s56 offen lds\n"
    "s_add_u32 m0, s67, 16384\n"
    "s_nop 0\n"
    "buffer_load_dwordx4 v204, s[12:15], s57 offen lds\n"
    "s_add_u32 m0, s67, 17408\n"
    "s_add_u32 s78, s57, 36864\n"
    "buffer_load_dwordx4 v204, s[12:15], s78 offen lds\n"
    "s_add_u32 m0, s67, 32768\n"
    "s_add_u32 s78, s56, 128\n"
    "buffer_load_dwordx4 v224, s[8:11], s78 offen lds\n"
    "s_add_u32 m0, s67, 33792\n"
    "s_add_u32 s78, s56, 128\n"
    "buffer_load_dwordx4 v225, s[8:11], s78 offen lds\n"
    "s_add_u32 m0, s67, 49152\n"
    "s_add_u32 s78, s57, 589824\n"
    "buffer_load_dwordx4 v204, s[12:15], s78 offen lds\n"
    "s_add_u32 m0, s67, 50176\n"
    "s_add_u32 s78, s57, 626688\n"
    "buffer_load_dwordx4 v204, s[12:15], s78 offen lds\n"
    "s_add_u32 m0, s67, 65536\n"
    "s_add_u32 s78, s56, 256\n"
    "buffer_load_dwordx4 v224, s[8:11], s78 offen lds\n"
    "s_add_u32 m0, s67, 66560\n"
    "s_add_u32 s78, s56, 256\n"
    "buffer_load_dwordx4 v225, s[8:11], s78 offen lds\n"
    "s_add_u32 m0, s67, 81920\n"
    "s_add_u32 s78, s57, 1179648\n"
    "buffer_load_dwordx4 v204, s[12:15], s78 offen lds\n"
    "s_add_u32 m0, s67, 82944\n"
    "s_add_u32 s78, s57, 1216512\n"
    "buffer_load_dwordx4 v204, s[12:15], s78 offen lds\n"
    "s_add_u32 m0, s67, 98304\n"
    "s_add_u32 s78, s56, 384\n"
    "buffer_load_dwordx4 v224, s[8:11], s78 offen lds\n"
    "s_add_u32 m0, s67, 99328\n"
    "s_add_u32 s78, s56, 384\n"
    "buffer_load_dwordx4 v225, s[8:11], s78 offen lds\n"
    "s_add_u32 m0, s67, 114688\n"
    "s_add_u32 s78, s57, 1769472\n"
    "buffer_load_dwordx4 v204, s[12:15], s78 offen lds\n"
    "s_add_u32 m0, s67, 115712\n"
    "s_add_u32 s78, s57, 1806336\n"
    "buffer_load_dwordx4 v204, s[12:15], s78 offen lds\n"
    "s_add_u32 m0, s67, 131072\n"
    "s_add_u32 s78, s56, 512\n"
    "buffer_load_dwordx4 v224, s[8:11], s78 offen lds\n"
    "s_add_u32 m0, s67, 132096\n"
    "s_add_u32 s78, s56, 512\n"
    "buffer_load_dwordx4 v225, s[8:11], s78 offen lds\n"
    "v_and_b32 v11, 1, v10\n"
    "v_lshlrev_b32 v11, 2, v11\n"
    "v_xor_b32 v11, v9, v11\n"
    "v_lshlrev_b32 v11, 4, v11\n"
    "v_mul_u32_u24 v8, 9216, v8\n"
    "v_add_u32 v8, v8, v11\n"
    "s_mov_b32 s16, s42\n"
    "s_and_b32 s17, s43, 0xffff\n"
    "s_mov_b32 s18, 5308416\n"
    "s_mov_b32 s19, 0x20000\n"
    "v_add_u32 v208, 0, v1\n"
    "v_mul_u32_u24 v209, 683, v208\n"
    "v_lshrrev_b32 v209, 14, v209\n"
    "v_mul_u32_u24 v210, 24, v209\n"
    "v_sub_u32 v208, v208, v210\n"
    "v_bfe_u32 v210, v209, 1, 1\n"
    "v_lshlrev_b32 v210, 2, v210\n"
    "v_xor_b32 v208, v208, v210\n"
    "v_mul_u32_u24 v209, 1152, v209\n"
    "v_lshl_add_u32 v9, v208, 4, v209\n"
    "v_add_u32 v208, 64, v1\n"
    "v_mul_u32_u24 v209, 683, v208\n"
    "v_lshrrev_b32 v209, 14, v209\n"
    "v_mul_u32_u24 v210, 24, v209\n"
    "v_sub_u32 v208, v208, v210\n"
    "v_bfe_u32 v210, v209, 1, 1\n"
    "v_lshlrev_b32 v210, 2, v210\n"
    "v_xor_b32 v208, v208, v210\n"
    "v_mul_u32_u24 v209, 1152, v209\n"
    "v_lshl_add_u32 v10, v208, 4, v209\n"
    "v_add_u32 v208, 128, v1\n"
    "v_mul_u32_u24 v209, 683, v208\n"
    "v_lshrrev_b32 v209, 14, v209\n"
    "v_mul_u32_u24 v210, 24, v209\n"
    "v_sub_u32 v208, v208, v210\n"
    "v_bfe_u32 v210, v209, 1, 1\n"
    "v_lshlrev_b32 v210, 2, v210\n"
    "v_xor_b32 v208, v208, v210\n"
    "v_mul_u32_u24 v209, 1152, v209\n"
    "v_lshl_add_u32 v11, v208, 4, v209\n"
    "v_and_b32 v208, 31, v1\n"
    "s_lshl_b32 s78, s51, 5\n"
    "v_or_b32 v208, s78, v208\n"
    "v_bfe_u32 v209, v208, 1, 3\n"
    "v_lshrrev_b32 v220, 5, v1\n"
    "v_xor_b32 v209, v209, v220\n"
    "v_lshlrev_b32 v209, 4, v209\n"
    "v_lshl_or_b32 v220, v208, 7, v209\n"
    "v_xor_b32 v221, 32, v220\n"
    "v_xor_b32 v222, 64, v220\n"
    "v_xor_b32 v223, 96, v220\n"
    "v_bfe_u32 v208, v1, 5, 1\n"
    "v_bfe_u32 v209, v1, 2, 2\n"
    "v_lshl_or_b32 v208, v208, 3, v209\n"
    "v_bfe_u32 v210, v1, 4, 1\n"
    "v_and_b32 v211, 3, v1\n"
    "v_lshlrev_b32 v211, 3, v211\n"
    "v_lshl_or_b32 v211, v210, 5, v211\n"
    "v_xor_b32 v209, s52, v209\n"
    "v_lshl_or_b32 v209, v209, 6, v211\n"
    "v_lshl_or_b32 v205, v208, 8, v209\n"
    "v_add_u32 v205, 16384, v205\n"
    "v_xor_b32 v206, 128, v205\n"
    "v_bfe_u32 v210, v1, 3, 1\n"
    "v_xor_b32 v209, s52, v210\n"
    "v_lshl_or_b32 v209, v209, 6, v211\n"
    "v_lshl_or_b32 v12, v208, 7, v209\n"
    "s_xor_b32 s79, s52, 0\n"
    "s_mul_i32 s79, s79, 3\n"
    "s_sub_u32 s80, s53, s51\n"
    "s_sub_u32 s80, s80, s79\n"
    "s_and_b32 s80, s80, 3\n"
    "s_cmp_lt_u32 s80, 3\n"
    "s_cselect_b32 s80, s80, 0\n"
    "s_cselect_b32 s77, 1, 0\n"
    "s_add_u32 s81, s80, 0\n"
    "s_cmp_ge_u32 s81, 3\n"
    "s_cselect_b32 s82, 3, 0\n"
    "s_sub_u32 s81, s81, s82\n"
    "s_add_u32 s71, s81, s79\n"
    "s_add_u32 s81, s80, 1\n"
    "s_cmp_ge_u32 s81, 3\n"
    "s_cselect_b32 s82, 3, 0\n"
    "s_sub_u32 s81, s81, s82\n"
    "s_add_u32 s72, s81, s79\n"
    "s_add_u32 s81, s80, 2\n"
    "s_cmp_ge_u32 s81, 3\n"
    "s_cselect_b32 s82, 3, 0\n"
    "s_sub_u32 s81, s81, s82\n"
    "s_add_u32 s73, s81, s79\n"
    "s_xor_b32 s79, s52, 1\n"
    "s_mul_i32 s79, s79, 3\n"
    "s_sub_u32 s80, s53, s51\n"
    "s_sub_u32 s80, s80, s79\n"
    "s_and_b32 s80, s80, 3\n"
    "s_cmp_lt_u32 s80, 3\n"
    "s_cselect_b32 s80, s80, 0\n"
    "s_add_u32 s81, s80, 0\n"
    "s_cmp_ge_u32 s81, 3\n"
    "s_cselect_b32 s82, 3, 0\n"
    "s_sub_u32 s81, s81, s82\n"
    "s_add_u32 s74, s81, s79\n"
    "s_add_u32 s81, s80, 1\n"
    "s_cmp_ge_u32 s81, 3\n"
    "s_cselect_b32 s82, 3, 0\n"
    "s_sub_u32 s81, s81, s82\n"
    "s_add_u32 s75, s81, s79\n"
    "s_add_u32 s81, s80, 2\n"
    "s_cmp_ge_u32 s81, 3\n"
    "s_cselect_b32 s82, 3, 0\n"
    "s_sub_u32 s81, s81, s82\n"
    "s_add_u32 s76, s81, s79\n"
    "v_bfe_u32 v208, v1, 5, 1\n"
    "v_bfe_u32 v209, v1, 2, 2\n"
    "v_lshl_or_b32 v208, v208, 2, v209\n"
    "s_lshl_b32 s79, s52, 5\n"
    "v_or_b32 v208, s79, v208\n"
    "v_mul_u32_u24 v208, 384, v208\n"
    "v_add_u32 v208, v208, v211\n"
    "v_xor_b32 v209, s71, v210\n"
    "v_lshl_add_u32 v226, v209, 6, v208\n"
    "v_xor_b32 v209, s72, v210\n"
    "v_lshl_add_u32 v227, v209, 6, v208\n"
    "v_xor_b32 v209, s73, v210\n"
    "v_lshl_add_u32 v228, v209, 6, v208\n"
    "v_xor_b32 v209, s74, v210\n"
    "v_lshl_add_u32 v229, v209, 6, v208\n"
    "v_xor_b32 v209, s75, v210\n"
    "v_lshl_add_u32 v230, v209, 6, v208\n"
    "v_xor_b32 v209, s76, v210\n"
    "v_lshl_add_u32 v231, v209, 6, v208\n"
    "v_bfe_u32 v208, v1, 5, 1\n"
    "s_lshl_b32 s78, s52, 7\n"
    "s_add_u32 s78, s78, 147456\n"
    "v_lshl_add_u32 v14, v208, 4, s78\n"
    "v_and_b32 v3, 3, v1\n"
    "v_lshlrev_b32 v3, 3, v3\n"
    "s_lshl_b32 s79, 1, s55\n"
    "s_andn2_b32 s88, 7, s79\n"
    "s_mov_b32 s89, 0\n"
    "s_lshl_b32 s79, s54, 2\n"
    "s_add_u32 s79, s79, s53\n"
    "s_lshl_b32 s80, s79, 3\n"
    "s_add_u32 s80, s80, s50\n"
    "s_lshl_b32 s80, s80, 7\n"
    "s_add_u32 s60, s80, 8192\n"
    "s_lshl_b32 s80, s79, 2\n"
    "s_add_u32 s80, s80, s51\n"
    "s_lshl_b32 s98, s80, 7\n"
    "s_mul_i32 s80, s79, 36864\n"
    "s_lshl_b32 s81, s51, 10\n"
    "s_add_u32 s80, s80, s81\n"
    "s_add_u32 s97, s80, 2097152\n"
    "s_mul_i32 s79, s79, 294912\n"
    "s_lshl_b32 s80, s50, 11\n"
    "s_add_u32 s79, s79, s80\n"
    "s_add_u32 s79, s79, 4456448\n"
    "s_mul_i32 s81, s55, 384\n"
    "s_mul_i32 s82, s50, 9216\n"
    "s_add_u32 s81, s81, s82\n"
    "s_mul_i32 s80, s53, 165888\n"
    "s_add_u32 s68, s80, s81\n"
    "s_and_b32 s80, s50, 6\n"
    "s_mul_i32 s80, s80, 1152\n"
    "s_add_u32 s68, s68, s80\n"
    "s_mul_i32 s80, s53, 1327104\n"
    "s_add_u32 s80, s80, s81\n"
    "s_add_u32 s81, s55, 0\n"
    "s_cmp_ge_u32 s81, 3\n"
    "s_cselect_b32 s82, 3, 0\n"
    "s_sub_u32 s81, s81, s82\n"
    "s_mul_i32 s82, s81, 98304\n"
    "s_add_u32 s64, s82, s79\n"
    "s_mul_i32 s82, s81, 442368\n"
    "s_add_u32 s61, s82, s80\n"
    "s_add_u32 s81, s55, 1\n"
    "s_cmp_ge_u32 s81, 3\n"
    "s_cselect_b32 s82, 3, 0\n"
    "s_sub_u32 s81, s81, s82\n"
    "s_mul_i32 s82, s81, 98304\n"
    "s_add_u32 s65, s82, s79\n"
    "s_mul_i32 s82, s81, 442368\n"
    "s_add_u32 s62, s82, s80\n"
    "s_add_u32 s81, s55, 2\n"
    "s_cmp_ge_u32 s81, 3\n"
    "s_cselect_b32 s82, 3, 0\n"
    "s_sub_u32 s81, s81, s82\n"
    "s_mul_i32 s82, s81, 98304\n"
    "s_add_u32 s66, s82, s79\n"
    "s_mul_i32 s82, s81, 442368\n"
    "s_add_u32 s63, s82, s80\n"
    "s_lshl_b32 s81, s55, 3\n"
    "s_add_u32 s59, s60, s81\n"
    "s_mul_i32 s99, s50, 768\n"
    "s_waitcnt vmcnt(19)\n"
    "v_add_co_u32 v4, vcc, 1, v4\n"
    "s_nop 1\n"
    "v_addc_co_u32 v5, vcc, 0, v5, vcc\n"
    "s_waitcnt vmcnt(18)\n"
    "v_lshlrev_b32 v218, 16, v217\n"
    "v_and_b32 v219, 0xffff0000, v217\n"
    "v_lshlrev_b32 v216, 1, v216\n"
    "v_add_u32 v216, 147456, v216\n"
    "ds_write_b64 v216, v[218:219]\n"
    "s_mov_b32 s28, s36\n"
    "s_and_b32 s29, s37, 0xffff\n"
    "s_mov_b32 s30, 663552\n"
    "s_mov_b32 s31, 0x20000\n"
    "s_waitcnt vmcnt(14) lgkmcnt(0)\n"
    "s_barrier\n"
    "s_and_b32 s79, %2, 1\n"
    "s_lshl_b32 s79, s79, 5\n"
    "s_lshr_b32 s80, %2, 3\n"
    "s_add_u32 s79, s79, s80\n"
    "s_lshl_b32 s80, s50, 6\n"
    "s_add_u32 s79, s79, s80\n"
    "s_mov_b32 s27, 0x20000\n"
    "s_cmp_ge_u32 s79, 162\n"
    "s_cbranch_scc1 Lpq_4\n"
    "s_sub_u32 s79, s79, 0\n"
    "s_lshl_b32 s79, s79, 6\n"
    "v_add_u32 v0, s79, v1\n"
    "s_mov_b32 s24, s38\n"
    "s_and_b32 s25, s39, 0xffff\n"
    "s_mov_b32 s26, 5308416\n"
    "v_mul_u32_u24 v208, 3641, v0\n"
    "v_lshrrev_b32 v208, 16, v208\n"
    "v_mul_u32_u24 v209, 18, v208\n"
    "v_sub_u32 v0, v0, v209\n"
    "v_mul_u32_u24 v208, 9216, v208\n"
    "v_lshl_add_u32 v0, v0, 7, v208\n"
    "s_mul_i32 s79, s53, 2304\n"
    "s_branch Lpgo_6\n"
    "Lpq_4:\n"
    "s_cmp_ge_u32 s79, 243\n"
    "s_cbranch_scc1 Lpw1_3\n"
    "s_sub_u32 s79, s79, 162\n"
    "s_lshl_b32 s79, s79, 6\n"
    "v_add_u32 v0, s79, v1\n"
    "s_mov_b32 s24, s34\n"
    "s_and_b32 s25, s35, 0xffff\n"
    "s_mov_b32 s26, 1990656\n"
    "v_mul_u32_u24 v208, 21846, v0\n"
    "v_lshrrev_b32 v208, 16, v208\n"
    "v_mul_u32_u24 v209, 3, v208\n"
    "v_sub_u32 v0, v0, v209\n"
    "v_mul_u32_u24 v208, 1152, v208\n"
    "v_lshl_add_u32 v0, v0, 7, v208\n"
    "s_lshl_b32 s79, s53, 8\n"
    "s_branch Lpgo_6\n"
    "Lpw1_3:\n"
    "s_cmp_ge_u32 s79, 405\n"
    "s_cbranch_scc1 Lpo_5\n"
    "s_sub_u32 s79, s79, 243\n"
    "s_lshl_b32 s79, s79, 6\n"
    "v_add_u32 v0, s79, v1\n"
    "s_mov_b32 s24, s42\n"
    "s_and_b32 s25, s43, 0xffff\n"
    "s_mov_b32 s26, 5308416\n"
    "v_lshlrev_b32 v0, 7, v0\n"
    "s_mul_i32 s79, s53, 1327104\n"
    "s_branch Lpgo_6\n"
    "Lpo_5:\n"
    "s_cmp_ge_u32 s79, 426\n"
    "s_cbranch_scc1 Lpskip_7\n"
    "s_sub_u32 s79, s79, 405\n"
    "s_lshl_b32 s79, s79, 6\n"
    "v_add_u32 v0, s79, v1\n"
    "s_mov_b32 s24, s36\n"
    "s_and_b32 s25, s37, 0xffff\n"
    "s_mov_b32 s26, 663552\n"
    "v_lshlrev_b32 v0, 7, v0\n"
    "s_mul_i32 s79, s53, 165888\n"
    "Lpgo_6:\n"
    "buffer_load_dword v207, v0, s[24:27], s79 offen\n"
    "Lpskip_7:\n"
    "ds_read_b128 v[160:163], v14 offset:0\n"
    "ds_read_b128 v[164:167], v14 offset:32\n"
    "ds_read_b128 v[168:171], v14 offset:64\n"
    "ds_read_b128 v[172:175], v14 offset:96\n"
    "ds_read_b128 v[176:179], v14 offset:256\n"
    "ds_read_b128 v[180:183], v14 offset:288\n"
    "ds_read_b128 v[184:187], v14 offset:320\n"
    "ds_read_b128 v[188:191], v14 offset:352\n"
    "ds_read_b128 v[238:241], v14 offset:512\n"
    "ds_read_b128 v[242:245], v14 offset:544\n"
    "ds_read_b128 v[246:249], v14 offset:576\n"
    "ds_read_b128 v[250:253], v14 offset:608\n"
    "ds_read_b128 v[16:19], v220\n"
    "ds_read_b64_tr_b16 v[192:193], v205 offset:0\n"
    "ds_read_b64_tr_b16 v[194:195], v205 offset:1024\n"
    "ds_read_b64_tr_b16 v[196:197], v206 offset:0\n"
    "ds_read_b64_tr_b16 v[198:199], v206 offset:1024\n"
    "s_waitcnt lgkmcnt(2)\n"
    "v_mfma_f32_32x32x16_bf16 v[160:175], v[192:195], v[16:19], v[160:175]\n"
    "ds_read_b128 v[20:23], v221\n"
    "ds_read_b64_tr_b16 v[200:201], v205 offset:4096\n"
    "ds_read_b64_tr_b16 v[202:203], v205 offset:5120\n"
    "s_waitcnt lgkmcnt(3)\n"
    "v_mfma_f32_32x32x16_bf16 v[176:191], v[196:199], v[16:19], v[176:191]\n"
    "ds_read_b64_tr_b16 v[192:193], v206 offset:4096\n"
    "ds_read_b64_tr_b16 v[194:195], v206 offset:5120\n"
    "s_waitcnt lgkmcnt(2)\n"
    "v_mfma_f32_32x32x16_bf16 v[160:175], v[200:203], v[20:23], v[160:175]\n"
    "ds_read_b128 v[24:27], v222\n"
    "ds_read_b64_tr_b16 v[196:197], v205 offset:8192\n"
    "ds_read_b64_tr_b16 v[198:199], v205 offset:9216\n"
    "s_waitcnt lgkmcnt(3)\n"
    "v_mfma_f32_32x32x16_bf16 v[176:191], v[192:195], v[20:23], v[176:191]\n"
    "ds_read_b64_tr_b16 v[200:201], v206 offset:8192\n"
    "ds_read_b64_tr_b16 v[202:203], v206 offset:9216\n"
    "s_waitcnt lgkmcnt(2)\n"
    "v_mfma_f32_32x32x16_bf16 v[160:175], v[196:199], v[24:27], v[160:175]\n"
    "ds_read_b128 v[28:31], v223\n"
    "ds_read_b64_tr_b16 v[192:193], v205 offset:12288\n"
    "ds_read_b64_tr_b16 v[194:195], v205 offset:13312\n"
    "s_waitcnt lgkmcnt(3)\n"
    "v_mfma_f32_32x32x16_bf16 v[176:191], v[200:203], v[24:27], v[176:191]\n"
    "ds_read_b64_tr_b16 v[196:197], v206 offset:12288\n"
    "ds_read_b64_tr_b16 v[198:199], v206 offset:13312\n"
    "s_waitcnt lgkmcnt(2)\n"
    "v_mfma_f32_32x32x16_bf16 v[160:175], v[192:195], v[28:31], v[160:175]\n"
    "s_waitcnt vmcnt(10)\n"
    "s_barrier\n"
    "v_add_u32 v220, 32768, v220\n"
    "v_add_u32 v221, 32768, v221\n"
    "v_add_u32 v222, 32768, v222\n"
    "v_add_u32 v223, 32768, v223\n"
    "v_add_u32 v205, 32768, v205\n"
    "v_add_u32 v206, 32768, v206\n"
    "ds_read_b128 v[32:35], v220\n"
    "ds_read_b64_tr_b16 v[200:201], v205 offset:0\n"
    "ds_read_b64_tr_b16 v[202:203], v205 offset:1024\n"
    "s_waitcnt lgkmcnt(3)\n"
    "v_mfma_f32_32x32x16_bf16 v[176:191], v[196:199], v[28:31], v[176:191]\n"
    "ds_read_b64_tr_b16 v[192:193], v206 offset:0\n"
    "ds_read_b64_tr_b16 v[194:195], v206 offset:1024\n"
    "s_waitcnt lgkmcnt(2)\n"
    "v_mfma_f32_32x32x16_bf16 v[160:175], v[200:203], v[32:35], v[160:175]\n"
    "ds_read_b128 v[36:39], v221\n"
    "ds_read_b64_tr_b16 v[196:197], v205 offset:4096\n"
    "ds_read_b64_tr_b16 v[198:199], v205 offset:5120\n"
    "s_waitcnt lgkmcnt(3)\n"
    "v_mfma_f32_32x32x16_bf16 v[176:191], v[192:195], v[32:35], v[176:191]\n"
    "ds_read_b64_tr_b16 v[200:201], v206 offset:4096\n"
    "ds_read_b64_tr_b16 v[202:203], v206 offset:5120\n"
    "s_waitcnt lgkmcnt(2)\n"
    "v_mfma_f32_32x32x16_bf16 v[160:175], v[196:199], v[36:39], v[160:175]\n"
    "ds_read_b128 v[40:43], v222\n"
    "ds_read_b64_tr_b16 v[192:193], v205 offset:8192\n"
    "ds_read_b64_tr_b16 v[194:195], v205 offset:9216\n"
    "s_waitcnt lgkmcnt(3)\n"
    "v_mfma_f32_32x32x16_bf16 v[176:191], v[200:203], v[36:39], v[176:191]\n"
    "ds_read_b64_tr_b16 v[196:197], v206 offset:8192\n"
    "ds_read_b64_tr_b16 v[198:199], v206 offset:9216\n"
    "s_waitcnt lgkmcnt(2)\n"
    "v_mfma_f32_32x32x16_bf16 v[160:175], v[192:195], v[40:43], v[160:175]\n"
    "ds_read_b128 v[44:47], v223\n"
    "ds_read_b64_tr_b16 v[200:201], v205 offset:12288\n"
    "ds_read_b64_tr_b16 v[202:203], v205 offset:13312\n"
    "s_waitcnt lgkmcnt(3)\n"
    "v_mfma_f32_32x32x16_bf16 v[176:191], v[196:199], v[40:43], v[176:191]\n"
    "ds_read_b64_tr_b16 v[192:193], v206 offset:12288\n"
    "ds_read_b64_tr_b16 v[194:195], v206 offset:13312\n"
    "s_waitcnt lgkmcnt(2)\n"
    "v_mfma_f32_32x32x16_bf16 v[160:175], v[200:203], v[44:47], v[160:175]\n"
    "s_waitcnt vmcnt(6)\n"
    "s_barrier\n"
    "v_add_u32 v220, 32768, v220\n"
    "v_add_u32 v221, 32768, v221\n"
    "v_add_u32 v222, 32768, v222\n"
    "v_add_u32 v223, 32768, v223\n"
    "v_add_u32 v205, 32768, v205\n"
    "v_add_u32 v206, 32768, v206\n"
    "ds_read_b128 v[48:51], v220\n"
    "ds_read_b64_tr_b16 v[196:197], v205 offset:0\n"
    "ds_read_b64_tr_b16 v[198:199], v205 offset:1024\n"
    "s_add_u32 m0, s67, 0\n"
    "s_add_u32 s78, s57, 2359296\n"
    "buffer_load_dwordx4 v204, s[12:15], s78 offen lds\n"
    "s_waitcnt lgkmcnt(3)\n"
    "v_mfma_f32_32x32x16_bf16 v[176:191], v[192:195], v[44:47], v[176:191]\n"
    "ds_read_b64_tr_b16 v[200:201], v206 offset:0\n"
    "ds_read_b64_tr_b16 v[202:203], v206 offset:1024\n"
    "s_waitcnt lgkmcnt(2)\n"
    "v_mfma_f32_32x32x16_bf16 v[160:175], v[196:199], v[48:51], v[160:175]\n"
    "ds_read_b128 v[52:55], v221\n"
    "ds_read_b64_tr_b16 v[192:193], v205 offset:4096\n"
    "ds_read_b64_tr_b16 v[194:195], v205 offset:5120\n"
    "s_add_u32 m0, s67, 1024\n"
    "s_add_u32 s78, s57, 2396160\n"
    "buffer_load_dwordx4 v204, s[12:15], s78 offen lds\n"
    "s_waitcnt lgkmcnt(3)\n"
    "v_mfma_f32_32x32x16_bf16 v[176:191], v[200:203], v[48:51], v[176:191]\n"
    "ds_read_b64_tr_b16 v[196:197], v206 offset:4096\n"
    "ds_read_b64_tr_b16 v[198:199], v206 offset:5120\n"
    "s_waitcnt lgkmcnt(2)\n"
    "v_mfma_f32_32x32x16_bf16 v[160:175], v[192:195], v[52:55], v[160:175]\n"
    "ds_read_b128 v[56:59], v222\n"
    "ds_read_b64_tr_b16 v[200:201], v205 offset:8192\n"
    "ds_read_b64_tr_b16 v[202:203], v205 offset:9216\n"
    "s_add_u32 m0, s67, 16384\n"
    "s_add_u32 s78, s56, 640\n"
    "buffer_load_dwordx4 v224, s[8:11], s78 offen lds\n"
    "s_waitcnt lgkmcnt(3)\n"
    "v_mfma_f32_32x32x16_bf16 v[176:191], v[196:199], v[52:55], v[176:191]\n"
    "ds_read_b64_tr_b16 v[192:193], v206 offset:8192\n"
    "ds_read_b64_tr_b16 v[194:195], v206 offset:9216\n"
    "s_waitcnt lgkmcnt(2)\n"
    "v_mfma_f32_32x32x16_bf16 v[160:175], v[200:203], v[56:59], v[160:175]\n"
    "ds_read_b128 v[60:63], v223\n"
    "ds_read_b64_tr_b16 v[196:197], v205 offset:12288\n"
    "ds_read_b64_tr_b16 v[198:199], v205 offset:13312\n"
    "s_add_u32 m0, s67, 17408\n"
    "s_add_u32 s78, s56, 640\n"
    "buffer_load_dwordx4 v225, s[8:11], s78 offen lds\n"
    "s_waitcnt lgkmcnt(3)\n"
    "v_mfma_f32_32x32x16_bf16 v[176:191], v[192:195], v[56:59], v[176:191]\n"
    "ds_read_b64_tr_b16 v[200:201], v206 offset:12288\n"
    "ds_read_b64_tr_b16 v[202:203], v206 offset:13312\n"
    "s_waitcnt lgkmcnt(2)\n"
    "v_mfma_f32_32x32x16_bf16 v[160:175], v[196:199], v[60:63], v[160:175]\n"
    "s_waitcnt vmcnt(6)\n"
    "s_barrier\n"
    "v_add_u32 v220, 32768, v220\n"
    "v_add_u32 v221, 32768, v221\n"
    "v_add_u32 v222, 32768, v222\n"
    "v_add_u32 v223, 32768, v223\n"
    "v_add_u32 v205, 32768, v205\n"
    "v_add_u32 v206, 32768, v206\n"
    "ds_read_b128 v[64:67], v220\n"
    "ds_read_b64_tr_b16 v[192:193], v205 offset:0\n"
    "ds_read_b64_tr_b16 v[194:195], v205 offset:1024\n"
    "s_add_u32 m0, s67, 32768\n"
    "s_add_u32 s78, s57, 2949120\n"
    "buffer_load_dwordx4 v204, s[12:15], s78 offen lds\n"
    "s_waitcnt lgkmcnt(3)\n"
    "v_mfma_f32_32x32x16_bf16 v[176:191], v[200:203], v[60:63], v[176:191]\n"
    "ds_read_b64_tr_b16 v[196:197], v206 offset:0\n"
    "ds_read_b64_tr_b16 v[198:199], v206 offset:1024\n"
    "s_waitcnt lgkmcnt(2)\n"
    "v_mfma_f32_32x32x16_bf16 v[160:175], v[192:195], v[64:67], v[160:175]\n"
    "ds_read_b128 v[68:71], v221\n"
    "ds_read_b64_tr_b16 v[200:201], v205 offset:4096\n"
    "ds_read_b64_tr_b16 v[202:203], v205 offset:5120\n"
    "s_add_u32 m0, s67, 33792\n"
    "s_add_u32 s78, s57, 2985984\n"
    "buffer_load_dwordx4 v204, s[12:15], s78 offen lds\n"
    "s_waitcnt lgkmcnt(3)\n"
    "v_mfma_f32_32x32x16_bf16 v[176:191], v[196:199], v[64:67], v[176:191]\n"
    "ds_read_b64_tr_b16 v[192:193], v206 offset:4096\n"
    "ds_read_b64_tr_b16 v[194:195], v206 offset:5120\n"
    "s_waitcnt lgkmcnt(2)\n"
    "v_mfma_f32_32x32x16_bf16 v[160:175], v[200:203], v[68:71], v[160:175]\n"
    "ds_read_b128 v[72:75], v222\n"
    "ds_read_b64_tr_b16 v[196:197], v205 offset:8192\n"
    "ds_read_b64_tr_b16 v[198:199], v205 offset:9216\n"
    "s_add_u32 m0, s67, 49152\n"
    "s_add_u32 s78, s56, 768\n"
    "buffer_load_dwordx4 v224, s[8:11], s78 offen lds\n"
    "s_waitcnt lgkmcnt(3)\n"
    "v_mfma_f32_32x32x16_bf16 v[176:191], v[192:195], v[68:71], v[176:191]\n"
    "ds_read_b64_tr_b16 v[200:201], v206 offset:8192\n"
    "ds_read_b64_tr_b16 v[202:203], v206 offset:9216\n"
    "s_waitcnt lgkmcnt(2)\n"
    "v_mfma_f32_32x32x16_bf16 v[160:175], v[196:199], v[72:75], v[160:175]\n"
    "ds_read_b128 v[76:79], v223\n"
    "ds_read_b64_tr_b16 v[192:193], v205 offset:12288\n"
    "ds_read_b64_tr_b16 v[194:195], v205 offset:13312\n"
    "s_add_u32 m0, s67, 50176\n"
    "s_add_u32 s78, s56, 768\n"
    "buffer_load_dwordx4 v225, s[8:11], s78 offen lds\n"
    "s_waitcnt lgkmcnt(3)\n"
    "v_mfma_f32_32x32x16_bf16 v[176:191], v[200:203], v[72:75], v[176:191]\n"
    "ds_read_b64_tr_b16 v[196:197], v206 offset:12288\n"
    "ds_read_b64_tr_b16 v[198:199], v206 offset:13312\n"
    "s_waitcnt lgkmcnt(2)\n"
    "v_mfma_f32_32x32x16_bf16 v[160:175], v[192:195], v[76:79], v[160:175]\n"
    "s_waitcnt vmcnt(6)\n"
    "s_barrier\n"
    "v_add_u32 v220, 32768, v220\n"
    "v_add_u32 v221, 32768, v221\n"
    "v_add_u32 v222, 32768, v222\n"
    "v_add_u32 v223, 32768, v223\n"
    "v_add_u32 v205, 4294852608, v205\n"
    "v_add_u32 v206, 4294852608, v206\n"
    "ds_read_b128 v[80:83], v220\n"
    "ds_read_b64_tr_b16 v[200:201], v205 offset:0\n"
    "ds_read_b64_tr_b16 v[202:203], v205 offset:1024\n"
    "s_add_u32 m0, s67, 65536\n"
    "s_add_u32 s78, s57, 3538944\n"
    "buffer_load_dwordx4 v204, s[12:15], s78 offen lds\n"
    "s_waitcnt lgkmcnt(3)\n"
    "v_mfma_f32_32x32x16_bf16 v[176:191], v[196:199], v[76:79], v[176:191]\n"
    "ds_read_b64_tr_b16 v[192:193], v206 offset:0\n"
    "ds_read_b64_tr_b16 v[194:195], v206 offset:1024\n"
    "s_waitcnt lgkmcnt(2)\n"
    "v_mfma_f32_32x32x16_bf16 v[160:175], v[200:203], v[80:83], v[160:175]\n"
    "ds_read_b128 v[84:87], v221\n"
    "ds_read_b64_tr_b16 v[196:197], v205 offset:4096\n"
    "ds_read_b64_tr_b16 v[198:199], v205 offset:5120\n"
    "s_add_u32 m0, s67, 66560\n"
    "s_add_u32 s78, s57, 3575808\n"
    "buffer_load_dwordx4 v204, s[12:15], s78 offen lds\n"
    "s_waitcnt lgkmcnt(3)\n"
    "v_mfma_f32_32x32x16_bf16 v[176:191], v[192:195], v[80:83], v[176:191]\n"
    "ds_read_b64_tr_b16 v[200:201], v206 offset:4096\n"
    "ds_read_b64_tr_b16 v[202:203], v206 offset:5120\n"
    "s_waitcnt lgkmcnt(2)\n"
    "v_mfma_f32_32x32x16_bf16 v[160:175], v[196:199], v[84:87], v[160:175]\n"
    "ds_read_b128 v[88:91], v222\n"
    "ds_read_b64_tr_b16 v[192:193], v205 offset:8192\n"
    "ds_read_b64_tr_b16 v[194:195], v205 offset:9216\n"
    "s_add_u32 m0, s67, 81920\n"
    "s_add_u32 s78, s56, 896\n"
    "buffer_load_dwordx4 v224, s[8:11], s78 offen lds\n"
    "s_waitcnt lgkmcnt(3)\n"
    "v_mfma_f32_32x32x16_bf16 v[176:191], v[200:203], v[84:87], v[176:191]\n"
    "ds_read_b64_tr_b16 v[196:197], v206 offset:8192\n"
    "ds_read_b64_tr_b16 v[198:199], v206 offset:9216\n"
    "s_waitcnt lgkmcnt(2)\n"
    "v_mfma_f32_32x32x16_bf16 v[160:175], v[192:195], v[88:91], v[160:175]\n"
    "ds_read_b128 v[92:95], v223\n"
    "ds_read_b64_tr_b16 v[200:201], v205 offset:12288\n"
    "ds_read_b64_tr_b16 v[202:203], v205 offset:13312\n"
    "s_add_u32 m0, s67, 82944\n"
    "s_add_u32 s78, s56, 896\n"
    "buffer_load_dwordx4 v225, s[8:11], s78 offen lds\n"
    "s_waitcnt lgkmcnt(3)\n"
    "v_mfma_f32_32x32x16_bf16 v[176:191], v[196:199], v[88:91], v[176:191]\n"
    "ds_read_b64_tr_b16 v[192:193], v206 offset:12288\n"
    "ds_read_b64_tr_b16 v[194:195], v206 offset:13312\n"
    "s_waitcnt lgkmcnt(2)\n"
    "v_mfma_f32_32x32x16_bf16 v[160:175], v[200:203], v[92:95], v[160:175]\n"
    "s_waitcnt vmcnt(6)\n"
    "s_barrier\n"
    "v_add_u32 v220, 4294852608, v220\n"
    "v_add_u32 v221, 4294852608, v221\n"
    "v_add_u32 v222, 4294852608, v222\n"
    "v_add_u32 v223, 4294852608, v223\n"
    "v_add_u32 v205, 32768, v205\n"
    "v_add_u32 v206, 32768, v206\n"
    "ds_read_b128 v[96:99], v220\n"
    "ds_read_b64_tr_b16 v[196:197], v205 offset:0\n"
    "ds_read_b64_tr_b16 v[198:199], v205 offset:1024\n"
    "s_add_u32 m0, s67, 98304\n"
    "s_add_u32 s78, s57, 4128768\n"
    "buffer_load_dwordx4 v204, s[12:15], s78 offen lds\n"
    "s_waitcnt lgkmcnt(3)\n"
    "v_mfma_f32_32x32x16_bf16 v[176:191], v[192:195], v[92:95], v[176:191]\n"
    "ds_read_b64_tr_b16 v[200:201], v206 offset:0\n"
    "ds_read_b64_tr_b16 v[202:203], v206 offset:1024\n"
    "s_waitcnt lgkmcnt(2)\n"
    "v_mfma_f32_32x32x16_bf16 v[160:175], v[196:199], v[96:99], v[160:175]\n"
    "ds_read_b128 v[100:103], v221\n"
    "ds_read_b64_tr_b16 v[192:193], v205 offset:4096\n"
    "ds_read_b64_tr_b16 v[194:195], v205 offset:5120\n"
    "s_add_u32 m0, s67, 99328\n"
    "s_add_u32 s78, s57, 4165632\n"
    "buffer_load_dwordx4 v204, s[12:15], s78 offen lds\n"
    "s_waitcnt lgkmcnt(3)\n"
    "v_mfma_f32_32x32x16_bf16 v[176:191], v[200:203], v[96:99], v[176:191]\n"
    "ds_read_b64_tr_b16 v[196:197], v206 offset:4096\n"
    "ds_read_b64_tr_b16 v[198:199], v206 offset:5120\n"
    "s_waitcnt lgkmcnt(2)\n"
    "v_mfma_f32_32x32x16_bf16 v[160:175], v[192:195], v[100:103], v[160:175]\n"
    "ds_read_b128 v[104:107], v222\n"
    "ds_read_b64_tr_b16 v[200:201], v205 offset:8192\n"
    "ds_read_b64_tr_b16 v[202:203], v205 offset:9216\n"
    "s_add_u32 m0, s67, 114688\n"
    "s_add_u32 s78, s56, 1024\n"
    "buffer_load_dwordx4 v224, s[8:11], s78 offen lds\n"
    "s_waitcnt lgkmcnt(3)\n"
    "v_mfma_f32_32x32x16_bf16 v[176:191], v[196:199], v[100:103], v[176:191]\n"
    "ds_read_b64_tr_b16 v[192:193], v206 offset:8192\n"
    "ds_read_b64_tr_b16 v[194:195], v206 offset:9216\n"
    "s_waitcnt lgkmcnt(2)\n"
    "v_mfma_f32_32x32x16_bf16 v[160:175], v[200:203], v[104:107], v[160:175]\n"
    "ds_read_b128 v[108:111], v223\n"
    "ds_read_b64_tr_b16 v[196:197], v205 offset:12288\n"
    "ds_read_b64_tr_b16 v[198:199], v205 offset:13312\n"
    "s_add_u32 m0, s67, 115712\n"
    "s_add_u32 s78, s56, 1024\n"
    "buffer_load_dwordx4 v225, s[8:11], s78 offen lds\n"
    "s_waitcnt lgkmcnt(3)\n"
    "v_mfma_f32_32x32x16_bf16 v[176:191], v[192:195], v[104:107], v[176:191]\n"
    "ds_read_b64_tr_b16 v[200:201], v206 offset:12288\n"
    "ds_read_b64_tr_b16 v[202:203], v206 offset:13312\n"
    "s_waitcnt lgkmcnt(2)\n"
    "v_mfma_f32_32x32x16_bf16 v[160:175], v[196:199], v[108:111], v[160:175]\n"
    "s_waitcnt vmcnt(6)\n"
    "s_barrier\n"
    "v_add_u32 v220, 32768, v220\n"
    "v_add_u32 v221, 32768, v221\n"
    "v_add_u32 v222, 32768, v222\n"
    "v_add_u32 v223, 32768, v223\n"
    "v_add_u32 v205, 32768, v205\n"
    "v_add_u32 v206, 32768, v206\n"
    "ds_read_b128 v[112:115], v220\n"
    "ds_read_b64_tr_b16 v[192:193], v205 offset:0\n"
    "ds_read_b64_tr_b16 v[194:195], v205 offset:1024\n"
    "s_add_u32 m0, s67, 131072\n"
    "s_add_u32 s78, s57, 4718592\n"
    "buffer_load_dwordx4 v204, s[12:15], s78 offen lds\n"
    "s_waitcnt lgkmcnt(3)\n"
    "v_mfma_f32_32x32x16_bf16 v[176:191], v[200:203], v[108:111], v[176:191]\n"
    "ds_read_b64_tr_b16 v[196:197], v206 offset:0\n"
    "ds_read_b64_tr_b16 v[198:199], v206 offset:1024\n"
    "s_waitcnt lgkmcnt(2)\n"
    "v_mfma_f32_32x32x16_bf16 v[160:175], v[192:195], v[112:115], v[160:175]\n"
    "ds_read_b128 v[116:119], v221\n"
    "ds_read_b64_tr_b16 v[200:201], v205 offset:4096\n"
    "ds_read_b64_tr_b16 v[202:203], v205 offset:5120\n"
    "s_waitcnt lgkmcnt(3)\n"
    "v_mfma_f32_32x32x16_bf16 v[176:191], v[196:199], v[112:115], v[176:191]\n"
    "ds_read_b64_tr_b16 v[192:193], v206 offset:4096\n"
    "ds_read_b64_tr_b16 v[194:195], v206 offset:5120\n"
    "s_waitcnt lgkmcnt(2)\n"
    "v_mfma_f32_32x32x16_bf16 v[160:175], v[200:203], v[116:119], v[160:175]\n"
    "ds_read_b128 v[120:123], v222\n"
    "ds_read_b64_tr_b16 v[196:197], v205 offset:8192\n"
    "ds_read_b64_tr_b16 v[198:199], v205 offset:9216\n"
    "s_add_u32 m0, s67, 132096\n"
    "s_add_u32 s78, s57, 4755456\n"
    "buffer_load_dwordx4 v204, s[12:15], s78 offen lds\n"
    "s_waitcnt lgkmcnt(3)\n"
    "v_mfma_f32_32x32x16_bf16 v[176:191], v[192:195], v[116:119], v[176:191]\n"
    "ds_read_b64_tr_b16 v[200:201], v206 offset:8192\n"
    "ds_read_b64_tr_b16 v[202:203], v206 offset:9216\n"
    "s_waitcnt lgkmcnt(2)\n"
    "v_mfma_f32_32x32x16_bf16 v[160:175], v[196:199], v[120:123], v[160:175]\n"
    "ds_read_b128 v[124:127], v223\n"
    "ds_read_b64_tr_b16 v[192:193], v205 offset:12288\n"
    "ds_read_b64_tr_b16 v[194:195], v205 offset:13312\n"
    "s_waitcnt lgkmcnt(3)\n"
    "v_mfma_f32_32x32x16_bf16 v[176:191], v[200:203], v[120:123], v[176:191]\n"
    "ds_read_b64_tr_b16 v[196:197], v206 offset:12288\n"
    "ds_read_b64_tr_b16 v[198:199], v206 offset:13312\n"
    "s_waitcnt lgkmcnt(2)\n"
    "v_mfma_f32_32x32x16_bf16 v[160:175], v[192:195], v[124:127], v[160:175]\n"
    "s_waitcnt vmcnt(4)\n"
    "s_barrier\n"
    "v_add_u32 v220, 32768, v220\n"
    "v_add_u32 v221, 32768, v221\n"
    "v_add_u32 v222, 32768, v222\n"
    "v_add_u32 v223, 32768, v223\n"
    "v_add_u32 v205, 32768, v205\n"
    "v_add_u32 v206, 32768, v206\n"
    "ds_read_b128 v[128:131], v220\n"
    "ds_read_b64_tr_b16 v[200:201], v205 offset:0\n"
    "ds_read_b64_tr_b16 v[202:203], v205 offset:1024\n"
    "s_add_u32 m0, s69, 0\n"
    "s_add_u32 s78, s58, 256\n"
    "buffer_load_dwordx4 v8, s[12:15], s78 offen lds\n"
    "s_waitcnt lgkmcnt(3)\n"
    "v_mfma_f32_32x32x16_bf16 v[176:191], v[196:199], v[124:127], v[176:191]\n"
    "ds_read_b64_tr_b16 v[192:193], v206 offset:0\n"
    "ds_read_b64_tr_b16 v[194:195], v206 offset:1024\n"
    "s_add_u32 m0, s69, 1024\n"
    "s_add_u32 s78, s58, 73984\n"
    "buffer_load_dwordx4 v8, s[12:15], s78 offen lds\n"
    "s_waitcnt lgkmcnt(2)\n"
    "v_mfma_f32_32x32x16_bf16 v[160:175], v[200:203], v[128:131], v[160:175]\n"
    "ds_read_b128 v[132:135], v221\n"
    "ds_read_b64_tr_b16 v[196:197], v205 offset:4096\n"
    "ds_read_b64_tr_b16 v[198:199], v205 offset:5120\n"
    "s_add_u32 m0, s69, 2048\n"
    "s_add_u32 s78, s58, 147712\n"
    "buffer_load_dwordx4 v8, s[12:15], s78 offen lds\n"
    "s_waitcnt lgkmcnt(3)\n"
    "v_mfma_f32_32x32x16_bf16 v[176:191], v[192:195], v[128:131], v[176:191]\n"
    "ds_read_b64_tr_b16 v[200:201], v206 offset:4096\n"
    "ds_read_b64_tr_b16 v[202:203], v206 offset:5120\n"
    "s_waitcnt lgkmcnt(2)\n"
    "v_mfma_f32_32x32x16_bf16 v[160:175], v[196:199], v[132:135], v[160:175]\n"
    "ds_read_b128 v[136:139], v222\n"
    "ds_read_b64_tr_b16 v[192:193], v205 offset:8192\n"
    "ds_read_b64_tr_b16 v[194:195], v205 offset:9216\n"
    "s_add_u32 m0, s69, 24576\n"
    "s_add_u32 s78, s58, 1769728\n"
    "buffer_load_dwordx4 v8, s[12:15], s78 offen lds\n"
    "s_waitcnt lgkmcnt(3)\n"
    "v_mfma_f32_32x32x16_bf16 v[176:191], v[200:203], v[132:135], v[176:191]\n"
    "ds_read_b64_tr_b16 v[196:197], v206 offset:8192\n"
    "ds_read_b64_tr_b16 v[198:199], v206 offset:9216\n"
    "s_add_u32 m0, s69, 25600\n"
    "s_add_u32 s78, s58, 1843456\n"
    "buffer_load_dwordx4 v8, s[12:15], s78 offen lds\n"
    "s_waitcnt lgkmcnt(2)\n"
    "v_mfma_f32_32x32x16_bf16 v[160:175], v[192:195], v[136:139], v[160:175]\n"
    "ds_read_b128 v[140:143], v223\n"
    "ds_read_b64_tr_b16 v[200:201], v205 offset:12288\n"
    "ds_read_b64_tr_b16 v[202:203], v205 offset:13312\n"
    "s_add_u32 m0, s69, 26624\n"
    "s_add_u32 s78, s58, 1917184\n"
    "buffer_load_dwordx4 v8, s[12:15], s78 offen lds\n"
    "s_waitcnt lgkmcnt(3)\n"
    "v_mfma_f32_32x32x16_bf16 v[176:191], v[196:199], v[136:139], v[176:191]\n"
    "ds_read_b64_tr_b16 v[192:193], v206 offset:12288\n"
    "ds_read_b64_tr_b16 v[194:195], v206 offset:13312\n"
    "s_waitcnt lgkmcnt(2)\n"
    "v_mfma_f32_32x32x16_bf16 v[160:175], v[200:203], v[140:143], v[160:175]\n"
    "s_waitcnt vmcnt(6)\n"
    "s_barrier\n"
    "v_add_u32 v220, 32768, v220\n"
    "v_add_u32 v221, 32768, v221\n"
    "v_add_u32 v222, 32768, v222\n"
    "v_add_u32 v223, 32768, v223\n"
    "v_add_u32 v205, 32768, v205\n"
    "v_add_u32 v206, 32768, v206\n"
    "ds_read_b128 v[144:147], v220\n"
    "ds_read_b64_tr_b16 v[196:197], v205 offset:0\n"
    "ds_read_b64_tr_b16 v[198:199], v205 offset:1024\n"
    "s_add_u32 m0, s69, 49152\n"
    "s_add_u32 s78, s58, 3539200\n"
    "buffer_load_dwordx4 v8, s[12:15], s78 offen lds\n"
    "s_waitcnt lgkmcnt(3)\n"
    "v_mfma_f32_32x32x16_bf16 v[176:191], v[192:195], v[140:143], v[176:191]\n"
    "ds_read_b64_tr_b16 v[200:201], v206 offset:0\n"
    "ds_read_b64_tr_b16 v[202:203], v206 offset:1024\n"
    "s_waitcnt lgkmcnt(2)\n"
    "v_mfma_f32_32x32x16_bf16 v[160:175], v[196:199], v[144:147], v[160:175]\n"
    "ds_read_b128 v[148:151], v221\n"
    "ds_read_b64_tr_b16 v[192:193], v205 offset:4096\n"
    "ds_read_b64_tr_b16 v[194:195], v205 offset:5120\n"
    "s_add_u32 m0, s69, 50176\n"
    "s_add_u32 s78, s58, 3612928\n"
    "buffer_load_dwordx4 v8, s[12:15], s78 offen lds\n"
    "s_waitcnt lgkmcnt(3)\n"
    "v_mfma_f32_32x32x16_bf16 v[176:191], v[200:203], v[144:147], v[176:191]\n"
    "ds_read_b64_tr_b16 v[196:197], v206 offset:4096\n"
    "ds_read_b64_tr_b16 v[198:199], v206 offset:5120\n"
    "s_waitcnt lgkmcnt(2)\n"
    "v_mfma_f32_32x32x16_bf16 v[160:175], v[192:195], v[148:151], v[160:175]\n"
    "ds_read_b128 v[152:155], v222\n"
    "ds_read_b64_tr_b16 v[200:201], v205 offset:8192\n"
    "ds_read_b64_tr_b16 v[202:203], v205 offset:9216\n"
    "s_waitcnt lgkmcnt(3)\n"
    "v_mfma_f32_32x32x16_bf16 v[176:191], v[196:199], v[148:151], v[176:191]\n"
    "ds_read_b64_tr_b16 v[192:193], v206 offset:8192\n"
    "ds_read_b64_tr_b16 v[194:195], v206 offset:9216\n"
    "s_add_u32 m0, s69, 51200\n"
    "s_add_u32 s78, s58, 3686656\n"
    "buffer_load_dwordx4 v8, s[12:15], s78 offen lds\n"
    "s_waitcnt lgkmcnt(2)\n"
    "v_mfma_f32_32x32x16_bf16 v[160:175], v[200:203], v[152:155], v[160:175]\n"
    "ds_read_b128 v[156:159], v223\n"
    "ds_read_b64_tr_b16 v[196:197], v205 offset:12288\n"
    "ds_read_b64_tr_b16 v[198:199], v205 offset:13312\n"
    "s_waitcnt lgkmcnt(3)\n"
    "v_mfma_f32_32x32x16_bf16 v[176:191], v[192:195], v[152:155], v[176:191]\n"
    "ds_read_b64_tr_b16 v[200:201], v206 offset:12288\n"
    "ds_read_b64_tr_b16 v[202:203], v206 offset:13312\n"
    "s_waitcnt lgkmcnt(2)\n"
    "v_mfma_f32_32x32x16_bf16 v[160:175], v[196:199], v[156:159], v[160:175]\n"
    "s_waitcnt vmcnt(6)\n"
    "s_barrier\n"
    "v_add_u32 v13, 0, v12\n"
    "ds_read_b64_tr_b16 v[192:193], v13 offset:0\n"
    "ds_read_b64_tr_b16 v[194:195], v13 offset:512\n"
    "s_add_u32 m0, s69, 73728\n"
    "s_add_u32 s78, s58, 384\n"
    "buffer_load_dwordx4 v8, s[12:15], s78 offen lds\n"
    "s_waitcnt lgkmcnt(2)\n"
    "v_mfma_f32_32x32x16_bf16 v[176:191], v[200:203], v[156:159], v[176:191]\n"
    "ds_read_b64_tr_b16 v[196:197], v13 offset:2048\n"
    "ds_read_b64_tr_b16 v[198:199], v13 offset:2560\n"
    "s_waitcnt lgkmcnt(2)\n"
    "v_mfma_f32_32x32x16_bf16 v[238:253], v[192:195], v[16:19], v[238:253]\n"
    "ds_read_b64_tr_b16 v[200:201], v13 offset:4096\n"
    "ds_read_b64_tr_b16 v[202:203], v13 offset:4608\n"
    "s_waitcnt lgkmcnt(2)\n"
    "v_mfma_f32_32x32x16_bf16 v[238:253], v[196:199], v[20:23], v[238:253]\n"
    "ds_read_b64_tr_b16 v[192:193], v13 offset:6144\n"
    "ds_read_b64_tr_b16 v[194:195], v13 offset:6656\n"
    "s_waitcnt lgkmcnt(2)\n"
    "v_mfma_f32_32x32x16_bf16 v[238:253], v[200:203], v[24:27], v[238:253]\n"
    "ds_read_b64_tr_b16 v[196:197], v13 offset:8192\n"
    "ds_read_b64_tr_b16 v[198:199], v13 offset:8704\n"
    "v_mul_f32 v208, s70, v160\n"
    "v_mul_f32 v209, s70, v161\n"
    "v_exp_f32 v210, v208\n"
    "s_add_u32 m0, s69, 74752\n"
    "s_add_u32 s78, s58, 74112\n"
    "buffer_load_dwordx4 v8, s[12:15], s78 offen lds\n"
    "s_waitcnt lgkmcnt(2)\n"
    "v_mfma_f32_32x32x16_bf16 v[238:253], v[192:195], v[28:31], v[238:253]\n"
    "ds_read_b64_tr_b16 v[200:201], v13 offset:10240\n"
    "ds_read_b64_tr_b16 v[202:203], v13 offset:10752\n"
    "v_exp_f32 v211, v209\n"
    "v_fma_f32 v208, v210, -0.5, -1.0\n"
    "v_fma_f32 v209, v211, -0.5, -1.0\n"
    "s_waitcnt lgkmcnt(2)\n"
    "v_mfma_f32_32x32x16_bf16 v[238:253], v[196:199], v[32:35], v[238:253]\n"
    "ds_read_b64_tr_b16 v[192:193], v13 offset:12288\n"
    "ds_read_b64_tr_b16 v[194:195], v13 offset:12800\n"
    "v_fma_f32 v208, v210, v208, -1.0\n"
    "v_fma_f32 v209, v211, v209, -1.0\n"
    "v_rcp_f32 v208, v208\n"
    "s_waitcnt lgkmcnt(2)\n"
    "v_mfma_f32_32x32x16_bf16 v[238:253], v[200:203], v[36:39], v[238:253]\n"
    "ds_read_b64_tr_b16 v[196:197], v13 offset:14336\n"
    "ds_read_b64_tr_b16 v[198:199], v13 offset:14848\n"
    "v_rcp_f32 v209, v209\n"
    "v_fma_f32 v210, v208, v160, v160\n"
    "v_fma_f32 v211, v209, v161, v161\n"
    "s_waitcnt lgkmcnt(2)\n"
    "v_mfma_f32_32x32x16_bf16 v[238:253], v[192:195], v[40:43], v[238:253]\n"
    "ds_read_b64_tr_b16 v[200:201], v13 offset:16384\n"
    "ds_read_b64_tr_b16 v[202:203], v13 offset:16896\n"
    "v_cvt_pk_bf16_f32 v212, v210, v211\n"
    "v_mul_f32 v208, s70, v162\n"
    "v_mul_f32 v209, s70, v163\n"
    "v_exp_f32 v210, v208\n"
    "s_add_u32 m0, s69, 75776\n"
    "s_add_u32 s78, s58, 147840\n"
    "buffer_load_dwordx4 v8, s[12:15], s78 offen lds\n"
    "s_waitcnt lgkmcnt(2)\n"
    "v_mfma_f32_32x32x16_bf16 v[238:253], v[196:199], v[44:47], v[238:253]\n"
    "ds_read_b64_tr_b16 v[192:193], v13 offset:18432\n"
    "ds_read_b64_tr_b16 v[194:195], v13 offset:18944\n"
    "v_exp_f32 v211, v209\n"
    "v_fma_f32 v208, v210, -0.5, -1.0\n"
    "v_fma_f32 v209, v211, -0.5, -1.0\n"
    "s_waitcnt lgkmcnt(2)\n"
    "v_mfma_f32_32x32x16_bf16 v[238:253], v[200:203], v[48:51], v[238:253]\n"
    "ds_read_b64_tr_b16 v[196:197], v13 offset:20480\n"
    "ds_read_b64_tr_b16 v[198:199], v13 offset:20992\n"
    "v_fma_f32 v208, v210, v208, -1.0\n"
    "v_fma_f32 v209, v211, v209, -1.0\n"
    "v_rcp_f32 v208, v208\n"
    "s_waitcnt lgkmcnt(2)\n"
    "v_mfma_f32_32x32x16_bf16 v[238:253], v[192:195], v[52:55], v[238:253]\n"
    "ds_read_b64_tr_b16 v[200:201], v13 offset:22528\n"
    "ds_read_b64_tr_b16 v[202:203], v13 offset:23040\n"
    "v_rcp_f32 v209, v209\n"
    "v_fma_f32 v210, v208, v162, v162\n"
    "v_fma_f32 v211, v209, v163, v163\n"
    "s_waitcnt lgkmcnt(2)\n"
    "v_mfma_f32_32x32x16_bf16 v[238:253], v[196:199], v[56:59], v[238:253]\n"
    "s_waitcnt vmcnt(6)\n"
    "s_barrier\n"
    "v_cmp_lt_u32 vcc, 23, v1\n"
    "v_subrev_u32 v205, 24, v1\n"
    "s_nop 0\n"
    "v_cndmask_b32 v205, v1, v205, vcc\n"
    "s_and_b32 s79, s50, 1\n"
    "s_lshl_b32 s79, s79, 2\n"
    "v_xor_b32 v205, s79, v205\n"
    "v_lshlrev_b32 v205, 4, v205\n"
    "v_mov_b32 v206, 1152\n"
    "v_cndmask_b32 v206, 0, v206, vcc\n"
    "v_add_u32 v204, v205, v206\n"
    "v_add_u32 v13, 24576, v12\n"
    "ds_read_b64_tr_b16 v[192:193], v13 offset:0\n"
    "ds_read_b64_tr_b16 v[194:195], v13 offset:512\n"
    "v_cvt_pk_bf16_f32 v213, v210, v211\n"
    "v_mul_f32 v208, s70, v164\n"
    "v_mul_f32 v209, s70, v165\n"
    "v_exp_f32 v210, v208\n"
    "ds_read_b128 v[160:163], v14 offset:768\n"
    "s_add_u32 m0, s69, 98304\n"
    "s_add_u32 s78, s58, 1769856\n"
    "buffer_load_dwordx4 v8, s[12:15], s78 offen lds\n"
    "s_waitcnt lgkmcnt(3)\n"
    "v_mfma_f32_32x32x16_bf16 v[238:253], v[200:203], v[60:63], v[238:253]\n"
    "ds_read_b64_tr_b16 v[196:197], v13 offset:2048\n"
    "ds_read_b64_tr_b16 v[198:199], v13 offset:2560\n"
    "v_exp_f32 v211, v209\n"
    "v_fma_f32 v208, v210, -0.5, -1.0\n"
    "s_waitcnt lgkmcnt(3)\n"
    "v_mfma_f32_32x32x16_bf16 v[238:253], v[192:195], v[64:67], v[238:253]\n"
    "ds_read_b64_tr_b16 v[200:201], v13 offset:4096\n"
    "ds_read_b64_tr_b16 v[202:203], v13 offset:4608\n"
    "v_fma_f32 v209, v211, -0.5, -1.0\n"
    "v_fma_f32 v208, v210, v208, -1.0\n"
    "v_fma_f32 v209, v211, v209, -1.0\n"
    "v_rcp_f32 v208, v208\n"
    "s_add_u32 m0, s69, 99328\n"
    "s_add_u32 s78, s58, 1843584\n"
    "buffer_load_dwordx4 v8, s[12:15], s78 offen lds\n"
    "s_waitcnt lgkmcnt(2)\n"
    "v_mfma_f32_32x32x16_bf16 v[238:253], v[196:199], v[68:71], v[238:253]\n"
    "ds_read_b64_tr_b16 v[192:193], v13 offset:6144\n"
    "ds_read_b64_tr_b16 v[194:195], v13 offset:6656\n"
    "v_rcp_f32 v209, v209\n"
    "v_fma_f32 v210, v208, v164, v164\n"
    "s_waitcnt lgkmcnt(2)\n"
    "v_mfma_f32_32x32x16_bf16 v[238:253], v[200:203], v[72:75], v[238:253]\n"
    "ds_read_b64_tr_b16 v[196:197], v13 offset:8192\n"
    "ds_read_b64_tr_b16 v[198:199], v13 offset:8704\n"
    "v_fma_f32 v211, v209, v165, v165\n"
    "v_cvt_pk_bf16_f32 v214, v210, v211\n"
    "v_mul_f32 v208, s70, v166\n"
    "v_mul_f32 v209, s70, v167\n"
    "s_add_u32 m0, s69, 100352\n"
    "s_add_u32 s78, s58, 1917312\n"
    "buffer_load_dwordx4 v8, s[12:15], s78 offen lds\n"
    "s_waitcnt lgkmcnt(2)\n"
    "v_mfma_f32_32x32x16_bf16 v[238:253], v[192:195], v[76:79], v[238:253]\n"
    "ds_read_b64_tr_b16 v[200:201], v13 offset:10240\n"
    "ds_read_b64_tr_b16 v[202:203], v13 offset:10752\n"
    "v_exp_f32 v210, v208\n"
    "v_exp_f32 v211, v209\n"
    "v_fma_f32 v208, v210, -0.5, -1.0\n"
    "s_waitcnt lgkmcnt(2)\n"
    "v_mfma_f32_32x32x16_bf16 v[238:253], v[196:199], v[80:83], v[238:253]\n"
    "ds_read_b64_tr_b16 v[192:193], v13 offset:12288\n"
    "ds_read_b64_tr_b16 v[194:195], v13 offset:12800\n"
    "v_fma_f32 v209, v211, -0.5, -1.0\n"
    "v_fma_f32 v208, v210, v208, -1.0\n"
    "v_fma_f32 v209, v211, v209, -1.0\n"
    "s_add_u32 m0, s69, 122880\n"
    "s_add_u32 s78, s58, 3539328\n"
    "buffer_load_dwordx4 v8, s[12:15], s78 offen lds\n"
    "s_waitcnt lgkmcnt(2)\n"
    "v_mfma_f32_32x32x16_bf16 v[238:253], v[200:203], v[84:87], v[238:253]\n"
    "ds_read_b64_tr_b16 v[196:197], v13 offset:14336\n"
    "ds_read_b64_tr_b16 v[198:199], v13 offset:14848\n"
    "v_rcp_f32 v208, v208\n"
    "v_rcp_f32 v209, v209\n"
    "v_fma_f32 v210, v208, v166, v166\n"
    "s_waitcnt lgkmcnt(2)\n"
    "v_mfma_f32_32x32x16_bf16 v[238:253], v[192:195], v[88:91], v[238:253]\n"
    "ds_read_b64_tr_b16 v[200:201], v13 offset:16384\n"
    "ds_read_b64_tr_b16 v[202:203], v13 offset:16896\n"
    "v_fma_f32 v211, v209, v167, v167\n"
    "v_cvt_pk_bf16_f32 v215, v210, v211\n"
    "v_mul_f32 v208, s70, v168\n"
    "v_mul_f32 v209, s70, v169\n"
    "ds_read_b128 v[164:167], v14 offset:800\n"
    "s_add_u32 m0, s69, 123904\n"
    "s_add_u32 s78, s58, 3613056\n"
    "buffer_load_dwordx4 v8, s[12:15], s78 offen lds\n"
    "s_waitcnt lgkmcnt(3)\n"
    "v_mfma_f32_32x32x16_bf16 v[238:253], v[196:199], v[92:95], v[238:253]\n"
    "ds_read_b64_tr_b16 v[192:193], v13 offset:18432\n"
    "ds_read_b64_tr_b16 v[194:195], v13 offset:18944\n"
    "v_exp_f32 v210, v208\n"
    "v_exp_f32 v211, v209\n"
    "s_add_u32 s78, s64, 0\n"
    "buffer_store_dwordx4 v[212:215], v2, s[20:23], s78 offen sc1\n"
    "s_waitcnt lgkmcnt(3)\n"
    "v_mfma_f32_32x32x16_bf16 v[238:253], v[200:203], v[96:99], v[238:253]\n"
    "ds_read_b64_tr_b16 v[196:197], v13 offset:20480\n"
    "ds_read_b64_tr_b16 v[198:199], v13 offset:20992\n"
    "v_fma_f32 v208, v210, -0.5, -1.0\n"
    "v_fma_f32 v209, v211, -0.5, -1.0\n"
    "v_fma_f32 v208, v210, v208, -1.0\n"
    "v_fma_f32 v209, v211, v209, -1.0\n"
    "s_add_u32 m0, s69, 124928\n"
    "s_add_u32 s78, s58, 3686784\n"
    "buffer_load_dwordx4 v8, s[12:15], s78 offen lds\n"
    "s_waitcnt lgkmcnt(2)\n"
    "v_mfma_f32_32x32x16_bf16 v[238:253], v[192:195], v[100:103], v[238:253]\n"
    "ds_read_b64_tr_b16 v[200:201], v13 offset:22528\n"
    "ds_read_b64_tr_b16 v[202:203], v13 offset:23040\n"
    "v_rcp_f32 v208, v208\n"
    "v_rcp_f32 v209, v209\n"
    "s_waitcnt lgkmcnt(2)\n"
    "v_mfma_f32_32x32x16_bf16 v[238:253], v[196:199], v[104:107], v[238:253]\n"
    "s_waitcnt vmcnt(10)\n"
    "s_barrier\n"
    "v_add_u32 v13, 49152, v12\n"
    "ds_read_b64_tr_b16 v[192:193], v13 offset:0\n"
    "ds_read_b64_tr_b16 v[194:195], v13 offset:512\n"
    "v_fma_f32 v210, v208, v168, v168\n"
    "v_fma_f32 v211, v209, v169, v169\n"
    "v_cvt_pk_bf16_f32 v216, v210, v211\n"
    "v_mul_f32 v208, s70, v170\n"
    "v_mul_f32 v209, s70, v171\n"
    "s_add_u32 m0, s69, 0\n"
    "s_add_u32 s78, s58, 512\n"
    "buffer_load_dwordx4 v8, s[12:15], s78 offen lds\n"
    "s_waitcnt lgkmcnt(2)\n"
    "v_mfma_f32_32x32x16_bf16 v[238:253], v[200:203], v[108:111], v[238:253]\n"
    "ds_read_b64_tr_b16 v[196:197], v13 offset:2048\n"
    "ds_read_b64_tr_b16 v[198:199], v13 offset:2560\n"
    "v_exp_f32 v210, v208\n"
    "v_exp_f32 v211, v209\n"
    "s_waitcnt lgkmcnt(2)\n"
    "v_mfma_f32_32x32x16_bf16 v[238:253], v[192:195], v[112:115], v[238:253]\n"
    "ds_read_b64_tr_b16 v[200:201], v13 offset:4096\n"
    "ds_read_b64_tr_b16 v[202:203], v13 offset:4608\n"
    "v_fma_f32 v208, v210, -0.5, -1.0\n"
    "v_fma_f32 v209, v211, -0.5, -1.0\n"
    "v_fma_f32 v208, v210, v208, -1.0\n"
    "v_fma_f32 v209, v211, v209, -1.0\n"
    "s_waitcnt lgkmcnt(2)\n"
    "v_mfma_f32_32x32x16_bf16 v[238:253], v[196:199], v[116:119], v[238:253]\n"
    "ds_read_b64_tr_b16 v[192:193], v13 offset:6144\n"
    "ds_read_b64_tr_b16 v[194:195], v13 offset:6656\n"
    "v_rcp_f32 v208, v208\n"
    "v_rcp_f32 v209, v209\n"
    "s_waitcnt lgkmcnt(2)\n"
    "v_mfma_f32_32x32x16_bf16 v[238:253], v[200:203], v[120:123], v[238:253]\n"
    "ds_read_b64_tr_b16 v[196:197], v13 offset:8192\n"
    "ds_read_b64_tr_b16 v[198:199], v13 offset:8704\n"
    "v_fma_f32 v210, v208, v170, v170\n"
    "v_fma_f32 v211, v209, v171, v171\n"
    "v_cvt_pk_bf16_f32 v217, v210, v211\n"
    "v_mul_f32 v208, s70, v172\n"
    "ds_read_b128 v[168:171], v14 offset:832\n"
    "s_add_u32 m0, s69, 1024\n"
    "s_add_u32 s78, s58, 74240\n"
    "buffer_load_dwordx4 v8, s[12:15], s78 offen lds\n"
    "s_waitcnt lgkmcnt(3)\n"
    "v_mfma_f32_32x32x16_bf16 v[238:253], v[192:195], v[124:127], v[238:253]\n"
    "ds_read_b64_tr_b16 v[200:201], v13 offset:10240\n"
    "ds_read_b64_tr_b16 v[202:203], v13 offset:10752\n"
    "v_mul_f32 v209, s70, v173\n"
    "v_exp_f32 v210, v208\n"
    "v_exp_f32 v211, v209\n"
    "s_waitcnt lgkmcnt(3)\n"
    "v_mfma_f32_32x32x16_bf16 v[238:253], v[196:199], v[128:131], v[238:253]\n"
    "ds_read_b64_tr_b16 v[192:193], v13 offset:12288\n"
    "ds_read_b64_tr_b16 v[194:195], v13 offset:12800\n"
    "v_fma_f32 v208, v210, -0.5, -1.0\n"
    "v_fma_f32 v209, v211, -0.5, -1.0\n"
    "v_fma_f32 v208, v210, v208, -1.0\n"
    "s_waitcnt lgkmcnt(2)\n"
    "v_mfma_f32_32x32x16_bf16 v[238:253], v[200:203], v[132:135], v[238:253]\n"
    "ds_read_b64_tr_b16 v[196:197], v13 offset:14336\n"
    "ds_read_b64_tr_b16 v[198:199], v13 offset:14848\n"
    "v_fma_f32 v209, v211, v209, -1.0\n"
    "v_rcp_f32 v208, v208\n"
    "v_rcp_f32 v209, v209\n"
    "s_waitcnt lgkmcnt(2)\n"
    "v_mfma_f32_32x32x16_bf16 v[238:253], v[192:195], v[136:139], v[238:253]\n"
    "ds_read_b64_tr_b16 v[200:201], v13 offset:16384\n"
    "ds_read_b64_tr_b16 v[202:203], v13 offset:16896\n"
    "v_fma_f32 v210, v208, v172, v172\n"
    "v_fma_f32 v211, v209, v173, v173\n"
    "v_cvt_pk_bf16_f32 v218, v210, v211\n"
    "v_mul_f32 v208, s70, v174\n"
    "s_add_u32 m0, s69, 2048\n"
    "s_add_u32 s78, s58, 147968\n"
    "buffer_load_dwordx4 v8, s[12:15], s78 offen lds\n"
    "s_waitcnt lgkmcnt(2)\n"
    "v_mfma_f32_32x32x16_bf16 v[238:253], v[196:199], v[140:143], v[238:253]\n"
    "ds_read_b64_tr_b16 v[192:193], v13 offset:18432\n"
    "ds_read_b64_tr_b16 v[194:195], v13 offset:18944\n"
    "v_mul_f32 v209, s70, v175\n"
    "v_exp_f32 v210, v208\n"
    "s_waitcnt lgkmcnt(2)\n"
    "v_mfma_f32_32x32x16_bf16 v[238:253], v[200:203], v[144:147], v[238:253]\n"
    "ds_read_b64_tr_b16 v[196:197], v13 offset:20480\n"
    "ds_read_b64_tr_b16 v[198:199], v13 offset:20992\n"
    "v_exp_f32 v211, v209\n"
    "v_fma_f32 v208, v210, -0.5, -1.0\n"
    "v_fma_f32 v209, v211, -0.5, -1.0\n"
    "v_fma_f32 v208, v210, v208, -1.0\n"
    "s_waitcnt lgkmcnt(2)\n"
    "v_mfma_f32_32x32x16_bf16 v[238:253], v[192:195], v[148:151], v[238:253]\n"
    "ds_read_b64_tr_b16 v[200:201], v13 offset:22528\n"
    "ds_read_b64_tr_b16 v[202:203], v13 offset:23040\n"
    "v_fma_f32 v209, v211, v209, -1.0\n"
    "v_rcp_f32 v208, v208\n"
    "s_waitcnt lgkmcnt(2)\n"
    "v_mfma_f32_32x32x16_bf16 v[238:253], v[196:199], v[152:155], v[238:253]\n"
    "s_waitcnt vmcnt(10)\n"
    "s_barrier\n"
    "v_add_u32 v13, 73728, v12\n"
    "ds_read_b64_tr_b16 v[192:193], v13 offset:0\n"
    "ds_read_b64_tr_b16 v[194:195], v13 offset:512\n"
    "v_rcp_f32 v209, v209\n"
    "v_fma_f32 v210, v208, v174, v174\n"
    "v_fma_f32 v211, v209, v175, v175\n"
    "v_cvt_pk_bf16_f32 v219, v210, v211\n"
    "ds_read_b128 v[172:175], v14 offset:864\n"
    "s_add_u32 m0, s69, 24576\n"
    "s_add_u32 s78, s58, 1769984\n"
    "buffer_load_dwordx4 v8, s[12:15], s78 offen lds\n"
    "s_waitcnt lgkmcnt(3)\n"
    "v_mfma_f32_32x32x16_bf16 v[238:253], v[200:203], v[156:159], v[238:253]\n"
    "ds_read_b64_tr_b16 v[196:197], v13 offset:2048\n"
    "ds_read_b64_tr_b16 v[198:199], v13 offset:2560\n"
    "s_add_u32 s78, s64, 1024\n"
    "buffer_store_dwordx4 v[216:219], v2, s[20:23], s78 offen sc1\n"
    "s_waitcnt lgkmcnt(2)\n"
    "v_mfma_f32_32x32x16_bf16 v[160:175], v[192:195], v[16:19], v[160:175]\n"
    "ds_read_b64_tr_b16 v[200:201], v13 offset:4096\n"
    "ds_read_b64_tr_b16 v[202:203], v13 offset:4608\n"
    "s_waitcnt lgkmcnt(2)\n"
    "v_mfma_f32_32x32x16_bf16 v[160:175], v[196:199], v[20:23], v[160:175]\n"
    "ds_read_b64_tr_b16 v[192:193], v13 offset:6144\n"
    "ds_read_b64_tr_b16 v[194:195], v13 offset:6656\n"
    "s_waitcnt lgkmcnt(2)\n"
    "v_mfma_f32_32x32x16_bf16 v[160:175], v[200:203], v[24:27], v[160:175]\n"
    "ds_read_b64_tr_b16 v[196:197], v13 offset:8192\n"
    "ds_read_b64_tr_b16 v[198:199], v13 offset:8704\n"
    "v_mul_f32 v208, s70, v176\n"
    "v_mul_f32 v209, s70, v177\n"
    "v_exp_f32 v210, v208\n"
    "s_add_u32 m0, s69, 25600\n"
    "s_add_u32 s78, s58, 1843712\n"
    "buffer_load_dwordx4 v8, s[12:15], s78 offen lds\n"
    "s_waitcnt lgkmcnt(2)\n"
    "v_mfma_f32_32x32x16_bf16 v[160:175], v[192:195], v[28:31], v[160:175]\n"
    "ds_read_b64_tr_b16 v[200:201], v13 offset:10240\n"
    "ds_read_b64_tr_b16 v[202:203], v13 offset:10752\n"
    "v_exp_f32 v211, v209\n"
    "v_fma_f32 v208, v210, -0.5, -1.0\n"
    "v_fma_f32 v209, v211, -0.5, -1.0\n"
    "s_waitcnt lgkmcnt(2)\n"
    "v_mfma_f32_32x32x16_bf16 v[160:175], v[196:199], v[32:35], v[160:175]\n"
    "ds_read_b64_tr_b16 v[192:193], v13 offset:12288\n"
    "ds_read_b64_tr_b16 v[194:195], v13 offset:12800\n"
    "v_fma_f32 v208, v210, v208, -1.0\n"
    "v_fma_f32 v209, v211, v209, -1.0\n"
    "v_rcp_f32 v208, v208\n"
    "s_waitcnt lgkmcnt(2)\n"
    "v_mfma_f32_32x32x16_bf16 v[160:175], v[200:203], v[36:39], v[160:175]\n"
    "ds_read_b64_tr_b16 v[196:197], v13 offset:14336\n"
    "ds_read_b64_tr_b16 v[198:199], v13 offset:14848\n"
    "v_rcp_f32 v209, v209\n"
    "v_fma_f32 v210, v208, v176, v176\n"
    "v_fma_f32 v211, v209, v177, v177\n"
    "s_waitcnt lgkmcnt(2)\n"
    "v_mfma_f32_32x32x16_bf16 v[160:175], v[192:195], v[40:43], v[160:175]\n"
    "ds_read_b64_tr_b16 v[200:201], v13 offset:16384\n"
    "ds_read_b64_tr_b16 v[202:203], v13 offset:16896\n"
    "v_cvt_pk_bf16_f32 v212, v210, v211\n"
    "v_mul_f32 v208, s70, v178\n"
    "v_mul_f32 v209, s70, v179\n"
    "v_exp_f32 v210, v208\n"
    "s_add_u32 m0, s69, 26624\n"
    "s_add_u32 s78, s58, 1917440\n"
    "buffer_load_dwordx4 v8, s[12:15], s78 offen lds\n"
    "s_waitcnt lgkmcnt(2)\n"
    "v_mfma_f32_32x32x16_bf16 v[160:175], v[196:199], v[44:47], v[160:175]\n"
    "ds_read_b64_tr_b16 v[192:193], v13 offset:18432\n"
    "ds_read_b64_tr_b16 v[194:195], v13 offset:18944\n"
    "v_exp_f32 v211, v209\n"
    "v_fma_f32 v208, v210, -0.5, -1.0\n"
    "v_fma_f32 v209, v211, -0.5, -1.0\n"
    "s_waitcnt lgkmcnt(2)\n"
    "v_mfma_f32_32x32x16_bf16 v[160:175], v[200:203], v[48:51], v[160:175]\n"
    "ds_read_b64_tr_b16 v[196:197], v13 offset:20480\n"
    "ds_read_b64_tr_b16 v[198:199], v13 offset:20992\n"
    "v_fma_f32 v208, v210, v208, -1.0\n"
    "v_fma_f32 v209, v211, v209, -1.0\n"
    "v_rcp_f32 v208, v208\n"
    "s_waitcnt lgkmcnt(2)\n"
    "v_mfma_f32_32x32x16_bf16 v[160:175], v[192:195], v[52:55], v[160:175]\n"
    "ds_read_b64_tr_b16 v[200:201], v13 offset:22528\n"
    "ds_read_b64_tr_b16 v[202:203], v13 offset:23040\n"
    "v_rcp_f32 v209, v209\n"
    "v_fma_f32 v210, v208, v178, v178\n"
    "v_fma_f32 v211, v209, v179, v179\n"
    "s_waitcnt lgkmcnt(2)\n"
    "v_mfma_f32_32x32x16_bf16 v[160:175], v[196:199], v[56:59], v[160:175]\n"
    "s_waitcnt vmcnt(11)\n"
    "s_barrier\n"
    "v_add_u32 v13, 98304, v12\n"
    "ds_read_b64_tr_b16 v[192:193], v13 offset:0\n"
    "ds_read_b64_tr_b16 v[194:195], v13 offset:512\n"
    "v_cvt_pk_bf16_f32 v213, v210, v211\n"
    "v_mul_f32 v208, s70, v180\n"
    "v_mul_f32 v209, s70, v181\n"
    "v_exp_f32 v210, v208\n"
    "ds_read_b128 v[176:179], v14 offset:1024\n"
    "s_add_u32 m0, s69, 49152\n"
    "s_add_u32 s78, s58, 3539456\n"
    "buffer_load_dwordx4 v8, s[12:15], s78 offen lds\n"
    "s_waitcnt lgkmcnt(3)\n"
    "v_mfma_f32_32x32x16_bf16 v[160:175], v[200:203], v[60:63], v[160:175]\n"
    "ds_read_b64_tr_b16 v[196:197], v13 offset:2048\n"
    "ds_read_b64_tr_b16 v[198:199], v13 offset:2560\n"
    "v_exp_f32 v211, v209\n"
    "v_fma_f32 v208, v210, -0.5, -1.0\n"
    "s_waitcnt lgkmcnt(3)\n"
    "v_mfma_f32_32x32x16_bf16 v[160:175], v[192:195], v[64:67], v[160:175]\n"
    "ds_read_b64_tr_b16 v[200:201], v13 offset:4096\n"
    "ds_read_b64_tr_b16 v[202:203], v13 offset:4608\n"
    "v_fma_f32 v209, v211, -0.5, -1.0\n"
    "v_fma_f32 v208, v210, v208, -1.0\n"
    "v_fma_f32 v209, v211, v209, -1.0\n"
    "v_rcp_f32 v208, v208\n"
    "s_waitcnt lgkmcnt(2)\n"
    "v_mfma_f32_32x32x16_bf16 v[160:175], v[196:199], v[68:71], v[160:175]\n"
    "ds_read_b64_tr_b16 v[192:193], v13 offset:6144\n"
    "ds_read_b64_tr_b16 v[194:195], v13 offset:6656\n"
    "v_rcp_f32 v209, v209\n"
    "v_fma_f32 v210, v208, v180, v180\n"
    "s_waitcnt lgkmcnt(2)\n"
    "v_mfma_f32_32x32x16_bf16 v[160:175], v[200:203], v[72:75], v[160:175]\n"
    "ds_read_b64_tr_b16 v[196:197], v13 offset:8192\n"
    "ds_read_b64_tr_b16 v[198:199], v13 offset:8704\n"
    "v_fma_f32 v211, v209, v181, v181\n"
    "v_cvt_pk_bf16_f32 v214, v210, v211\n"
    "v_mul_f32 v208, s70, v182\n"
    "v_mul_f32 v209, s70, v183\n"
    "s_add_u32 m0, s69, 50176\n"
    "s_add_u32 s78, s58, 3613184\n"
    "buffer_load_dwordx4 v8, s[12:15], s78 offen lds\n"
    "s_waitcnt lgkmcnt(2)\n"
    "v_mfma_f32_32x32x16_bf16 v[160:175], v[192:195], v[76:79], v[160:175]\n"
    "ds_read_b64_tr_b16 v[200:201], v13 offset:10240\n"
    "ds_read_b64_tr_b16 v[202:203], v13 offset:10752\n"
    "v_exp_f32 v210, v208\n"
    "v_exp_f32 v211, v209\n"
    "v_fma_f32 v208, v210, -0.5, -1.0\n"
    "s_waitcnt lgkmcnt(2)\n"
    "v_mfma_f32_32x32x16_bf16 v[160:175], v[196:199], v[80:83], v[160:175]\n"
    "ds_read_b64_tr_b16 v[192:193], v13 offset:12288\n"
    "ds_read_b64_tr_b16 v[194:195], v13 offset:12800\n"
    "v_fma_f32 v209, v211, -0.5, -1.0\n"
    "v_fma_f32 v208, v210, v208, -1.0\n"
    "v_fma_f32 v209, v211, v209, -1.0\n"
    "s_waitcnt lgkmcnt(2)\n"
    "v_mfma_f32_32x32x16_bf16 v[160:175], v[200:203], v[84:87], v[160:175]\n"
    "ds_read_b64_tr_b16 v[196:197], v13 offset:14336\n"
    "ds_read_b64_tr_b16 v[198:199], v13 offset:14848\n"
    "v_rcp_f32 v208, v208\n"
    "v_rcp_f32 v209, v209\n"
    "v_fma_f32 v210, v208, v182, v182\n"
    "s_waitcnt lgkmcnt(2)\n"
    "v_mfma_f32_32x32x16_bf16 v[160:175], v[192:195], v[88:91], v[160:175]\n"
    "ds_read_b64_tr_b16 v[200:201], v13 offset:16384\n"
    "ds_read_b64_tr_b16 v[202:203], v13 offset:16896\n"
    "v_fma_f32 v211, v209, v183, v183\n"
    "v_cvt_pk_bf16_f32 v215, v210, v211\n"
    "v_mul_f32 v208, s70, v184\n"
    "v_mul_f32 v209, s70, v185\n"
    "ds_read_b128 v[180:183], v14 offset:1056\n"
    "s_add_u32 m0, s69, 51200\n"
    "s_add_u32 s78, s58, 3686912\n"
    "buffer_load_dwordx4 v8, s[12:15], s78 offen lds\n"
    "s_waitcnt lgkmcnt(3)\n"
    "v_mfma_f32_32x32x16_bf16 v[160:175], v[196:199], v[92:95], v[160:175]\n"
    "ds_read_b64_tr_b16 v[192:193], v13 offset:18432\n"
    "ds_read_b64_tr_b16 v[194:195], v13 offset:18944\n"
    "v_exp_f32 v210, v208\n"
    "v_exp_f32 v211, v209\n"
    "s_add_u32 s78, s64, 16384\n"
    "buffer_store_dwordx4 v[212:215], v2, s[20:23], s78 offen sc1\n"
    "s_waitcnt lgkmcnt(3)\n"
    "v_mfma_f32_32x32x16_bf16 v[160:175], v[200:203], v[96:99], v[160:175]\n"
    "ds_read_b64_tr_b16 v[196:197], v13 offset:20480\n"
    "ds_read_b64_tr_b16 v[198:199], v13 offset:20992\n"
    "v_fma_f32 v208, v210, -0.5, -1.0\n"
    "v_fma_f32 v209, v211, -0.5, -1.0\n"
    "v_fma_f32 v208, v210, v208, -1.0\n"
    "v_fma_f32 v209, v211, v209, -1.0\n"
    "s_waitcnt lgkmcnt(2)\n"
    "v_mfma_f32_32x32x16_bf16 v[160:175], v[192:195], v[100:103], v[160:175]\n"
    "ds_read_b64_tr_b16 v[200:201], v13 offset:22528\n"
    "ds_read_b64_tr_b16 v[202:203], v13 offset:23040\n"
    "v_rcp_f32 v208, v208\n"
    "v_rcp_f32 v209, v209\n"
    "s_waitcnt lgkmcnt(2)\n"
    "v_mfma_f32_32x32x16_bf16 v[160:175], v[196:199], v[104:107], v[160:175]\n"
    "s_waitcnt vmcnt(11)\n"
    "s_barrier\n"
    "v_add_u32 v13, 122880, v12\n"
    "ds_read_b64_tr_b16 v[192:193], v13 offset:0\n"
    "ds_read_b64_tr_b16 v[194:195], v13 offset:512\n"
    "v_fma_f32 v210, v208, v184, v184\n"
    "v_fma_f32 v211, v209, v185, v185\n"
    "v_cvt_pk_bf16_f32 v216, v210, v211\n"
    "v_mul_f32 v208, s70, v186\n"
    "v_mul_f32 v209, s70, v187\n"
    "s_add_u32 m0, s69, 73728\n"
    "s_add_u32 s78, s58, 640\n"
    "buffer_load_dwordx4 v8, s[12:15], s78 offen lds\n"
    "s_waitcnt lgkmcnt(2)\n"
    "v_mfma_f32_32x32x16_bf16 v[160:175], v[200:203], v[108:111], v[160:175]\n"
    "ds_read_b64_tr_b16 v[196:197], v13 offset:2048\n"
    "ds_read_b64_tr_b16 v[198:199], v13 offset:2560\n"
    "v_exp_f32 v210, v208\n"
    "v_exp_f32 v211, v209\n"
    "s_waitcnt lgkmcnt(2)\n"
    "v_mfma_f32_32x32x16_bf16 v[160:175], v[192:195], v[112:115], v[160:175]\n"
    "ds_read_b64_tr_b16 v[200:201], v13 offset:4096\n"
    "ds_read_b64_tr_b16 v[202:203], v13 offset:4608\n"
    "v_fma_f32 v208, v210, -0.5, -1.0\n"
    "v_fma_f32 v209, v211, -0.5, -1.0\n"
    "v_fma_f32 v208, v210, v208, -1.0\n"
    "v_fma_f32 v209, v211, v209, -1.0\n"
    "s_waitcnt lgkmcnt(2)\n"
    "v_mfma_f32_32x32x16_bf16 v[160:175], v[196:199], v[116:119], v[160:175]\n"
    "ds_read_b64_tr_b16 v[192:193], v13 offset:6144\n"
    "ds_read_b64_tr_b16 v[194:195], v13 offset:6656\n"
    "v_rcp_f32 v208, v208\n"
    "v_rcp_f32 v209, v209\n"
    "s_waitcnt lgkmcnt(2)\n"
    "v_mfma_f32_32x32x16_bf16 v[160:175], v[200:203], v[120:123], v[160:175]\n"
    "ds_read_b64_tr_b16 v[196:197], v13 offset:8192\n"
    "ds_read_b64_tr_b16 v[198:199], v13 offset:8704\n"
    "v_fma_f32 v210, v208, v186, v186\n"
    "v_fma_f32 v211, v209, v187, v187\n"
    "v_cvt_pk_bf16_f32 v217, v210, v211\n"
    "v_mul_f32 v208, s70, v188\n"
    "ds_read_b128 v[184:187], v14 offset:1088\n"
    "s_add_u32 m0, s69, 74752\n"
    "s_add_u32 s78, s58, 74368\n"
    "buffer_load_dwordx4 v8, s[12:15], s78 offen lds\n"
    "s_waitcnt lgkmcnt(3)\n"
    "v_mfma_f32_32x32x16_bf16 v[160:175], v[192:195], v[124:127], v[160:175]\n"
    "ds_read_b64_tr_b16 v[200:201], v13 offset:10240\n"
    "ds_read_b64_tr_b16 v[202:203], v13 offset:10752\n"
    "v_mul_f32 v209, s70, v189\n"
    "v_exp_f32 v210, v208\n"
    "v_exp_f32 v211, v209\n"
    "s_waitcnt lgkmcnt(3)\n"
    "v_mfma_f32_32x32x16_bf16 v[160:175], v[196:199], v[128:131], v[160:175]\n"
    "ds_read_b64_tr_b16 v[192:193], v13 offset:12288\n"
    "ds_read_b64_tr_b16 v[194:195], v13 offset:12800\n"
    "v_fma_f32 v208, v210, -0.5, -1.0\n"
    "v_fma_f32 v209, v211, -0.5, -1.0\n"
    "v_fma_f32 v208, v210, v208, -1.0\n"
    "s_waitcnt lgkmcnt(2)\n"
    "v_mfma_f32_32x32x16_bf16 v[160:175], v[200:203], v[132:135], v[160:175]\n"
    "ds_read_b64_tr_b16 v[196:197], v13 offset:14336\n"
    "ds_read_b64_tr_b16 v[198:199], v13 offset:14848\n"
    "v_fma_f32 v209, v211, v209, -1.0\n"
    "v_rcp_f32 v208, v208\n"
    "v_rcp_f32 v209, v209\n"
    "s_waitcnt lgkmcnt(2)\n"
    "v_mfma_f32_32x32x16_bf16 v[160:175], v[192:195], v[136:139], v[160:175]\n"
    "ds_read_b64_tr_b16 v[200:201], v13 offset:16384\n"
    "ds_read_b64_tr_b16 v[202:203], v13 offset:16896\n"
    "v_fma_f32 v210, v208, v188, v188\n"
    "v_fma_f32 v211, v209, v189, v189\n"
    "v_cvt_pk_bf16_f32 v218, v210, v211\n"
    "v_mul_f32 v208, s70, v190\n"
    "s_add_u32 m0, s69, 75776\n"
    "s_add_u32 s78, s58, 148096\n"
    "buffer_load_dwordx4 v8, s[12:15], s78 offen lds\n"
    "s_waitcnt lgkmcnt(2)\n"
    "v_mfma_f32_32x32x16_bf16 v[160:175], v[196:199], v[140:143], v[160:175]\n"
    "ds_read_b64_tr_b16 v[192:193], v13 offset:18432\n"
    "ds_read_b64_tr_b16 v[194:195], v13 offset:18944\n"
    "v_mul_f32 v209, s70, v191\n"
    "v_exp_f32 v210, v208\n"
    "s_waitcnt lgkmcnt(2)\n"
    "v_mfma_f32_32x32x16_bf16 v[160:175], v[200:203], v[144:147], v[160:175]\n"
    "ds_read_b64_tr_b16 v[196:197], v13 offset:20480\n"
    "ds_read_b64_tr_b16 v[198:199], v13 offset:20992\n"
    "v_exp_f32 v211, v209\n"
    "v_fma_f32 v208, v210, -0.5, -1.0\n"
    "v_fma_f32 v209, v211, -0.5, -1.0\n"
    "v_fma_f32 v208, v210, v208, -1.0\n"
    "s_waitcnt lgkmcnt(2)\n"
    "v_mfma_f32_32x32x16_bf16 v[160:175], v[192:195], v[148:151], v[160:175]\n"
    "ds_read_b64_tr_b16 v[200:201], v13 offset:22528\n"
    "ds_read_b64_tr_b16 v[202:203], v13 offset:23040\n"
    "v_fma_f32 v209, v211, v209, -1.0\n"
    "v_rcp_f32 v208, v208\n"
    "s_waitcnt lgkmcnt(2)\n"
    "v_mfma_f32_32x32x16_bf16 v[160:175], v[196:199], v[152:155], v[160:175]\n"
    "s_waitcnt vmcnt(11)\n"
    "s_barrier\n"
    "v_add_u32 v13, 0, v12\n"
    "ds_read_b64_tr_b16 v[192:193], v13 offset:0\n"
    "ds_read_b64_tr_b16 v[194:195], v13 offset:512\n"
    "v_rcp_f32 v209, v209\n"
    "v_fma_f32 v210, v208, v190, v190\n"
    "v_fma_f32 v211, v209, v191, v191\n"
    "v_cvt_pk_bf16_f32 v219, v210, v211\n"
    "ds_read_b128 v[188:191], v14 offset:1120\n"
    "s_add_u32 m0, s69, 98304\n"
    "s_add_u32 s78, s58, 1770112\n"
    "buffer_load_dwordx4 v8, s[12:15], s78 offen lds\n"
    "s_waitcnt lgkmcnt(3)\n"
    "v_mfma_f32_32x32x16_bf16 v[160:175], v[200:203], v[156:159], v[160:175]\n"
    "ds_read_b64_tr_b16 v[196:197], v13 offset:2048\n"
    "ds_read_b64_tr_b16 v[198:199], v13 offset:2560\n"
    "s_add_u32 s78, s64, 17408\n"
    "buffer_store_dwordx4 v[216:219], v2, s[20:23], s78 offen sc1\n"
    "s_waitcnt lgkmcnt(2)\n"
    "v_mfma_f32_32x32x16_bf16 v[176:191], v[192:195], v[16:19], v[176:191]\n"
    "ds_read_b64_tr_b16 v[200:201], v13 offset:4096\n"
    "ds_read_b64_tr_b16 v[202:203], v13 offset:4608\n"
    "s_waitcnt lgkmcnt(2)\n"
    "v_mfma_f32_32x32x16_bf16 v[176:191], v[196:199], v[20:23], v[176:191]\n"
    "ds_read_b64_tr_b16 v[192:193], v13 offset:6144\n"
    "ds_read_b64_tr_b16 v[194:195], v13 offset:6656\n"
    "s_waitcnt lgkmcnt(2)\n"
    "v_mfma_f32_32x32x16_bf16 v[176:191], v[200:203], v[24:27], v[176:191]\n"
    "ds_read_b64_tr_b16 v[196:197], v13 offset:8192\n"
    "ds_read_b64_tr_b16 v[198:199], v13 offset:8704\n"
    "v_mul_f32 v208, s70, v238\n"
    "v_mul_f32 v209, s70, v239\n"
    "v_exp_f32 v210, v208\n"
    "s_add_u32 m0, s69, 99328\n"
    "s_add_u32 s78, s58, 1843840\n"
    "buffer_load_dwordx4 v8, s[12:15], s78 offen lds\n"
    "s_waitcnt lgkmcnt(2)\n"
    "v_mfma_f32_32x32x16_bf16 v[176:191], v[192:195], v[28:31], v[176:191]\n"
    "ds_read_b64_tr_b16 v[200:201], v13 offset:10240\n"
    "ds_read_b64_tr_b16 v[202:203], v13 offset:10752\n"
    "v_exp_f32 v211, v209\n"
    "v_fma_f32 v208, v210, -0.5, -1.0\n"
    "v_fma_f32 v209, v211, -0.5, -1.0\n"
    "s_waitcnt lgkmcnt(2)\n"
    "v_mfma_f32_32x32x16_bf16 v[176:191], v[196:199], v[32:35], v[176:191]\n"
    "ds_read_b64_tr_b16 v[192:193], v13 offset:12288\n"
    "ds_read_b64_tr_b16 v[194:195], v13 offset:12800\n"
    "v_fma_f32 v208, v210, v208, -1.0\n"
    "v_fma_f32 v209, v211, v209, -1.0\n"
    "v_rcp_f32 v208, v208\n"
    "s_waitcnt lgkmcnt(2)\n"
    "v_mfma_f32_32x32x16_bf16 v[176:191], v[200:203], v[36:39], v[176:191]\n"
    "ds_read_b64_tr_b16 v[196:197], v13 offset:14336\n"
    "ds_read_b64_tr_b16 v[198:199], v13 offset:14848\n"
    "v_rcp_f32 v209, v209\n"
    "v_fma_f32 v210, v208, v238, v238\n"
    "v_fma_f32 v211, v209, v239, v239\n"
    "s_waitcnt lgkmcnt(2)\n"
    "v_mfma_f32_32x32x16_bf16 v[176:191], v[192:195], v[40:43], v[176:191]\n"
    "ds_read_b64_tr_b16 v[200:201], v13 offset:16384\n"
    "ds_read_b64_tr_b16 v[202:203], v13 offset:16896\n"
    "v_cvt_pk_bf16_f32 v212, v210, v211\n"
    "v_mul_f32 v208, s70, v240\n"
    "v_mul_f32 v209, s70, v241\n"
    "v_exp_f32 v210, v208\n"
    "s_add_u32 m0, s69, 100352\n"
    "s_add_u32 s78, s58, 1917568\n"
    "buffer_load_dwordx4 v8, s[12:15], s78 offen lds\n"
    "s_waitcnt lgkmcnt(2)\n"
    "v_mfma_f32_32x32x16_bf16 v[176:191], v[196:199], v[44:47], v[176:191]\n"
    "ds_read_b64_tr_b16 v[192:193], v13 offset:18432\n"
    "ds_read_b64_tr_b16 v[194:195], v13 offset:18944\n"
    "v_exp_f32 v211, v209\n"
    "v_fma_f32 v208, v210, -0.5, -1.0\n"
    "v_fma_f32 v209, v211, -0.5, -1.0\n"
    "s_waitcnt lgkmcnt(2)\n"
    "v_mfma_f32_32x32x16_bf16 v[176:191], v[200:203], v[48:51], v[176:191]\n"
    "ds_read_b64_tr_b16 v[196:197], v13 offset:20480\n"
    "ds_read_b64_tr_b16 v[198:199], v13 offset:20992\n"
    "v_fma_f32 v208, v210, v208, -1.0\n"
    "v_fma_f32 v209, v211, v209, -1.0\n"
    "v_rcp_f32 v208, v208\n"
    "s_waitcnt lgkmcnt(2)\n"
    "v_mfma_f32_32x32x16_bf16 v[176:191], v[192:195], v[52:55], v[176:191]\n"
    "ds_read_b64_tr_b16 v[200:201], v13 offset:22528\n"
    "ds_read_b64_tr_b16 v[202:203], v13 offset:23040\n"
    "v_rcp_f32 v209, v209\n"
    "v_fma_f32 v210, v208, v240, v240\n"
    "v_fma_f32 v211, v209, v241, v241\n"
    "s_waitcnt lgkmcnt(2)\n"
    "v_mfma_f32_32x32x16_bf16 v[176:191], v[196:199], v[56:59], v[176:191]\n"
    "s_waitcnt vmcnt(11)\n"
    "s_barrier\n"
    "v_add_u32 v13, 24576, v12\n"
    "ds_read_b64_tr_b16 v[192:193], v13 offset:0\n"
    "ds_read_b64_tr_b16 v[194:195], v13 offset:512\n"
    "v_cvt_pk_bf16_f32 v213, v210, v211\n"
    "v_mul_f32 v208, s70, v242\n"
    "v_mul_f32 v209, s70, v243\n"
    "v_exp_f32 v210, v208\n"
    "ds_read_b128 v[238:241], v14 offset:1280\n"
    "s_add_u32 m0, s69, 122880\n"
    "s_add_u32 s78, s58, 3539584\n"
    "buffer_load_dwordx4 v8, s[12:15], s78 offen lds\n"
    "s_waitcnt lgkmcnt(3)\n"
    "v_mfma_f32_32x32x16_bf16 v[176:191], v[200:203], v[60:63], v[176:191]\n"
    "ds_read_b64_tr_b16 v[196:197], v13 offset:2048\n"
    "ds_read_b64_tr_b16 v[198:199], v13 offset:2560\n"
    "v_exp_f32 v211, v209\n"
    "v_fma_f32 v208, v210, -0.5, -1.0\n"
    "s_waitcnt lgkmcnt(3)\n"
    "v_mfma_f32_32x32x16_bf16 v[176:191], v[192:195], v[64:67], v[176:191]\n"
    "ds_read_b64_tr_b16 v[200:201], v13 offset:4096\n"
    "ds_read_b64_tr_b16 v[202:203], v13 offset:4608\n"
    "v_fma_f32 v209, v211, -0.5, -1.0\n"
    "v_fma_f32 v208, v210, v208, -1.0\n"
    "v_fma_f32 v209, v211, v209, -1.0\n"
    "v_rcp_f32 v208, v208\n"
    "s_waitcnt lgkmcnt(2)\n"
    "v_mfma_f32_32x32x16_bf16 v[176:191], v[196:199], v[68:71], v[176:191]\n"
    "ds_read_b64_tr_b16 v[192:193], v13 offset:6144\n"
    "ds_read_b64_tr_b16 v[194:195], v13 offset:6656\n"
    "v_rcp_f32 v209, v209\n"
    "v_fma_f32 v210, v208, v242, v242\n"
    "s_waitcnt lgkmcnt(2)\n"
    "v_mfma_f32_32x32x16_bf16 v[176:191], v[200:203], v[72:75], v[176:191]\n"
    "ds_read_b64_tr_b16 v[196:197], v13 offset:8192\n"
    "ds_read_b64_tr_b16 v[198:199], v13 offset:8704\n"
    "v_fma_f32 v211, v209, v243, v243\n"
    "v_cvt_pk_bf16_f32 v214, v210, v211\n"
    "v_mul_f32 v208, s70, v244\n"
    "v_mul_f32 v209, s70, v245\n"
    "s_add_u32 m0, s69, 123904\n"
    "s_add_u32 s78, s58, 3613312\n"
    "buffer_load_dwordx4 v8, s[12:15], s78 offen lds\n"
    "s_waitcnt lgkmcnt(2)\n"
    "v_mfma_f32_32x32x16_bf16 v[176:191], v[192:195], v[76:79], v[176:191]\n"
    "ds_read_b64_tr_b16 v[200:201], v13 offset:10240\n"
    "ds_read_b64_tr_b16 v[202:203], v13 offset:10752\n"
    "v_exp_f32 v210, v208\n"
    "v_exp_f32 v211, v209\n"
    "v_fma_f32 v208, v210, -0.5, -1.0\n"
    "s_waitcnt lgkmcnt(2)\n"
    "v_mfma_f32_32x32x16_bf16 v[176:191], v[196:199], v[80:83], v[176:191]\n"
    "ds_read_b64_tr_b16 v[192:193], v13 offset:12288\n"
    "ds_read_b64_tr_b16 v[194:195], v13 offset:12800\n"
    "v_fma_f32 v209, v211, -0.5, -1.0\n"
    "v_fma_f32 v208, v210, v208, -1.0\n"
    "v_fma_f32 v209, v211, v209, -1.0\n"
    "s_waitcnt lgkmcnt(2)\n"
    "v_mfma_f32_32x32x16_bf16 v[176:191], v[200:203], v[84:87], v[176:191]\n"
    "ds_read_b64_tr_b16 v[196:197], v13 offset:14336\n"
    "ds_read_b64_tr_b16 v[198:199], v13 offset:14848\n"
    "v_rcp_f32 v208, v208\n"
    "v_rcp_f32 v209, v209\n"
    "v_fma_f32 v210, v208, v244, v244\n"
    "s_waitcnt lgkmcnt(2)\n"
    "v_mfma_f32_32x32x16_bf16 v[176:191], v[192:195], v[88:91], v[176:191]\n"
    "ds_read_b64_tr_b16 v[200:201], v13 offset:16384\n"
    "ds_read_b64_tr_b16 v[202:203], v13 offset:16896\n"
    "v_fma_f32 v211, v209, v245, v245\n"
    "v_cvt_pk_bf16_f32 v215, v210, v211\n"
    "v_mul_f32 v208, s70, v246\n"
    "v_mul_f32 v209, s70, v247\n"
    "ds_read_b128 v[242:245], v14 offset:1312\n"
    "s_add_u32 m0, s69, 124928\n"
    "s_add_u32 s78, s58, 3687040\n"
    "buffer_load_dwordx4 v8, s[12:15], s78 offen lds\n"
    "s_waitcnt lgkmcnt(3)\n"
    "v_mfma_f32_32x32x16_bf16 v[176:191], v[196:199], v[92:95], v[176:191]\n"
    "ds_read_b64_tr_b16 v[192:193], v13 offset:18432\n"
    "ds_read_b64_tr_b16 v[194:195], v13 offset:18944\n"
    "v_exp_f32 v210, v208\n"
    "v_exp_f32 v211, v209\n"
    "s_add_u32 s78, s64, 32768\n"
    "buffer_store_dwordx4 v[212:215], v2, s[20:23], s78 offen sc1\n"
    "s_waitcnt lgkmcnt(3)\n"
    "v_mfma_f32_32x32x16_bf16 v[176:191], v[200:203], v[96:99], v[176:191]\n"
    "ds_read_b64_tr_b16 v[196:197], v13 offset:20480\n"
    "ds_read_b64_tr_b16 v[198:199], v13 offset:20992\n"
    "v_fma_f32 v208, v210, -0.5, -1.0\n"
    "v_fma_f32 v209, v211, -0.5, -1.0\n"
    "v_fma_f32 v208, v210, v208, -1.0\n"
    "v_fma_f32 v209, v211, v209, -1.0\n"
    "s_waitcnt lgkmcnt(2)\n"
    "v_mfma_f32_32x32x16_bf16 v[176:191], v[192:195], v[100:103], v[176:191]\n"
    "ds_read_b64_tr_b16 v[200:201], v13 offset:22528\n"
    "ds_read_b64_tr_b16 v[202:203], v13 offset:23040\n"
    "v_rcp_f32 v208, v208\n"
    "v_rcp_f32 v209, v209\n"
    "s_waitcnt lgkmcnt(2)\n"
    "v_mfma_f32_32x32x16_bf16 v[176:191], v[196:199], v[104:107], v[176:191]\n"
    "s_waitcnt vmcnt(12)\n"
    "s_barrier\n"
    "v_add_u32 v13, 49152, v12\n"
    "ds_read_b64_tr_b16 v[192:193], v13 offset:0\n"
    "ds_read_b64_tr_b16 v[194:195], v13 offset:512\n"
    "v_fma_f32 v210, v208, v246, v246\n"
    "v_fma_f32 v211, v209, v247, v247\n"
    "v_cvt_pk_bf16_f32 v216, v210, v211\n"
    "v_mul_f32 v208, s70, v248\n"
    "v_mul_f32 v209, s70, v249\n"
    "s_add_u32 m0, s69, 0\n"
    "s_add_u32 s78, s61, 221184\n"
    "buffer_load_dwordx4 v9, s[16:19], s78 offen lds\n"
    "s_waitcnt lgkmcnt(2)\n"
    "v_mfma_f32_32x32x16_bf16 v[176:191], v[200:203], v[108:111], v[176:191]\n"
    "ds_read_b64_tr_b16 v[196:197], v13 offset:2048\n"
    "ds_read_b64_tr_b16 v[198:199], v13 offset:2560\n"
    "v_exp_f32 v210, v208\n"
    "v_exp_f32 v211, v209\n"
    "s_waitcnt lgkmcnt(2)\n"
    "v_mfma_f32_32x32x16_bf16 v[176:191], v[192:195], v[112:115], v[176:191]\n"
    "ds_read_b64_tr_b16 v[200:201], v13 offset:4096\n"
    "ds_read_b64_tr_b16 v[202:203], v13 offset:4608\n"
    "v_fma_f32 v208, v210, -0.5, -1.0\n"
    "v_fma_f32 v209, v211, -0.5, -1.0\n"
    "v_fma_f32 v208, v210, v208, -1.0\n"
    "v_fma_f32 v209, v211, v209, -1.0\n"
    "s_waitcnt lgkmcnt(2)\n"
    "v_mfma_f32_32x32x16_bf16 v[176:191], v[196:199], v[116:119], v[176:191]\n"
    "ds_read_b64_tr_b16 v[192:193], v13 offset:6144\n"
    "ds_read_b64_tr_b16 v[194:195], v13 offset:6656\n"
    "v_rcp_f32 v208, v208\n"
    "v_rcp_f32 v209, v209\n"
    "s_waitcnt lgkmcnt(2)\n"
    "v_mfma_f32_32x32x16_bf16 v[176:191], v[200:203], v[120:123], v[176:191]\n"
    "ds_read_b64_tr_b16 v[196:197], v13 offset:8192\n"
    "ds_read_b64_tr_b16 v[198:199], v13 offset:8704\n"
    "v_fma_f32 v210, v208, v248, v248\n"
    "v_fma_f32 v211, v209, v249, v249\n"
    "v_cvt_pk_bf16_f32 v217, v210, v211\n"
    "v_mul_f32 v208, s70, v250\n"
    "ds_read_b128 v[246:249], v14 offset:1344\n"
    "s_add_u32 m0, s69, 1024\n"
    "s_add_u32 s78, s61, 221184\n"
    "buffer_load_dwordx4 v10, s[16:19], s78 offen lds\n"
    "s_waitcnt lgkmcnt(3)\n"
    "v_mfma_f32_32x32x16_bf16 v[176:191], v[192:195], v[124:127], v[176:191]\n"
    "ds_read_b64_tr_b16 v[200:201], v13 offset:10240\n"
    "ds_read_b64_tr_b16 v[202:203], v13 offset:10752\n"
    "v_mul_f32 v209, s70, v251\n"
    "v_exp_f32 v210, v208\n"
    "v_exp_f32 v211, v209\n"
    "s_waitcnt lgkmcnt(3)\n"
    "v_mfma_f32_32x32x16_bf16 v[176:191], v[196:199], v[128:131], v[176:191]\n"
    "ds_read_b64_tr_b16 v[192:193], v13 offset:12288\n"
    "ds_read_b64_tr_b16 v[194:195], v13 offset:12800\n"
    "v_fma_f32 v208, v210, -0.5, -1.0\n"
    "v_fma_f32 v209, v211, -0.5, -1.0\n"
    "v_fma_f32 v208, v210, v208, -1.0\n"
    "s_waitcnt lgkmcnt(2)\n"
    "v_mfma_f32_32x32x16_bf16 v[176:191], v[200:203], v[132:135], v[176:191]\n"
    "ds_read_b64_tr_b16 v[196:197], v13 offset:14336\n"
    "ds_read_b64_tr_b16 v[198:199], v13 offset:14848\n"
    "v_fma_f32 v209, v211, v209, -1.0\n"
    "v_rcp_f32 v208, v208\n"
    "v_rcp_f32 v209, v209\n"
    "s_waitcnt lgkmcnt(2)\n"
    "v_mfma_f32_32x32x16_bf16 v[176:191], v[192:195], v[136:139], v[176:191]\n"
    "ds_read_b64_tr_b16 v[200:201], v13 offset:16384\n"
    "ds_read_b64_tr_b16 v[202:203], v13 offset:16896\n"
    "v_fma_f32 v210, v208, v250, v250\n"
    "v_fma_f32 v211, v209, v251, v251\n"
    "v_cvt_pk_bf16_f32 v218, v210, v211\n"
    "v_mul_f32 v208, s70, v252\n"
    "s_add_u32 m0, s69, 2048\n"
    "s_add_u32 s78, s61, 221184\n"
    "buffer_load_dwordx4 v11, s[16:19], s78 offen lds\n"
    "s_waitcnt lgkmcnt(2)\n"
    "v_mfma_f32_32x32x16_bf16 v[176:191], v[196:199], v[140:143], v[176:191]\n"
    "ds_read_b64_tr_b16 v[192:193], v13 offset:18432\n"
    "ds_read_b64_tr_b16 v[194:195], v13 offset:18944\n"
    "v_mul_f32 v209, s70, v253\n"
    "v_exp_f32 v210, v208\n"
    "s_waitcnt lgkmcnt(2)\n"
    "v_mfma_f32_32x32x16_bf16 v[176:191], v[200:203], v[144:147], v[176:191]\n"
    "ds_read_b64_tr_b16 v[196:197], v13 offset:20480\n"
    "ds_read_b64_tr_b16 v[198:199], v13 offset:20992\n"
    "v_exp_f32 v211, v209\n"
    "v_fma_f32 v208, v210, -0.5, -1.0\n"
    "v_fma_f32 v209, v211, -0.5, -1.0\n"
    "v_fma_f32 v208, v210, v208, -1.0\n"
    "s_waitcnt lgkmcnt(2)\n"
    "v_mfma_f32_32x32x16_bf16 v[176:191], v[192:195], v[148:151], v[176:191]\n"
    "ds_read_b64_tr_b16 v[200:201], v13 offset:22528\n"
    "ds_read_b64_tr_b16 v[202:203], v13 offset:23040\n"
    "v_fma_f32 v209, v211, v209, -1.0\n"
    "v_rcp_f32 v208, v208\n"
    "s_waitcnt lgkmcnt(2)\n"
    "v_mfma_f32_32x32x16_bf16 v[176:191], v[196:199], v[152:155], v[176:191]\n"
    "s_waitcnt vmcnt(11)\n"
    "s_barrier\n"
    "v_add_u32 v13, 73728, v12\n"
    "ds_read_b64_tr_b16 v[192:193], v13 offset:0\n"
    "ds_read_b64_tr_b16 v[194:195], v13 offset:512\n"
    "v_rcp_f32 v209, v209\n"
    "v_fma_f32 v210, v208, v252, v252\n"
    "v_fma_f32 v211, v209, v253, v253\n"
    "v_cvt_pk_bf16_f32 v219, v210, v211\n"
    "ds_read_b128 v[250:253], v14 offset:1376\n"
    "s_add_u32 m0, s69, 24576\n"
    "s_nop 0\n"
    "buffer_load_dwordx4 v9, s[16:19], s61 offen lds\n"
    "s_waitcnt lgkmcnt(3)\n"
    "v_mfma_f32_32x32x16_bf16 v[176:191], v[200:203], v[156:159], v[176:191]\n"
    "ds_read_b64_tr_b16 v[196:197], v13 offset:2048\n"
    "ds_read_b64_tr_b16 v[198:199], v13 offset:2560\n"
    "s_add_u32 s78, s64, 33792\n"
    "buffer_store_dwordx4 v[216:219], v2, s[20:23], s78 offen sc1\n"
    "s_waitcnt lgkmcnt(2)\n"
    "v_mfma_f32_32x32x16_bf16 v[238:253], v[192:195], v[16:19], v[238:253]\n"
    "ds_read_b64_tr_b16 v[200:201], v13 offset:4096\n"
    "ds_read_b64_tr_b16 v[202:203], v13 offset:4608\n"
    "s_waitcnt lgkmcnt(2)\n"
    "v_mfma_f32_32x32x16_bf16 v[238:253], v[196:199], v[20:23], v[238:253]\n"
    "ds_read_b64_tr_b16 v[192:193], v13 offset:6144\n"
    "ds_read_b64_tr_b16 v[194:195], v13 offset:6656\n"
    "s_waitcnt lgkmcnt(2)\n"
    "v_mfma_f32_32x32x16_bf16 v[238:253], v[200:203], v[24:27], v[238:253]\n"
    "ds_read_b64_tr_b16 v[196:197], v13 offset:8192\n"
    "ds_read_b64_tr_b16 v[198:199], v13 offset:8704\n"
    "v_mul_f32 v208, s70, v160\n"
    "v_mul_f32 v209, s70, v161\n"
    "v_exp_f32 v210, v208\n"
    "s_add_u32 m0, s69, 25600\n"
    "s_nop 0\n"
    "buffer_load_dwordx4 v10, s[16:19], s61 offen lds\n"
    "s_waitcnt lgkmcnt(2)\n"
    "v_mfma_f32_32x32x16_bf16 v[238:253], v[192:195], v[28:31], v[238:253]\n"
    "ds_read_b64_tr_b16 v[200:201], v13 offset:10240\n"
    "ds_read_b64_tr_b16 v[202:203], v13 offset:10752\n"
    "v_exp_f32 v211, v209\n"
    "v_fma_f32 v208, v210, -0.5, -1.0\n"
    "v_fma_f32 v209, v211, -0.5, -1.0\n"
    "s_waitcnt lgkmcnt(2)\n"
    "v_mfma_f32_32x32x16_bf16 v[238:253], v[196:199], v[32:35], v[238:253]\n"
    "ds_read_b64_tr_b16 v[192:193], v13 offset:12288\n"
    "ds_read_b64_tr_b16 v[194:195], v13 offset:12800\n"
    "v_fma_f32 v208, v210, v208, -1.0\n"
    "v_fma_f32 v209, v211, v209, -1.0\n"
    "v_rcp_f32 v208, v208\n"
    "s_waitcnt lgkmcnt(2)\n"
    "v_mfma_f32_32x32x16_bf16 v[238:253], v[200:203], v[36:39], v[238:253]\n"
    "ds_read_b64_tr_b16 v[196:197], v13 offset:14336\n"
    "ds_read_b64_tr_b16 v[198:199], v13 offset:14848\n"
    "v_rcp_f32 v209, v209\n"
    "v_fma_f32 v210, v208, v160, v160\n"
    "v_fma_f32 v211, v209, v161, v161\n"
    "s_waitcnt lgkmcnt(2)\n"
    "v_mfma_f32_32x32x16_bf16 v[238:253], v[192:195], v[40:43], v[238:253]\n"
    "ds_read_b64_tr_b16 v[200:201], v13 offset:16384\n"
    "ds_read_b64_tr_b16 v[202:203], v13 offset:16896\n"
    "v_cvt_pk_bf16_f32 v16, v210, v211\n"
    "v_mul_f32 v208, s70, v162\n"
    "v_mul_f32 v209, s70, v163\n"
    "v_exp_f32 v210, v208\n"
    "s_add_u32 m0, s69, 26624\n"
    "s_nop 0\n"
    "buffer_load_dwordx4 v11, s[16:19], s61 offen lds\n"
    "s_waitcnt lgkmcnt(2)\n"
    "v_mfma_f32_32x32x16_bf16 v[238:253], v[196:199], v[44:47], v[238:253]\n"
    "ds_read_b64_tr_b16 v[192:193], v13 offset:18432\n"
    "ds_read_b64_tr_b16 v[194:195], v13 offset:18944\n"
    "v_exp_f32 v211, v209\n"
    "v_fma_f32 v208, v210, -0.5, -1.0\n"
    "v_fma_f32 v209, v211, -0.5, -1.0\n"
    "s_waitcnt lgkmcnt(2)\n"
    "v_mfma_f32_32x32x16_bf16 v[238:253], v[200:203], v[48:51], v[238:253]\n"
    "ds_read_b64_tr_b16 v[196:197], v13 offset:20480\n"
    "ds_read_b64_tr_b16 v[198:199], v13 offset:20992\n"
    "v_fma_f32 v208, v210, v208, -1.0\n"
    "v_fma_f32 v209, v211, v209, -1.0\n"
    "v_rcp_f32 v208, v208\n"
    "s_waitcnt lgkmcnt(2)\n"
    "v_mfma_f32_32x32x16_bf16 v[238:253], v[192:195], v[52:55], v[238:253]\n"
    "ds_read_b64_tr_b16 v[200:201], v13 offset:22528\n"
    "ds_read_b64_tr_b16 v[202:203], v13 offset:23040\n"
    "v_rcp_f32 v209, v209\n"
    "v_fma_f32 v210, v208, v162, v162\n"
    "v_fma_f32 v211, v209, v163, v163\n"
    "s_waitcnt lgkmcnt(2)\n"
    "v_mfma_f32_32x32x16_bf16 v[238:253], v[196:199], v[56:59], v[238:253]\n"
    "s_waitcnt vmcnt(11)\n"
    "s_barrier\n"
    "v_add_u32 v13, 98304, v12\n"
    "ds_read_b64_tr_b16 v[192:193], v13 offset:0\n"
    "ds_read_b64_tr_b16 v[194:195], v13 offset:512\n"
    "v_cvt_pk_bf16_f32 v17, v210, v211\n"
    "v_mul_f32 v208, s70, v164\n"
    "v_mul_f32 v209, s70, v165\n"
    "v_exp_f32 v210, v208\n"
    "s_add_u32 m0, s69, 49152\n"
    "s_add_u32 s78, s61, 73728\n"
    "buffer_load_dwordx4 v9, s[16:19], s78 offen lds\n"
    "s_waitcnt lgkmcnt(2)\n"
    "v_mfma_f32_32x32x16_bf16 v[238:253], v[200:203], v[60:63], v[238:253]\n"
    "ds_read_b64_tr_b16 v[196:197], v13 offset:2048\n"
    "ds_read_b64_tr_b16 v[198:199], v13 offset:2560\n"
    "v_exp_f32 v211, v209\n"
    "v_fma_f32 v208, v210, -0.5, -1.0\n"
    "s_waitcnt lgkmcnt(2)\n"
    "v_mfma_f32_32x32x16_bf16 v[238:253], v[192:195], v[64:67], v[238:253]\n"
    "ds_read_b64_tr_b16 v[200:201], v13 offset:4096\n"
    "ds_read_b64_tr_b16 v[202:203], v13 offset:4608\n"
    "v_fma_f32 v209, v211, -0.5, -1.0\n"
    "v_fma_f32 v208, v210, v208, -1.0\n"
    "v_fma_f32 v209, v211, v209, -1.0\n"
    "v_rcp_f32 v208, v208\n"
    "s_add_u32 m0, s69, 50176\n"
    "s_add_u32 s78, s61, 73728\n"
    "buffer_load_dwordx4 v10, s[16:19], s78 offen lds\n"
    "s_waitcnt lgkmcnt(2)\n"
    "v_mfma_f32_32x32x16_bf16 v[238:253], v[196:199], v[68:71], v[238:253]\n"
    "ds_read_b64_tr_b16 v[192:193], v13 offset:6144\n"
    "ds_read_b64_tr_b16 v[194:195], v13 offset:6656\n"
    "v_rcp_f32 v209, v209\n"
    "v_fma_f32 v210, v208, v164, v164\n"
    "s_waitcnt lgkmcnt(2)\n"
    "v_mfma_f32_32x32x16_bf16 v[238:253], v[200:203], v[72:75], v[238:253]\n"
    "ds_read_b64_tr_b16 v[196:197], v13 offset:8192\n"
    "ds_read_b64_tr_b16 v[198:199], v13 offset:8704\n"
    "v_fma_f32 v211, v209, v165, v165\n"
    "v_cvt_pk_bf16_f32 v18, v210, v211\n"
    "v_mul_f32 v208, s70, v166\n"
    "v_mul_f32 v209, s70, v167\n"
    "s_add_u32 m0, s69, 51200\n"
    "s_add_u32 s78, s61, 73728\n"
    "buffer_load_dwordx4 v11, s[16:19], s78 offen lds\n"
    "s_waitcnt lgkmcnt(2)\n"
    "v_mfma_f32_32x32x16_bf16 v[238:253], v[192:195], v[76:79], v[238:253]\n"
    "ds_read_b64_tr_b16 v[200:201], v13 offset:10240\n"
    "ds_read_b64_tr_b16 v[202:203], v13 offset:10752\n"
    "v_exp_f32 v210, v208\n"
    "v_exp_f32 v211, v209\n"
    "v_fma_f32 v208, v210, -0.5, -1.0\n"
    "s_waitcnt lgkmcnt(2)\n"
    "v_mfma_f32_32x32x16_bf16 v[238:253], v[196:199], v[80:83], v[238:253]\n"
    "ds_read_b64_tr_b16 v[192:193], v13 offset:12288\n"
    "ds_read_b64_tr_b16 v[194:195], v13 offset:12800\n"
    "v_fma_f32 v209, v211, -0.5, -1.0\n"
    "v_fma_f32 v208, v210, v208, -1.0\n"
    "v_fma_f32 v209, v211, v209, -1.0\n"
    "s_waitcnt lgkmcnt(2)\n"
    "v_mfma_f32_32x32x16_bf16 v[238:253], v[200:203], v[84:87], v[238:253]\n"
    "ds_read_b64_tr_b16 v[196:197], v13 offset:14336\n"
    "ds_read_b64_tr_b16 v[198:199], v13 offset:14848\n"
    "v_rcp_f32 v208, v208\n"
    "v_rcp_f32 v209, v209\n"
    "v_fma_f32 v210, v208, v166, v166\n"
    "s_add_u32 s78, s64, 0\n"
    "buffer_load_dwordx4 v[48:51], v2, s[20:23], s78 offen sc1\n"
    "s_waitcnt lgkmcnt(2)\n"
    "v_mfma_f32_32x32x16_bf16 v[238:253], v[192:195], v[88:91], v[238:253]\n"
    "ds_read_b64_tr_b16 v[200:201], v13 offset:16384\n"
    "ds_read_b64_tr_b16 v[202:203], v13 offset:16896\n"
    "v_fma_f32 v211, v209, v167, v167\n"
    "v_cvt_pk_bf16_f32 v19, v210, v211\n"
    "v_mul_f32 v208, s70, v168\n"
    "v_mul_f32 v209, s70, v169\n"
    "s_waitcnt lgkmcnt(2)\n"
    "v_mfma_f32_32x32x16_bf16 v[238:253], v[196:199], v[92:95], v[238:253]\n"
    "ds_read_b64_tr_b16 v[192:193], v13 offset:18432\n"
    "ds_read_b64_tr_b16 v[194:195], v13 offset:18944\n"
    "v_exp_f32 v210, v208\n"
    "v_exp_f32 v211, v209\n"
    "s_add_u32 s78, s64, 49152\n"
    "buffer_store_dwordx4 v[16:19], v2, s[20:23], s78 offen sc1\n"
    "s_add_u32 s78, s64, 1024\n"
    "buffer_load_dwordx4 v[52:55], v2, s[20:23], s78 offen sc1\n"
    "s_waitcnt lgkmcnt(2)\n"
    "v_mfma_f32_32x32x16_bf16 v[238:253], v[200:203], v[96:99], v[238:253]\n"
    "ds_read_b64_tr_b16 v[196:197], v13 offset:20480\n"
    "ds_read_b64_tr_b16 v[198:199], v13 offset:20992\n"
    "v_fma_f32 v208, v210, -0.5, -1.0\n"
    "v_fma_f32 v209, v211, -0.5, -1.0\n"
    "v_fma_f32 v208, v210, v208, -1.0\n"
    "v_fma_f32 v209, v211, v209, -1.0\n"
    "s_waitcnt lgkmcnt(2)\n"
    "v_mfma_f32_32x32x16_bf16 v[238:253], v[192:195], v[100:103], v[238:253]\n"
    "ds_read_b64_tr_b16 v[200:201], v13 offset:22528\n"
    "ds_read_b64_tr_b16 v[202:203], v13 offset:23040\n"
    "v_rcp_f32 v208, v208\n"
    "v_rcp_f32 v209, v209\n"
    "s_waitcnt lgkmcnt(2)\n"
    "v_mfma_f32_32x32x16_bf16 v[238:253], v[196:199], v[104:107], v[238:253]\n"
    "s_waitcnt vmcnt(8)\n"
    "s_barrier\n"
    "s_add_u32 s78, s59, 0\n"
    "s_mov_b64 exec, 1\n"
    "buffer_store_dwordx2 v[4:5], off, s[20:23], s78 sc1\n"
    "s_mov_b64 exec, -1\n"
    "v_add_u32 v13, 122880, v12\n"
    "ds_read_b64_tr_b16 v[192:193], v13 offset:0\n"
    "ds_read_b64_tr_b16 v[194:195], v13 offset:512\n"
    "v_fma_f32 v210, v208, v168, v168\n"
    "v_fma_f32 v211, v209, v169, v169\n"
    "v_cvt_pk_bf16_f32 v20, v210, v211\n"
    "v_mul_f32 v208, s70, v170\n"
    "v_mul_f32 v209, s70, v171\n"
    "s_add_u32 m0, s69, 73728\n"
    "s_add_u32 s78, s61, 147456\n"
    "buffer_load_dwordx4 v9, s[16:19], s78 offen lds\n"
    "s_waitcnt lgkmcnt(2)\n"
    "v_mfma_f32_32x32x16_bf16 v[238:253], v[200:203], v[108:111], v[238:253]\n"
    "ds_read_b64_tr_b16 v[196:197], v13 offset:2048\n"
    "ds_read_b64_tr_b16 v[198:199], v13 offset:2560\n"
    "v_exp_f32 v210, v208\n"
    "v_exp_f32 v211, v209\n"
    "s_add_u32 m0, s69, 74752\n"
    "s_add_u32 s78, s61, 147456\n"
    "buffer_load_dwordx4 v10, s[16:19], s78 offen lds\n"
    "s_waitcnt lgkmcnt(2)\n"
    "v_mfma_f32_32x32x16_bf16 v[238:253], v[192:195], v[112:115], v[238:253]\n"
    "ds_read_b64_tr_b16 v[200:201], v13 offset:4096\n"
    "ds_read_b64_tr_b16 v[202:203], v13 offset:4608\n"
    "v_fma_f32 v208, v210, -0.5, -1.0\n"
    "v_fma_f32 v209, v211, -0.5, -1.0\n"
    "v_fma_f32 v208, v210, v208, -1.0\n"
    "v_fma_f32 v209, v211, v209, -1.0\n"
    "s_waitcnt lgkmcnt(2)\n"
    "v_mfma_f32_32x32x16_bf16 v[238:253], v[196:199], v[116:119], v[238:253]\n"
    "ds_read_b64_tr_b16 v[192:193], v13 offset:6144\n"
    "ds_read_b64_tr_b16 v[194:195], v13 offset:6656\n"
    "v_rcp_f32 v208, v208\n"
    "v_rcp_f32 v209, v209\n"
    "s_add_u32 m0, s69, 75776\n"
    "s_add_u32 s78, s61, 147456\n"
    "buffer_load_dwordx4 v11, s[16:19], s78 offen lds\n"
    "s_waitcnt lgkmcnt(2)\n"
    "v_mfma_f32_32x32x16_bf16 v[238:253], v[200:203], v[120:123], v[238:253]\n"
    "ds_read_b64_tr_b16 v[196:197], v13 offset:8192\n"
    "ds_read_b64_tr_b16 v[198:199], v13 offset:8704\n"
    "v_fma_f32 v210, v208, v170, v170\n"
    "v_fma_f32 v211, v209, v171, v171\n"
    "v_cvt_pk_bf16_f32 v21, v210, v211\n"
    "v_mul_f32 v208, s70, v172\n"
    "s_waitcnt lgkmcnt(2)\n"
    "v_mfma_f32_32x32x16_bf16 v[238:253], v[192:195], v[124:127], v[238:253]\n"
    "ds_read_b64_tr_b16 v[200:201], v13 offset:10240\n"
    "ds_read_b64_tr_b16 v[202:203], v13 offset:10752\n"
    "v_mul_f32 v209, s70, v173\n"
    "v_exp_f32 v210, v208\n"
    "v_exp_f32 v211, v209\n"
    "s_add_u32 s78, s64, 16384\n"
    "buffer_load_dwordx4 v[56:59], v2, s[20:23], s78 offen sc1\n"
    "s_waitcnt lgkmcnt(2)\n"
    "v_mfma_f32_32x32x16_bf16 v[238:253], v[196:199], v[128:131], v[238:253]\n"
    "ds_read_b64_tr_b16 v[192:193], v13 offset:12288\n"
    "ds_read_b64_tr_b16 v[194:195], v13 offset:12800\n"
    "v_fma_f32 v208, v210, -0.5, -1.0\n"
    "v_fma_f32 v209, v211, -0.5, -1.0\n"
    "v_fma_f32 v208, v210, v208, -1.0\n"
    "s_add_u32 s78, s64, 17408\n"
    "buffer_load_dwordx4 v[60:63], v2, s[20:23], s78 offen sc1\n"
    "s_waitcnt lgkmcnt(2)\n"
    "v_mfma_f32_32x32x16_bf16 v[238:253], v[200:203], v[132:135], v[238:253]\n"
    "ds_read_b64_tr_b16 v[196:197], v13 offset:14336\n"
    "ds_read_b64_tr_b16 v[198:199], v13 offset:14848\n"
    "v_fma_f32 v209, v211, v209, -1.0\n"
    "v_rcp_f32 v208, v208\n"
    "v_rcp_f32 v209, v209\n"
    "s_waitcnt lgkmcnt(2)\n"
    "v_mfma_f32_32x32x16_bf16 v[238:253], v[192:195], v[136:139], v[238:253]\n"
    "ds_read_b64_tr_b16 v[200:201], v13 offset:16384\n"
    "ds_read_b64_tr_b16 v[202:203], v13 offset:16896\n"
    "v_fma_f32 v210, v208, v172, v172\n"
    "v_fma_f32 v211, v209, v173, v173\n"
    "v_cvt_pk_bf16_f32 v22, v210, v211\n"
    "v_mul_f32 v208, s70, v174\n"
    "s_add_u32 s78, s64, 32768\n"
    "buffer_load_dwordx4 v[40:43], v2, s[20:23], s78 offen sc1\n"
    "s_waitcnt lgkmcnt(2)\n"
    "v_mfma_f32_32x32x16_bf16 v[238:253], v[196:199], v[140:143], v[238:253]\n"
    "ds_read_b64_tr_b16 v[192:193], v13 offset:18432\n"
    "ds_read_b64_tr_b16 v[194:195], v13 offset:18944\n"
    "v_mul_f32 v209, s70, v175\n"
    "v_exp_f32 v210, v208\n"
    "s_waitcnt lgkmcnt(2)\n"
    "v_mfma_f32_32x32x16_bf16 v[238:253], v[200:203], v[144:147], v[238:253]\n"
    "ds_read_b64_tr_b16 v[196:197], v13 offset:20480\n"
    "ds_read_b64_tr_b16 v[198:199], v13 offset:20992\n"
    "v_exp_f32 v211, v209\n"
    "v_fma_f32 v208, v210, -0.5, -1.0\n"
    "v_fma_f32 v209, v211, -0.5, -1.0\n"
    "v_fma_f32 v208, v210, v208, -1.0\n"
    "s_add_u32 s78, s64, 33792\n"
    "buffer_load_dwordx4 v[44:47], v2, s[20:23], s78 offen sc1\n"
    "s_waitcnt lgkmcnt(2)\n"
    "v_mfma_f32_32x32x16_bf16 v[238:253], v[192:195], v[148:151], v[238:253]\n"
    "ds_read_b64_tr_b16 v[200:201], v13 offset:22528\n"
    "ds_read_b64_tr_b16 v[202:203], v13 offset:23040\n"
    "v_fma_f32 v209, v211, v209, -1.0\n"
    "v_rcp_f32 v208, v208\n"
    "s_waitcnt lgkmcnt(2)\n"
    "v_mfma_f32_32x32x16_bf16 v[238:253], v[196:199], v[152:155], v[238:253]\n"
    "s_barrier\n"
    "s_add_u32 s78, s60, 0\n"
    "buffer_load_dwordx2 v[6:7], v3, s[20:23], s78 offen sc1\n"
    "v_add_u32 v232, 0, v226\n"
    "ds_read_b64_tr_b16 v[192:193], v232 offset:0\n"
    "ds_read_b64_tr_b16 v[194:195], v232 offset:3072\n"
    "v_rcp_f32 v209, v209\n"
    "v_fma_f32 v210, v208, v174, v174\n"
    "v_fma_f32 v211, v209, v175, v175\n"
    "v_cvt_pk_bf16_f32 v23, v210, v211\n"
    "s_add_u32 m0, s69, 98304\n"
    "s_add_u32 s78, s61, 294912\n"
    "buffer_load_dwordx4 v9, s[16:19], s78 offen lds\n"
    "s_waitcnt lgkmcnt(2)\n"
    "v_mfma_f32_32x32x16_bf16 v[238:253], v[200:203], v[156:159], v[238:253]\n"
    "v_add_u32 v233, 0, v227\n"
    "ds_read_b64_tr_b16 v[196:197], v233 offset:0\n"
    "ds_read_b64_tr_b16 v[198:199], v233 offset:3072\n"
    "s_add_u32 s78, s64, 50176\n"
    "buffer_store_dwordx4 v[20:23], v2, s[20:23], s78 offen sc1\n"
    "s_waitcnt lgkmcnt(2)\n"
    "v_mfma_f32_32x32x16_bf16 v[64:79], v[192:195], v[16:19], 0\n"
    "v_add_u32 v234, 0, v228\n"
    "ds_read_b64_tr_b16 v[200:201], v234 offset:0\n"
    "ds_read_b64_tr_b16 v[202:203], v234 offset:3072\n"
    "s_waitcnt lgkmcnt(2)\n"
    "v_mfma_f32_32x32x16_bf16 v[80:95], v[196:199], v[16:19], 0\n"
    "v_add_u32 v235, 0, v229\n"
    "ds_read_b64_tr_b16 v[192:193], v235 offset:0\n"
    "ds_read_b64_tr_b16 v[194:195], v235 offset:3072\n"
    "s_waitcnt lgkmcnt(2)\n"
    "v_mfma_f32_32x32x16_bf16 v[96:111], v[200:203], v[16:19], 0\n"
    "v_add_u32 v236, 0, v230\n"
    "ds_read_b64_tr_b16 v[196:197], v236 offset:0\n"
    "ds_read_b64_tr_b16 v[198:199], v236 offset:3072\n"
    "v_mul_f32 v208, s70, v176\n"
    "v_mul_f32 v209, s70, v177\n"
    "v_exp_f32 v210, v208\n"
    "s_add_u32 m0, s69, 99328\n"
    "s_add_u32 s78, s61, 294912\n"
    "buffer_load_dwordx4 v10, s[16:19], s78 offen lds\n"
    "s_waitcnt lgkmcnt(2)\n"
    "v_mfma_f32_32x32x16_bf16 v[112:127], v[192:195], v[16:19], 0\n"
    "v_add_u32 v237, 0, v231\n"
    "ds_read_b64_tr_b16 v[200:201], v237 offset:0\n"
    "ds_read_b64_tr_b16 v[202:203], v237 offset:3072\n"
    "v_exp_f32 v211, v209\n"
    "v_fma_f32 v208, v210, -0.5, -1.0\n"
    "v_fma_f32 v209, v211, -0.5, -1.0\n"
    "s_waitcnt lgkmcnt(2)\n"
    "v_mfma_f32_32x32x16_bf16 v[128:143], v[196:199], v[16:19], 0\n"
    "ds_read_b64_tr_b16 v[192:193], v232 offset:6144\n"
    "ds_read_b64_tr_b16 v[194:195], v232 offset:9216\n"
    "v_fma_f32 v208, v210, v208, -1.0\n"
    "v_fma_f32 v209, v211, v209, -1.0\n"
    "v_rcp_f32 v208, v208\n"
    "s_waitcnt lgkmcnt(2)\n"
    "v_mfma_f32_32x32x16_bf16 v[144:159], v[200:203], v[16:19], 0\n"
    "ds_read_b64_tr_b16 v[196:197], v233 offset:6144\n"
    "ds_read_b64_tr_b16 v[198:199], v233 offset:9216\n"
    "v_rcp_f32 v209, v209\n"
    "v_fma_f32 v210, v208, v176, v176\n"
    "v_fma_f32 v211, v209, v177, v177\n"
    "s_waitcnt lgkmcnt(2)\n"
    "v_mfma_f32_32x32x16_bf16 v[64:79], v[192:195], v[20:23], v[64:79]\n"
    "ds_read_b64_tr_b16 v[200:201], v234 offset:6144\n"
    "ds_read_b64_tr_b16 v[202:203], v234 offset:9216\n"
    "v_cvt_pk_bf16_f32 v32, v210, v211\n"
    "v_mul_f32 v208, s70, v178\n"
    "v_mul_f32 v209, s70, v179\n"
    "v_exp_f32 v210, v208\n"
    "s_add_u32 m0, s69, 100352\n"
    "s_add_u32 s78, s61, 294912\n"
    "buffer_load_dwordx4 v11, s[16:19], s78 offen lds\n"
    "s_waitcnt lgkmcnt(2)\n"
    "v_mfma_f32_32x32x16_bf16 v[80:95], v[196:199], v[20:23], v[80:95]\n"
    "ds_read_b64_tr_b16 v[192:193], v235 offset:6144\n"
    "ds_read_b64_tr_b16 v[194:195], v235 offset:9216\n"
    "v_exp_f32 v211, v209\n"
    "v_fma_f32 v208, v210, -0.5, -1.0\n"
    "v_fma_f32 v209, v211, -0.5, -1.0\n"
    "s_waitcnt lgkmcnt(2)\n"
    "v_mfma_f32_32x32x16_bf16 v[96:111], v[200:203], v[20:23], v[96:111]\n"
    "ds_read_b64_tr_b16 v[196:197], v236 offset:6144\n"
    "ds_read_b64_tr_b16 v[198:199], v236 offset:9216\n"
    "v_fma_f32 v208, v210, v208, -1.0\n"
    "v_fma_f32 v209, v211, v209, -1.0\n"
    "v_rcp_f32 v208, v208\n"
    "s_waitcnt lgkmcnt(2)\n"
    "v_mfma_f32_32x32x16_bf16 v[112:127], v[192:195], v[20:23], v[112:127]\n"
    "ds_read_b64_tr_b16 v[200:201], v237 offset:6144\n"
    "ds_read_b64_tr_b16 v[202:203], v237 offset:9216\n"
    "v_rcp_f32 v209, v209\n"
    "v_fma_f32 v210, v208, v178, v178\n"
    "v_fma_f32 v211, v209, v179, v179\n"
    "s_waitcnt lgkmcnt(2)\n"
    "v_mfma_f32_32x32x16_bf16 v[128:143], v[196:199], v[20:23], v[128:143]\n"
    "s_waitcnt vmcnt(13)\n"
    "s_barrier\n"
    "v_add_u32 v232, 24576, v226\n"
    "ds_read_b64_tr_b16 v[192:193], v232 offset:0\n"
    "ds_read_b64_tr_b16 v[194:195], v232 offset:3072\n"
    "v_cvt_pk_bf16_f32 v33, v210, v211\n"
    "v_mul_f32 v208, s70, v180\n"
    "v_mul_f32 v209, s70, v181\n"
    "v_exp_f32 v210, v208\n"
    "s_add_u32 m0, s69, 122880\n"
    "s_nop 0\n"
    "buffer_load_dwordx4 v9, s[16:19], s62 offen lds\n"
    "s_waitcnt lgkmcnt(2)\n"
    "v_mfma_f32_32x32x16_bf16 v[144:159], v[200:203], v[20:23], v[144:159]\n"
    "v_add_u32 v233, 24576, v227\n"
    "ds_read_b64_tr_b16 v[196:197], v233 offset:0\n"
    "ds_read_b64_tr_b16 v[198:199], v233 offset:3072\n"
    "v_exp_f32 v211, v209\n"
    "v_fma_f32 v208, v210, -0.5, -1.0\n"
    "s_waitcnt lgkmcnt(2)\n"
    "v_mfma_f32_32x32x16_bf16 v[64:79], v[192:195], v[48:51], v[64:79]\n"
    "v_add_u32 v234, 24576, v228\n"
    "ds_read_b64_tr_b16 v[200:201], v234 offset:0\n"
    "ds_read_b64_tr_b16 v[202:203], v234 offset:3072\n"
    "v_fma_f32 v209, v211, -0.5, -1.0\n"
    "v_fma_f32 v208, v210, v208, -1.0\n"
    "v_fma_f32 v209, v211, v209, -1.0\n"
    "v_rcp_f32 v208, v208\n"
    "s_waitcnt lgkmcnt(2)\n"
    "v_mfma_f32_32x32x16_bf16 v[80:95], v[196:199], v[48:51], v[80:95]\n"
    "v_add_u32 v235, 24576, v229\n"
    "ds_read_b64_tr_b16 v[192:193], v235 offset:0\n"
    "ds_read_b64_tr_b16 v[194:195], v235 offset:3072\n"
    "v_rcp_f32 v209, v209\n"
    "v_fma_f32 v210, v208, v180, v180\n"
    "s_waitcnt lgkmcnt(2)\n"
    "v_mfma_f32_32x32x16_bf16 v[96:111], v[200:203], v[48:51], v[96:111]\n"
    "v_add_u32 v236, 24576, v230\n"
    "ds_read_b64_tr_b16 v[196:197], v236 offset:0\n"
    "ds_read_b64_tr_b16 v[198:199], v236 offset:3072\n"
    "v_fma_f32 v211, v209, v181, v181\n"
    "v_cvt_pk_bf16_f32 v34, v210, v211\n"
    "v_mul_f32 v208, s70, v182\n"
    "v_mul_f32 v209, s70, v183\n"
    "s_add_u32 m0, s69, 123904\n"
    "s_nop 0\n"
    "buffer_load_dwordx4 v10, s[16:19], s62 offen lds\n"
    "s_waitcnt lgkmcnt(2)\n"
    "v_mfma_f32_32x32x16_bf16 v[112:127], v[192:195], v[48:51], v[112:127]\n"
    "v_add_u32 v237, 24576, v231\n"
    "ds_read_b64_tr_b16 v[200:201], v237 offset:0\n"
    "ds_read_b64_tr_b16 v[202:203], v237 offset:3072\n"
    "v_exp_f32 v210, v208\n"
    "v_exp_f32 v211, v209\n"
    "v_fma_f32 v208, v210, -0.5, -1.0\n"
    "s_waitcnt lgkmcnt(2)\n"
    "v_mfma_f32_32x32x16_bf16 v[128:143], v[196:199], v[48:51], v[128:143]\n"
    "ds_read_b64_tr_b16 v[192:193], v232 offset:6144\n"
    "ds_read_b64_tr_b16 v[194:195], v232 offset:9216\n"
    "v_fma_f32 v209, v211, -0.5, -1.0\n"
    "v_fma_f32 v208, v210, v208, -1.0\n"
    "v_fma_f32 v209, v211, v209, -1.0\n"
    "s_waitcnt lgkmcnt(2)\n"
    "v_mfma_f32_32x32x16_bf16 v[144:159], v[200:203], v[48:51], v[144:159]\n"
    "ds_read_b64_tr_b16 v[196:197], v233 offset:6144\n"
    "ds_read_b64_tr_b16 v[198:199], v233 offset:9216\n"
    "v_rcp_f32 v208, v208\n"
    "v_rcp_f32 v209, v209\n"
    "v_fma_f32 v210, v208, v182, v182\n"
    "s_waitcnt lgkmcnt(2)\n"
    "v_mfma_f32_32x32x16_bf16 v[64:79], v[192:195], v[52:55], v[64:79]\n"
    "ds_read_b64_tr_b16 v[200:201], v234 offset:6144\n"
    "ds_read_b64_tr_b16 v[202:203], v234 offset:9216\n"
    "v_fma_f32 v211, v209, v183, v183\n"
    "v_cvt_pk_bf16_f32 v35, v210, v211\n"
    "v_mul_f32 v208, s70, v184\n"
    "v_mul_f32 v209, s70, v185\n"
    "s_add_u32 m0, s69, 124928\n"
    "s_nop 0\n"
    "buffer_load_dwordx4 v11, s[16:19], s62 offen lds\n"
    "s_waitcnt lgkmcnt(2)\n"
    "v_mfma_f32_32x32x16_bf16 v[80:95], v[196:199], v[52:55], v[80:95]\n"
    "ds_read_b64_tr_b16 v[192:193], v235 offset:6144\n"
    "ds_read_b64_tr_b16 v[194:195], v235 offset:9216\n"
    "v_exp_f32 v210, v208\n"
    "v_exp_f32 v211, v209\n"
    "s_add_u32 s78, s64, 65536\n"
    "buffer_store_dwordx4 v[32:35], v2, s[20:23], s78 offen sc1\n"
    "s_waitcnt lgkmcnt(2)\n"
    "v_mfma_f32_32x32x16_bf16 v[96:111], v[200:203], v[52:55], v[96:111]\n"
    "ds_read_b64_tr_b16 v[196:197], v236 offset:6144\n"
    "ds_read_b64_tr_b16 v[198:199], v236 offset:9216\n"
    "v_fma_f32 v208, v210, -0.5, -1.0\n"
    "v_fma_f32 v209, v211, -0.5, -1.0\n"
    "v_fma_f32 v208, v210, v208, -1.0\n"
    "v_fma_f32 v209, v211, v209, -1.0\n"
    "s_waitcnt lgkmcnt(2)\n"
    "v_mfma_f32_32x32x16_bf16 v[112:127], v[192:195], v[52:55], v[112:127]\n"
    "ds_read_b64_tr_b16 v[200:201], v237 offset:6144\n"
    "ds_read_b64_tr_b16 v[202:203], v237 offset:9216\n"
    "v_rcp_f32 v208, v208\n"
    "v_rcp_f32 v209, v209\n"
    "s_waitcnt lgkmcnt(2)\n"
    "v_mfma_f32_32x32x16_bf16 v[128:143], v[196:199], v[52:55], v[128:143]\n"
    "s_waitcnt vmcnt(8)\n"
    "s_barrier\n"
    "v_cmp_ne_u64 vcc, v[6:7], v[4:5]\n"
    "s_nop 0\n"
    "s_and_b64 vcc, vcc, s[88:89]\n"
    "s_cbranch_vccz Lok_9\n"
    "s_mov_b32 s86, 0x40000\n"
    "s_add_u32 s78, s60, 0\n"
    "Lspin_8:\n"
    "s_sleep 1\n"
    "buffer_load_dwordx2 v[6:7], v3, s[20:23], s78 offen sc1\n"
    "s_waitcnt vmcnt(0)\n"
    "s_sub_u32 s86, s86, 1\n"
    "s_cbranch_scc1 Lok_9\n"
    "v_cmp_ne_u64 vcc, v[6:7], v[4:5]\n"
    "s_nop 1\n"
    "s_and_b64 vcc, vcc, s[88:89]\n"
    "s_cbranch_vccnz Lspin_8\n"
    "Lok_9:\n"
    "s_mov_b64 vcc, s[90:91]\n"
    "v_add_u32 v232, 49152, v226\n"
    "ds_read_b64_tr_b16 v[192:193], v232 offset:0\n"
    "ds_read_b64_tr_b16 v[194:195], v232 offset:3072\n"
    "v_fma_f32 v210, v208, v184, v184\n"
    "v_fma_f32 v211, v209, v185, v185\n"
    "v_cvt_pk_bf16_f32 v36, v210, v211\n"
    "v_mul_f32 v208, s70, v186\n"
    "v_mul_f32 v209, s70, v187\n"
    "s_add_u32 m0, s69, 0\n"
    "s_add_u32 s78, s62, 73728\n"
    "buffer_load_dwordx4 v9, s[16:19], s78 offen lds\n"
    "s_waitcnt lgkmcnt(2)\n"
    "v_mfma_f32_32x32x16_bf16 v[144:159], v[200:203], v[52:55], v[144:159]\n"
    "v_add_u32 v233, 49152, v227\n"
    "ds_read_b64_tr_b16 v[196:197], v233 offset:0\n"
    "ds_read_b64_tr_b16 v[198:199], v233 offset:3072\n"
    "v_exp_f32 v210, v208\n"
    "v_exp_f32 v211, v209\n"
    "s_add_u32 m0, s69, 1024\n"
    "s_add_u32 s78, s62, 73728\n"
    "buffer_load_dwordx4 v10, s[16:19], s78 offen lds\n"
    "s_waitcnt lgkmcnt(2)\n"
    "v_mfma_f32_32x32x16_bf16 v[64:79], v[192:195], v[56:59], v[64:79]\n"
    "v_add_u32 v234, 49152, v228\n"
    "ds_read_b64_tr_b16 v[200:201], v234 offset:0\n"
    "ds_read_b64_tr_b16 v[202:203], v234 offset:3072\n"
    "v_fma_f32 v208, v210, -0.5, -1.0\n"
    "v_fma_f32 v209, v211, -0.5, -1.0\n"
    "v_fma_f32 v208, v210, v208, -1.0\n"
    "v_fma_f32 v209, v211, v209, -1.0\n"
    "s_waitcnt lgkmcnt(2)\n"
    "v_mfma_f32_32x32x16_bf16 v[80:95], v[196:199], v[56:59], v[80:95]\n"
    "v_add_u32 v235, 49152, v229\n"
    "ds_read_b64_tr_b16 v[192:193], v235 offset:0\n"
    "ds_read_b64_tr_b16 v[194:195], v235 offset:3072\n"
    "v_rcp_f32 v208, v208\n"
    "v_rcp_f32 v209, v209\n"
    "s_add_u32 m0, s69, 2048\n"
    "s_add_u32 s78, s62, 73728\n"
    "buffer_load_dwordx4 v11, s[16:19], s78 offen lds\n"
    "s_waitcnt lgkmcnt(2)\n"
    "v_mfma_f32_32x32x16_bf16 v[96:111], v[200:203], v[56:59], v[96:111]\n"
    "v_add_u32 v236, 49152, v230\n"
    "ds_read_b64_tr_b16 v[196:197], v236 offset:0\n"
    "ds_read_b64_tr_b16 v[198:199], v236 offset:3072\n"
    "v_fma_f32 v210, v208, v186, v186\n"
    "v_fma_f32 v211, v209, v187, v187\n"
    "v_cvt_pk_bf16_f32 v37, v210, v211\n"
    "v_mul_f32 v208, s70, v188\n"
    "s_waitcnt lgkmcnt(2)\n"
    "v_mfma_f32_32x32x16_bf16 v[112:127], v[192:195], v[56:59], v[112:127]\n"
    "v_add_u32 v237, 49152, v231\n"
    "ds_read_b64_tr_b16 v[200:201], v237 offset:0\n"
    "ds_read_b64_tr_b16 v[202:203], v237 offset:3072\n"
    "v_mul_f32 v209, s70, v189\n"
    "v_exp_f32 v210, v208\n"
    "v_exp_f32 v211, v209\n"
    "s_add_u32 s78, s65, 0\n"
    "buffer_load_dwordx4 v[16:19], v2, s[20:23], s78 offen sc1\n"
    "s_waitcnt lgkmcnt(2)\n"
    "v_mfma_f32_32x32x16_bf16 v[128:143], v[196:199], v[56:59], v[128:143]\n"
    "ds_read_b64_tr_b16 v[192:193], v232 offset:6144\n"
    "ds_read_b64_tr_b16 v[194:195], v232 offset:9216\n"
    "v_fma_f32 v208, v210, -0.5, -1.0\n"
    "v_fma_f32 v209, v211, -0.5, -1.0\n"
    "v_fma_f32 v208, v210, v208, -1.0\n"
    "s_add_u32 s78, s65, 1024\n"
    "buffer_load_dwordx4 v[20:23], v2, s[20:23], s78 offen sc1\n"
    "s_waitcnt lgkmcnt(2)\n"
    "v_mfma_f32_32x32x16_bf16 v[144:159], v[200:203], v[56:59], v[144:159]\n"
    "ds_read_b64_tr_b16 v[196:197], v233 offset:6144\n"
    "ds_read_b64_tr_b16 v[198:199], v233 offset:9216\n"
    "v_fma_f32 v209, v211, v209, -1.0\n"
    "v_rcp_f32 v208, v208\n"
    "v_rcp_f32 v209, v209\n"
    "s_waitcnt lgkmcnt(2)\n"
    "v_mfma_f32_32x32x16_bf16 v[64:79], v[192:195], v[60:63], v[64:79]\n"
    "ds_read_b64_tr_b16 v[200:201], v234 offset:6144\n"
    "ds_read_b64_tr_b16 v[202:203], v234 offset:9216\n"
    "v_fma_f32 v210, v208, v188, v188\n"
    "v_fma_f32 v211, v209, v189, v189\n"
    "v_cvt_pk_bf16_f32 v38, v210, v211\n"
    "v_mul_f32 v208, s70, v190\n"
    "s_add_u32 s78, s65, 16384\n"
    "buffer_load_dwordx4 v[24:27], v2, s[20:23], s78 offen sc1\n"
    "s_waitcnt lgkmcnt(2)\n"
    "v_mfma_f32_32x32x16_bf16 v[80:95], v[196:199], v[60:63], v[80:95]\n"
    "ds_read_b64_tr_b16 v[192:193], v235 offset:6144\n"
    "ds_read_b64_tr_b16 v[194:195], v235 offset:9216\n"
    "v_mul_f32 v209, s70, v191\n"
    "v_exp_f32 v210, v208\n"
    "s_waitcnt lgkmcnt(2)\n"
    "v_mfma_f32_32x32x16_bf16 v[96:111], v[200:203], v[60:63], v[96:111]\n"
    "ds_read_b64_tr_b16 v[196:197], v236 offset:6144\n"
    "ds_read_b64_tr_b16 v[198:199], v236 offset:9216\n"
    "v_exp_f32 v211, v209\n"
    "v_fma_f32 v208, v210, -0.5, -1.0\n"
    "v_fma_f32 v209, v211, -0.5, -1.0\n"
    "v_fma_f32 v208, v210, v208, -1.0\n"
    "s_add_u32 s78, s65, 17408\n"
    "buffer_load_dwordx4 v[28:31], v2, s[20:23], s78 offen sc1\n"
    "s_waitcnt lgkmcnt(2)\n"
    "v_mfma_f32_32x32x16_bf16 v[112:127], v[192:195], v[60:63], v[112:127]\n"
    "ds_read_b64_tr_b16 v[200:201], v237 offset:6144\n"
    "ds_read_b64_tr_b16 v[202:203], v237 offset:9216\n"
    "v_fma_f32 v209, v211, v209, -1.0\n"
    "v_rcp_f32 v208, v208\n"
    "s_waitcnt lgkmcnt(2)\n"
    "v_mfma_f32_32x32x16_bf16 v[128:143], v[196:199], v[60:63], v[128:143]\n"
    "s_barrier\n"
    "v_add_u32 v232, 73728, v226\n"
    "ds_read_b64_tr_b16 v[192:193], v232 offset:0\n"
    "ds_read_b64_tr_b16 v[194:195], v232 offset:3072\n"
    "v_rcp_f32 v209, v209\n"
    "v_fma_f32 v210, v208, v190, v190\n"
    "v_fma_f32 v211, v209, v191, v191\n"
    "v_cvt_pk_bf16_f32 v39, v210, v211\n"
    "s_add_u32 m0, s69, 24576\n"
    "s_add_u32 s78, s62, 147456\n"
    "buffer_load_dwordx4 v9, s[16:19], s78 offen lds\n"
    "s_waitcnt lgkmcnt(2)\n"
    "v_mfma_f32_32x32x16_bf16 v[144:159], v[200:203], v[60:63], v[144:159]\n"
    "v_add_u32 v233, 73728, v227\n"
    "ds_read_b64_tr_b16 v[196:197], v233 offset:0\n"
    "ds_read_b64_tr_b16 v[198:199], v233 offset:3072\n"
    "s_add_u32 s78, s64, 66560\n"
    "buffer_store_dwordx4 v[36:39], v2, s[20:23], s78 offen sc1\n"
    "s_waitcnt lgkmcnt(2)\n"
    "v_mfma_f32_32x32x16_bf16 v[64:79], v[192:195], v[40:43], v[64:79]\n"
    "v_add_u32 v234, 73728, v228\n"
    "ds_read_b64_tr_b16 v[200:201], v234 offset:0\n"
    "ds_read_b64_tr_b16 v[202:203], v234 offset:3072\n"
    "s_add_u32 m0, s69, 25600\n"
    "s_add_u32 s78, s62, 147456\n"
    "buffer_load_dwordx4 v10, s[16:19], s78 offen lds\n"
    "s_waitcnt lgkmcnt(2)\n"
    "v_mfma_f32_32x32x16_bf16 v[80:95], v[196:199], v[40:43], v[80:95]\n"
    "v_add_u32 v235, 73728, v229\n"
    "ds_read_b64_tr_b16 v[192:193], v235 offset:0\n"
    "ds_read_b64_tr_b16 v[194:195], v235 offset:3072\n"
    "s_waitcnt lgkmcnt(2)\n"
    "v_mfma_f32_32x32x16_bf16 v[96:111], v[200:203], v[40:43], v[96:111]\n"
    "v_add_u32 v236, 73728, v230\n"
    "ds_read_b64_tr_b16 v[196:197], v236 offset:0\n"
    "ds_read_b64_tr_b16 v[198:199], v236 offset:3072\n"
    "v_mul_f32 v208, s70, v238\n"
    "v_mul_f32 v209, s70, v239\n"
    "v_exp_f32 v210, v208\n"
    "s_add_u32 m0, s69, 26624\n"
    "s_add_u32 s78, s62, 147456\n"
    "buffer_load_dwordx4 v11, s[16:19], s78 offen lds\n"
    "s_waitcnt lgkmcnt(2)\n"
    "v_mfma_f32_32x32x16_bf16 v[112:127], v[192:195], v[40:43], v[112:127]\n"
    "v_add_u32 v237, 73728, v231\n"
    "ds_read_b64_tr_b16 v[200:201], v237 offset:0\n"
    "ds_read_b64_tr_b16 v[202:203], v237 offset:3072\n"
    "v_exp_f32 v211, v209\n"
    "v_fma_f32 v208, v210, -0.5, -1.0\n"
    "v_fma_f32 v209, v211, -0.5, -1.0\n"
    "s_waitcnt lgkmcnt(2)\n"
    "v_mfma_f32_32x32x16_bf16 v[128:143], v[196:199], v[40:43], v[128:143]\n"
    "ds_read_b64_tr_b16 v[192:193], v232 offset:6144\n"
    "ds_read_b64_tr_b16 v[194:195], v232 offset:9216\n"
    "v_fma_f32 v208, v210, v208, -1.0\n"
    "v_fma_f32 v209, v211, v209, -1.0\n"
    "v_rcp_f32 v208, v208\n"
    "s_waitcnt lgkmcnt(2)\n"
    "v_mfma_f32_32x32x16_bf16 v[144:159], v[200:203], v[40:43], v[144:159]\n"
    "ds_read_b64_tr_b16 v[196:197], v233 offset:6144\n"
    "ds_read_b64_tr_b16 v[198:199], v233 offset:9216\n"
    "v_rcp_f32 v209, v209\n"
    "v_fma_f32 v210, v208, v238, v238\n"
    "v_fma_f32 v211, v209, v239, v239\n"
    "s_add_u32 s78, s65, 32768\n"
    "buffer_load_dwordx4 v[48:51], v2, s[20:23], s78 offen sc1\n"
    "s_waitcnt lgkmcnt(2)\n"
    "v_mfma_f32_32x32x16_bf16 v[64:79], v[192:195], v[44:47], v[64:79]\n"
    "ds_read_b64_tr_b16 v[200:201], v234 offset:6144\n"
    "ds_read_b64_tr_b16 v[202:203], v234 offset:9216\n"
    "v_cvt_pk_bf16_f32 v56, v210, v211\n"
    "v_mul_f32 v208, s70, v240\n"
    "v_mul_f32 v209, s70, v241\n"
    "v_exp_f32 v210, v208\n"
    "s_waitcnt lgkmcnt(2)\n"
    "v_mfma_f32_32x32x16_bf16 v[80:95], v[196:199], v[44:47], v[80:95]\n"
    "ds_read_b64_tr_b16 v[192:193], v235 offset:6144\n"
    "ds_read_b64_tr_b16 v[194:195], v235 offset:9216\n"
    "v_exp_f32 v211, v209\n"
    "v_fma_f32 v208, v210, -0.5, -1.0\n"
    "v_fma_f32 v209, v211, -0.5, -1.0\n"
    "s_add_u32 s78, s65, 33792\n"
    "buffer_load_dwordx4 v[52:55], v2, s[20:23], s78 offen sc1\n"
    "s_waitcnt lgkmcnt(2)\n"
    "v_mfma_f32_32x32x16_bf16 v[96:111], v[200:203], v[44:47], v[96:111]\n"
    "ds_read_b64_tr_b16 v[196:197], v236 offset:6144\n"
    "ds_read_b64_tr_b16 v[198:199], v236 offset:9216\n"
    "v_fma_f32 v208, v210, v208, -1.0\n"
    "v_fma_f32 v209, v211, v209, -1.0\n"
    "v_rcp_f32 v208, v208\n"
    "s_waitcnt lgkmcnt(2)\n"
    "v_mfma_f32_32x32x16_bf16 v[112:127], v[192:195], v[44:47], v[112:127]\n"
    "ds_read_b64_tr_b16 v[200:201], v237 offset:6144\n"
    "ds_read_b64_tr_b16 v[202:203], v237 offset:9216\n"
    "v_rcp_f32 v209, v209\n"
    "v_fma_f32 v210, v208, v240, v240\n"
    "v_fma_f32 v211, v209, v241, v241\n"
    "s_waitcnt lgkmcnt(2)\n"
    "v_mfma_f32_32x32x16_bf16 v[128:143], v[196:199], v[44:47], v[128:143]\n"
    "s_waitcnt vmcnt(17)\n"
    "s_barrier\n"
    "v_add_u32 v232, 98304, v226\n"
    "ds_read_b64_tr_b16 v[192:193], v232 offset:0\n"
    "ds_read_b64_tr_b16 v[194:195], v232 offset:3072\n"
    "v_cvt_pk_bf16_f32 v57, v210, v211\n"
    "v_mul_f32 v208, s70, v242\n"
    "v_mul_f32 v209, s70, v243\n"
    "v_exp_f32 v210, v208\n"
    "s_add_u32 m0, s69, 49152\n"
    "s_add_u32 s78, s61, 368640\n"
    "buffer_load_dwordx4 v9, s[16:19], s78 offen lds\n"
    "s_waitcnt lgkmcnt(2)\n"
    "v_mfma_f32_32x32x16_bf16 v[144:159], v[200:203], v[44:47], v[144:159]\n"
    "v_add_u32 v233, 98304, v227\n"
    "ds_read_b64_tr_b16 v[196:197], v233 offset:0\n"
    "ds_read_b64_tr_b16 v[198:199], v233 offset:3072\n"
    "v_exp_f32 v211, v209\n"
    "v_fma_f32 v208, v210, -0.5, -1.0\n"
    "s_waitcnt lgkmcnt(2)\n"
    "v_mfma_f32_32x32x16_bf16 v[64:79], v[192:195], v[32:35], v[64:79]\n"
    "v_add_u32 v234, 98304, v228\n"
    "ds_read_b64_tr_b16 v[200:201], v234 offset:0\n"
    "ds_read_b64_tr_b16 v[202:203], v234 offset:3072\n"
    "v_fma_f32 v209, v211, -0.5, -1.0\n"
    "v_fma_f32 v208, v210, v208, -1.0\n"
    "v_fma_f32 v209, v211, v209, -1.0\n"
    "v_rcp_f32 v208, v208\n"
    "s_waitcnt lgkmcnt(2)\n"
    "v_mfma_f32_32x32x16_bf16 v[80:95], v[196:199], v[32:35], v[80:95]\n"
    "v_add_u32 v235, 98304, v229\n"
    "ds_read_b64_tr_b16 v[192:193], v235 offset:0\n"
    "ds_read_b64_tr_b16 v[194:195], v235 offset:3072\n"
    "v_rcp_f32 v209, v209\n"
    "v_fma_f32 v210, v208, v242, v242\n"
    "s_waitcnt lgkmcnt(2)\n"
    "v_mfma_f32_32x32x16_bf16 v[96:111], v[200:203], v[32:35], v[96:111]\n"
    "v_add_u32 v236, 98304, v230\n"
    "ds_read_b64_tr_b16 v[196:197], v236 offset:0\n"
    "ds_read_b64_tr_b16 v[198:199], v236 offset:3072\n"
    "v_fma_f32 v211, v209, v243, v243\n"
    "v_cvt_pk_bf16_f32 v58, v210, v211\n"
    "v_mul_f32 v208, s70, v244\n"
    "v_mul_f32 v209, s70, v245\n"
    "s_add_u32 m0, s69, 50176\n"
    "s_add_u32 s78, s61, 368640\n"
    "buffer_load_dwordx4 v10, s[16:19], s78 offen lds\n"
    "s_waitcnt lgkmcnt(2)\n"
    "v_mfma_f32_32x32x16_bf16 v[112:127], v[192:195], v[32:35], v[112:127]\n"
    "v_add_u32 v237, 98304, v231\n"
    "ds_read_b64_tr_b16 v[200:201], v237 offset:0\n"
    "ds_read_b64_tr_b16 v[202:203], v237 offset:3072\n"
    "v_exp_f32 v210, v208\n"
    "v_exp_f32 v211, v209\n"
    "v_fma_f32 v208, v210, -0.5, -1.0\n"
    "s_waitcnt lgkmcnt(2)\n"
    "v_mfma_f32_32x32x16_bf16 v[128:143], v[196:199], v[32:35], v[128:143]\n"
    "ds_read_b64_tr_b16 v[192:193], v232 offset:6144\n"
    "ds_read_b64_tr_b16 v[194:195], v232 offset:9216\n"
    "v_fma_f32 v209, v211, -0.5, -1.0\n"
    "v_fma_f32 v208, v210, v208, -1.0\n"
    "v_fma_f32 v209, v211, v209, -1.0\n"
    "s_waitcnt lgkmcnt(2)\n"
    "v_mfma_f32_32x32x16_bf16 v[144:159], v[200:203], v[32:35], v[144:159]\n"
    "ds_read_b64_tr_b16 v[196:197], v233 offset:6144\n"
    "ds_read_b64_tr_b16 v[198:199], v233 offset:9216\n"
    "v_rcp_f32 v208, v208\n"
    "v_rcp_f32 v209, v209\n"
    "v_fma_f32 v210, v208, v244, v244\n"
    "s_waitcnt lgkmcnt(2)\n"
    "v_mfma_f32_32x32x16_bf16 v[64:79], v[192:195], v[36:39], v[64:79]\n"
    "ds_read_b64_tr_b16 v[200:201], v234 offset:6144\n"
    "ds_read_b64_tr_b16 v[202:203], v234 offset:9216\n"
    "v_fma_f32 v211, v209, v245, v245\n"
    "v_cvt_pk_bf16_f32 v59, v210, v211\n"
    "v_mul_f32 v208, s70, v246\n"
    "v_mul_f32 v209, s70, v247\n"
    "s_add_u32 m0, s69, 51200\n"
    "s_add_u32 s78, s61, 368640\n"
    "buffer_load_dwordx4 v11, s[16:19], s78 offen lds\n"
    "s_waitcnt lgkmcnt(2)\n"
    "v_mfma_f32_32x32x16_bf16 v[80:95], v[196:199], v[36:39], v[80:95]\n"
    "ds_read_b64_tr_b16 v[192:193], v235 offset:6144\n"
    "ds_read_b64_tr_b16 v[194:195], v235 offset:9216\n"
    "v_exp_f32 v210, v208\n"
    "v_exp_f32 v211, v209\n"
    "s_add_u32 s78, s64, 81920\n"
    "buffer_store_dwordx4 v[56:59], v2, s[20:23], s78 offen sc1\n"
    "s_waitcnt lgkmcnt(2)\n"
    "v_mfma_f32_32x32x16_bf16 v[96:111], v[200:203], v[36:39], v[96:111]\n"
    "ds_read_b64_tr_b16 v[196:197], v236 offset:6144\n"
    "ds_read_b64_tr_b16 v[198:199], v236 offset:9216\n"
    "v_fma_f32 v208, v210, -0.5, -1.0\n"
    "v_fma_f32 v209, v211, -0.5, -1.0\n"
    "v_fma_f32 v208, v210, v208, -1.0\n"
    "v_fma_f32 v209, v211, v209, -1.0\n"
    "s_waitcnt lgkmcnt(2)\n"
    "v_mfma_f32_32x32x16_bf16 v[112:127], v[192:195], v[36:39], v[112:127]\n"
    "ds_read_b64_tr_b16 v[200:201], v237 offset:6144\n"
    "ds_read_b64_tr_b16 v[202:203], v237 offset:9216\n"
    "v_rcp_f32 v208, v208\n"
    "v_rcp_f32 v209, v209\n"
    "s_waitcnt lgkmcnt(2)\n"
    "v_mfma_f32_32x32x16_bf16 v[128:143], v[196:199], v[36:39], v[128:143]\n"
    "s_waitcnt vmcnt(8)\n"
    "s_barrier\n"
    "s_add_u32 s78, s59, 32\n"
    "s_mov_b64 exec, 1\n"
    "buffer_store_dwordx2 v[4:5], off, s[20:23], s78 sc1\n"
    "s_mov_b64 exec, -1\n"
    "v_add_u32 v232, 122880, v226\n"
    "ds_read_b64_tr_b16 v[192:193], v232 offset:0\n"
    "ds_read_b64_tr_b16 v[194:195], v232 offset:3072\n"
    "v_fma_f32 v210, v208, v246, v246\n"
    "v_fma_f32 v211, v209, v247, v247\n"
    "v_cvt_pk_bf16_f32 v60, v210, v211\n"
    "v_mul_f32 v208, s70, v248\n"
    "v_mul_f32 v209, s70, v249\n"
    "s_add_u32 m0, s69, 73728\n"
    "s_nop 0\n"
    "buffer_load_dwordx4 v9, s[16:19], s63 offen lds\n"
    "s_waitcnt lgkmcnt(2)\n"
    "v_mfma_f32_32x32x16_bf16 v[144:159], v[200:203], v[36:39], v[144:159]\n"
    "v_add_u32 v233, 122880, v227\n"
    "ds_read_b64_tr_b16 v[196:197], v233 offset:0\n"
    "ds_read_b64_tr_b16 v[198:199], v233 offset:3072\n"
    "v_exp_f32 v210, v208\n"
    "v_exp_f32 v211, v209\n"
    "s_waitcnt lgkmcnt(2)\n"
    "v_mfma_f32_32x32x16_bf16 v[64:79], v[192:195], v[16:19], v[64:79]\n"
    "v_add_u32 v234, 122880, v228\n"
    "ds_read_b64_tr_b16 v[200:201], v234 offset:0\n"
    "ds_read_b64_tr_b16 v[202:203], v234 offset:3072\n"
    "v_fma_f32 v208, v210, -0.5, -1.0\n"
    "v_fma_f32 v209, v211, -0.5, -1.0\n"
    "v_fma_f32 v208, v210, v208, -1.0\n"
    "v_fma_f32 v209, v211, v209, -1.0\n"
    "s_add_u32 m0, s69, 74752\n"
    "s_nop 0\n"
    "buffer_load_dwordx4 v10, s[16:19], s63 offen lds\n"
    "s_waitcnt lgkmcnt(2)\n"
    "v_mfma_f32_32x32x16_bf16 v[80:95], v[196:199], v[16:19], v[80:95]\n"
    "v_add_u32 v235, 122880, v229\n"
    "ds_read_b64_tr_b16 v[192:193], v235 offset:0\n"
    "ds_read_b64_tr_b16 v[194:195], v235 offset:3072\n"
    "v_rcp_f32 v208, v208\n"
    "v_rcp_f32 v209, v209\n"
    "s_waitcnt lgkmcnt(2)\n"
    "v_mfma_f32_32x32x16_bf16 v[96:111], v[200:203], v[16:19], v[96:111]\n"
    "v_add_u32 v236, 122880, v230\n"
    "ds_read_b64_tr_b16 v[196:197], v236 offset:0\n"
    "ds_read_b64_tr_b16 v[198:199], v236 offset:3072\n"
    "v_fma_f32 v210, v208, v248, v248\n"
    "v_fma_f32 v211, v209, v249, v249\n"
    "v_cvt_pk_bf16_f32 v61, v210, v211\n"
    "v_mul_f32 v208, s70, v250\n"
    "s_add_u32 m0, s69, 75776\n"
    "s_nop 0\n"
    "buffer_load_dwordx4 v11, s[16:19], s63 offen lds\n"
    "s_waitcnt lgkmcnt(2)\n"
    "v_mfma_f32_32x32x16_bf16 v[112:127], v[192:195], v[16:19], v[112:127]\n"
    "v_add_u32 v237, 122880, v231\n"
    "ds_read_b64_tr_b16 v[200:201], v237 offset:0\n"
    "ds_read_b64_tr_b16 v[202:203], v237 offset:3072\n"
    "v_mul_f32 v209, s70, v251\n"
    "v_exp_f32 v210, v208\n"
    "v_exp_f32 v211, v209\n"
    "s_waitcnt lgkmcnt(2)\n"
    "v_mfma_f32_32x32x16_bf16 v[128:143], v[196:199], v[16:19], v[128:143]\n"
    "ds_read_b64_tr_b16 v[192:193], v232 offset:6144\n"
    "ds_read_b64_tr_b16 v[194:195], v232 offset:9216\n"
    "v_fma_f32 v208, v210, -0.5, -1.0\n"
    "v_fma_f32 v209, v211, -0.5, -1.0\n"
    "v_fma_f32 v208, v210, v208, -1.0\n"
    "s_waitcnt lgkmcnt(2)\n"
    "v_mfma_f32_32x32x16_bf16 v[144:159], v[200:203], v[16:19], v[144:159]\n"
    "ds_read_b64_tr_b16 v[196:197], v233 offset:6144\n"
    "ds_read_b64_tr_b16 v[198:199], v233 offset:9216\n"
    "v_fma_f32 v209, v211, v209, -1.0\n"
    "v_rcp_f32 v208, v208\n"
    "v_rcp_f32 v209, v209\n"
    "s_add_u32 s78, s66, 0\n"
    "buffer_load_dwordx4 v[40:43], v2, s[20:23], s78 offen sc1\n"
    "s_waitcnt lgkmcnt(2)\n"
    "v_mfma_f32_32x32x16_bf16 v[64:79], v[192:195], v[20:23], v[64:79]\n"
    "ds_read_b64_tr_b16 v[200:201], v234 offset:6144\n"
    "ds_read_b64_tr_b16 v[202:203], v234 offset:9216\n"
    "v_fma_f32 v210, v208, v250, v250\n"
    "v_fma_f32 v211, v209, v251, v251\n"
    "v_cvt_pk_bf16_f32 v62, v210, v211\n"
    "v_mul_f32 v208, s70, v252\n"
    "s_waitcnt lgkmcnt(2)\n"
    "v_mfma_f32_32x32x16_bf16 v[80:95], v[196:199], v[20:23], v[80:95]\n"
    "ds_read_b64_tr_b16 v[192:193], v235 offset:6144\n"
    "ds_read_b64_tr_b16 v[194:195], v235 offset:9216\n"
    "v_mul_f32 v209, s70, v253\n"
    "v_exp_f32 v210, v208\n"
    "s_add_u32 s78, s66, 1024\n"
    "buffer_load_dwordx4 v[44:47], v2, s[20:23], s78 offen sc1\n"
    "s_waitcnt lgkmcnt(2)\n"
    "v_mfma_f32_32x32x16_bf16 v[96:111], v[200:203], v[20:23], v[96:111]\n"
    "ds_read_b64_tr_b16 v[196:197], v236 offset:6144\n"
    "ds_read_b64_tr_b16 v[198:199], v236 offset:9216\n"
    "v_exp_f32 v211, v209\n"
    "v_fma_f32 v208, v210, -0.5, -1.0\n"
    "v_fma_f32 v209, v211, -0.5, -1.0\n"
    "v_fma_f32 v208, v210, v208, -1.0\n"
    "s_waitcnt lgkmcnt(2)\n"
    "v_mfma_f32_32x32x16_bf16 v[112:127], v[192:195], v[20:23], v[112:127]\n"
    "ds_read_b64_tr_b16 v[200:201], v237 offset:6144\n"
    "ds_read_b64_tr_b16 v[202:203], v237 offset:9216\n"
    "v_fma_f32 v209, v211, v209, -1.0\n"
    "v_rcp_f32 v208, v208\n"
    "s_waitcnt lgkmcnt(2)\n"
    "v_mfma_f32_32x32x16_bf16 v[128:143], v[196:199], v[20:23], v[128:143]\n"
    "s_barrier\n"
    "s_add_u32 s78, s60, 32\n"
    "buffer_load_dwordx2 v[6:7], v3, s[20:23], s78 offen sc1\n"
    "v_add_u32 v232, 0, v226\n"
    "ds_read_b64_tr_b16 v[192:193], v232 offset:0\n"
    "ds_read_b64_tr_b16 v[194:195], v232 offset:3072\n"
    "v_rcp_f32 v209, v209\n"
    "v_fma_f32 v210, v208, v252, v252\n"
    "v_fma_f32 v211, v209, v253, v253\n"
    "v_cvt_pk_bf16_f32 v63, v210, v211\n"
    "s_add_u32 m0, s69, 98304\n"
    "s_add_u32 s78, s63, 73728\n"
    "buffer_load_dwordx4 v9, s[16:19], s78 offen lds\n"
    "s_waitcnt lgkmcnt(2)\n"
    "v_mfma_f32_32x32x16_bf16 v[144:159], v[200:203], v[20:23], v[144:159]\n"
    "v_add_u32 v233, 0, v227\n"
    "ds_read_b64_tr_b16 v[196:197], v233 offset:0\n"
    "ds_read_b64_tr_b16 v[198:199], v233 offset:3072\n"
    "s_add_u32 s78, s64, 82944\n"
    "buffer_store_dwordx4 v[60:63], v2, s[20:23], s78 offen sc1\n"
    "s_waitcnt lgkmcnt(2)\n"
    "v_mfma_f32_32x32x16_bf16 v[64:79], v[192:195], v[24:27], v[64:79]\n"
    "v_add_u32 v234, 0, v228\n"
    "ds_read_b64_tr_b16 v[200:201], v234 offset:0\n"
    "ds_read_b64_tr_b16 v[202:203], v234 offset:3072\n"
    "s_add_u32 m0, s69, 99328\n"
    "s_add_u32 s78, s63, 73728\n"
    "buffer_load_dwordx4 v10, s[16:19], s78 offen lds\n"
    "s_waitcnt lgkmcnt(2)\n"
    "v_mfma_f32_32x32x16_bf16 v[80:95], v[196:199], v[24:27], v[80:95]\n"
    "v_add_u32 v235, 0, v229\n"
    "ds_read_b64_tr_b16 v[192:193], v235 offset:0\n"
    "ds_read_b64_tr_b16 v[194:195], v235 offset:3072\n"
    "s_waitcnt lgkmcnt(2)\n"
    "v_mfma_f32_32x32x16_bf16 v[96:111], v[200:203], v[24:27], v[96:111]\n"
    "v_add_u32 v236, 0, v230\n"
    "ds_read_b64_tr_b16 v[196:197], v236 offset:0\n"
    "ds_read_b64_tr_b16 v[198:199], v236 offset:3072\n"
    "s_add_u32 m0, s69, 100352\n"
    "s_add_u32 s78, s63, 73728\n"
    "buffer_load_dwordx4 v11, s[16:19], s78 offen lds\n"
    "s_waitcnt lgkmcnt(2)\n"
    "v_mfma_f32_32x32x16_bf16 v[112:127], v[192:195], v[24:27], v[112:127]\n"
    "v_add_u32 v237, 0, v231\n"
    "ds_read_b64_tr_b16 v[200:201], v237 offset:0\n"
    "ds_read_b64_tr_b16 v[202:203], v237 offset:3072\n"
    "s_waitcnt lgkmcnt(2)\n"
    "v_mfma_f32_32x32x16_bf16 v[128:143], v[196:199], v[24:27], v[128:143]\n"
    "ds_read_b64_tr_b16 v[192:193], v232 offset:6144\n"
    "ds_read_b64_tr_b16 v[194:195], v232 offset:9216\n"
    "s_waitcnt lgkmcnt(2)\n"
    "v_mfma_f32_32x32x16_bf16 v[144:159], v[200:203], v[24:27], v[144:159]\n"
    "ds_read_b64_tr_b16 v[196:197], v233 offset:6144\n"
    "ds_read_b64_tr_b16 v[198:199], v233 offset:9216\n"
    "s_add_u32 s78, s66, 16384\n"
    "buffer_load_dwordx4 v[32:35], v2, s[20:23], s78 offen sc1\n"
    "s_waitcnt lgkmcnt(2)\n"
    "v_mfma_f32_32x32x16_bf16 v[64:79], v[192:195], v[28:31], v[64:79]\n"
    "ds_read_b64_tr_b16 v[200:201], v234 offset:6144\n"
    "ds_read_b64_tr_b16 v[202:203], v234 offset:9216\n"
    "s_waitcnt lgkmcnt(2)\n"
    "v_mfma_f32_32x32x16_bf16 v[80:95], v[196:199], v[28:31], v[80:95]\n"
    "ds_read_b64_tr_b16 v[192:193], v235 offset:6144\n"
    "ds_read_b64_tr_b16 v[194:195], v235 offset:9216\n"
    "s_add_u32 s78, s66, 17408\n"
    "buffer_load_dwordx4 v[36:39], v2, s[20:23], s78 offen sc1\n"
    "s_waitcnt lgkmcnt(2)\n"
    "v_mfma_f32_32x32x16_bf16 v[96:111], v[200:203], v[28:31], v[96:111]\n"
    "ds_read_b64_tr_b16 v[196:197], v236 offset:6144\n"
    "ds_read_b64_tr_b16 v[198:199], v236 offset:9216\n"
    "s_waitcnt lgkmcnt(2)\n"
    "v_mfma_f32_32x32x16_bf16 v[112:127], v[192:195], v[28:31], v[112:127]\n"
    "ds_read_b64_tr_b16 v[200:201], v237 offset:6144\n"
    "ds_read_b64_tr_b16 v[202:203], v237 offset:9216\n"
    "s_waitcnt lgkmcnt(2)\n"
    "v_mfma_f32_32x32x16_bf16 v[128:143], v[196:199], v[28:31], v[128:143]\n"
    "s_waitcnt vmcnt(17)\n"
    "s_barrier\n"
    "v_add_u32 v232, 24576, v226\n"
    "ds_read_b64_tr_b16 v[192:193], v232 offset:0\n"
    "ds_read_b64_tr_b16 v[194:195], v232 offset:3072\n"
    "s_add_u32 m0, s69, 122880\n"
    "s_add_u32 s78, s63, 147456\n"
    "buffer_load_dwordx4 v9, s[16:19], s78 offen lds\n"
    "s_waitcnt lgkmcnt(2)\n"
    "v_mfma_f32_32x32x16_bf16 v[144:159], v[200:203], v[28:31], v[144:159]\n"
    "v_add_u32 v233, 24576, v227\n"
    "ds_read_b64_tr_b16 v[196:197], v233 offset:0\n"
    "ds_read_b64_tr_b16 v[198:199], v233 offset:3072\n"
    "s_waitcnt lgkmcnt(2)\n"
    "v_mfma_f32_32x32x16_bf16 v[64:79], v[192:195], v[48:51], v[64:79]\n"
    "v_add_u32 v234, 24576, v228\n"
    "ds_read_b64_tr_b16 v[200:201], v234 offset:0\n"
    "ds_read_b64_tr_b16 v[202:203], v234 offset:3072\n"
    "s_add_u32 m0, s69, 123904\n"
    "s_add_u32 s78, s63, 147456\n"
    "buffer_load_dwordx4 v10, s[16:19], s78 offen lds\n"
    "s_waitcnt lgkmcnt(2)\n"
    "v_mfma_f32_32x32x16_bf16 v[80:95], v[196:199], v[48:51], v[80:95]\n"
    "v_add_u32 v235, 24576, v229\n"
    "ds_read_b64_tr_b16 v[192:193], v235 offset:0\n"
    "ds_read_b64_tr_b16 v[194:195], v235 offset:3072\n"
    "s_waitcnt lgkmcnt(2)\n"
    "v_mfma_f32_32x32x16_bf16 v[96:111], v[200:203], v[48:51], v[96:111]\n"
    "v_add_u32 v236, 24576, v230\n"
    "ds_read_b64_tr_b16 v[196:197], v236 offset:0\n"
    "ds_read_b64_tr_b16 v[198:199], v236 offset:3072\n"
    "s_add_u32 m0, s69, 124928\n"
    "s_add_u32 s78, s63, 147456\n"
    "buffer_load_dwordx4 v11, s[16:19], s78 offen lds\n"
    "s_waitcnt lgkmcnt(2)\n"
    "v_mfma_f32_32x32x16_bf16 v[112:127], v[192:195], v[48:51], v[112:127]\n"
    "v_add_u32 v237, 24576, v231\n"
    "ds_read_b64_tr_b16 v[200:201], v237 offset:0\n"
    "ds_read_b64_tr_b16 v[202:203], v237 offset:3072\n"
    "s_waitcnt lgkmcnt(2)\n"
    "v_mfma_f32_32x32x16_bf16 v[128:143], v[196:199], v[48:51], v[128:143]\n"
    "ds_read_b64_tr_b16 v[192:193], v232 offset:6144\n"
    "ds_read_b64_tr_b16 v[194:195], v232 offset:9216\n"
    "s_waitcnt lgkmcnt(2)\n"
    "v_mfma_f32_32x32x16_bf16 v[144:159], v[200:203], v[48:51], v[144:159]\n"
    "ds_read_b64_tr_b16 v[196:197], v233 offset:6144\n"
    "ds_read_b64_tr_b16 v[198:199], v233 offset:9216\n"
    "s_add_u32 s78, s66, 32768\n"
    "buffer_load_dwordx4 v[16:19], v2, s[20:23], s78 offen sc1\n"
    "s_waitcnt lgkmcnt(2)\n"
    "v_mfma_f32_32x32x16_bf16 v[64:79], v[192:195], v[52:55], v[64:79]\n"
    "ds_read_b64_tr_b16 v[200:201], v234 offset:6144\n"
    "ds_read_b64_tr_b16 v[202:203], v234 offset:9216\n"
    "s_waitcnt lgkmcnt(2)\n"
    "v_mfma_f32_32x32x16_bf16 v[80:95], v[196:199], v[52:55], v[80:95]\n"
    "ds_read_b64_tr_b16 v[192:193], v235 offset:6144\n"
    "ds_read_b64_tr_b16 v[194:195], v235 offset:9216\n"
    "s_add_u32 s78, s66, 33792\n"
    "buffer_load_dwordx4 v[20:23], v2, s[20:23], s78 offen sc1\n"
    "s_waitcnt lgkmcnt(2)\n"
    "v_mfma_f32_32x32x16_bf16 v[96:111], v[200:203], v[52:55], v[96:111]\n"
    "ds_read_b64_tr_b16 v[196:197], v236 offset:6144\n"
    "ds_read_b64_tr_b16 v[198:199], v236 offset:9216\n"
    "s_waitcnt lgkmcnt(2)\n"
    "v_mfma_f32_32x32x16_bf16 v[112:127], v[192:195], v[52:55], v[112:127]\n"
    "ds_read_b64_tr_b16 v[200:201], v237 offset:6144\n"
    "ds_read_b64_tr_b16 v[202:203], v237 offset:9216\n"
    "s_waitcnt lgkmcnt(2)\n"
    "v_mfma_f32_32x32x16_bf16 v[128:143], v[196:199], v[52:55], v[128:143]\n"
    "s_waitcnt vmcnt(9)\n"
    "s_barrier\n"
    "v_cmp_ne_u64 vcc, v[6:7], v[4:5]\n"
    "s_nop 0\n"
    "s_and_b64 vcc, vcc, s[88:89]\n"
    "s_cbranch_vccz Lok_11\n"
    "s_mov_b32 s86, 0x40000\n"
    "s_add_u32 s78, s60, 32\n"
    "Lspin_10:\n"
    "s_sleep 1\n"
    "buffer_load_dwordx2 v[6:7], v3, s[20:23], s78 offen sc1\n"
    "s_waitcnt vmcnt(0)\n"
    "s_sub_u32 s86, s86, 1\n"
    "s_cbranch_scc1 Lok_11\n"
    "v_cmp_ne_u64 vcc, v[6:7], v[4:5]\n"
    "s_nop 1\n"
    "s_and_b64 vcc, vcc, s[88:89]\n"
    "s_cbranch_vccnz Lspin_10\n"
    "Lok_11:\n"
    "s_mov_b64 vcc, s[90:91]\n"
    "s_add_u32 s78, s59, 64\n"
    "s_mov_b64 exec, 1\n"
    "buffer_store_dwordx2 v[4:5], off, s[20:23], s78 sc1\n"
    "s_mov_b64 exec, -1\n"
    "v_add_u32 v232, 49152, v226\n"
    "ds_read_b64_tr_b16 v[192:193], v232 offset:0\n"
    "ds_read_b64_tr_b16 v[194:195], v232 offset:3072\n"
    "s_add_u32 m0, s69, 0\n"
    "s_add_u32 s78, s62, 221184\n"
    "buffer_load_dwordx4 v9, s[16:19], s78 offen lds\n"
    "s_waitcnt lgkmcnt(2)\n"
    "v_mfma_f32_32x32x16_bf16 v[144:159], v[200:203], v[52:55], v[144:159]\n"
    "v_add_u32 v233, 49152, v227\n"
    "ds_read_b64_tr_b16 v[196:197], v233 offset:0\n"
    "ds_read_b64_tr_b16 v[198:199], v233 offset:3072\n"
    "s_waitcnt lgkmcnt(2)\n"
    "v_mfma_f32_32x32x16_bf16 v[64:79], v[192:195], v[56:59], v[64:79]\n"
    "v_add_u32 v234, 49152, v228\n"
    "ds_read_b64_tr_b16 v[200:201], v234 offset:0\n"
    "ds_read_b64_tr_b16 v[202:203], v234 offset:3072\n"
    "s_add_u32 m0, s69, 1024\n"
    "s_add_u32 s78, s62, 221184\n"
    "buffer_load_dwordx4 v10, s[16:19], s78 offen lds\n"
    "s_waitcnt lgkmcnt(2)\n"
    "v_mfma_f32_32x32x16_bf16 v[80:95], v[196:199], v[56:59], v[80:95]\n"
    "v_add_u32 v235, 49152, v229\n"
    "ds_read_b64_tr_b16 v[192:193], v235 offset:0\n"
    "ds_read_b64_tr_b16 v[194:195], v235 offset:3072\n"
    "s_waitcnt lgkmcnt(2)\n"
    "v_mfma_f32_32x32x16_bf16 v[96:111], v[200:203], v[56:59], v[96:111]\n"
    "v_add_u32 v236, 49152, v230\n"
    "ds_read_b64_tr_b16 v[196:197], v236 offset:0\n"
    "ds_read_b64_tr_b16 v[198:199], v236 offset:3072\n"
    "s_add_u32 m0, s69, 2048\n"
    "s_add_u32 s78, s62, 221184\n"
    "buffer_load_dwordx4 v11, s[16:19], s78 offen lds\n"
    "s_waitcnt lgkmcnt(2)\n"
    "v_mfma_f32_32x32x16_bf16 v[112:127], v[192:195], v[56:59], v[112:127]\n"
    "v_add_u32 v237, 49152, v231\n"
    "ds_read_b64_tr_b16 v[200:201], v237 offset:0\n"
    "ds_read_b64_tr_b16 v[202:203], v237 offset:3072\n"
    "s_waitcnt lgkmcnt(2)\n"
    "v_mfma_f32_32x32x16_bf16 v[128:143], v[196:199], v[56:59], v[128:143]\n"
    "ds_read_b64_tr_b16 v[192:193], v232 offset:6144\n"
    "ds_read_b64_tr_b16 v[194:195], v232 offset:9216\n"
    "s_waitcnt lgkmcnt(2)\n"
    "v_mfma_f32_32x32x16_bf16 v[144:159], v[200:203], v[56:59], v[144:159]\n"
    "ds_read_b64_tr_b16 v[196:197], v233 offset:6144\n"
    "ds_read_b64_tr_b16 v[198:199], v233 offset:9216\n"
    "s_add_u32 s78, s65, 49152\n"
    "buffer_load_dwordx4 v[24:27], v2, s[20:23], s78 offen sc1\n"
    "s_waitcnt lgkmcnt(2)\n"
    "v_mfma_f32_32x32x16_bf16 v[64:79], v[192:195], v[60:63], v[64:79]\n"
    "ds_read_b64_tr_b16 v[200:201], v234 offset:6144\n"
    "ds_read_b64_tr_b16 v[202:203], v234 offset:9216\n"
    "s_waitcnt lgkmcnt(2)\n"
    "v_mfma_f32_32x32x16_bf16 v[80:95], v[196:199], v[60:63], v[80:95]\n"
    "ds_read_b64_tr_b16 v[192:193], v235 offset:6144\n"
    "ds_read_b64_tr_b16 v[194:195], v235 offset:9216\n"
    "s_add_u32 s78, s65, 50176\n"
    "buffer_load_dwordx4 v[28:31], v2, s[20:23], s78 offen sc1\n"
    "s_waitcnt lgkmcnt(2)\n"
    "v_mfma_f32_32x32x16_bf16 v[96:111], v[200:203], v[60:63], v[96:111]\n"
    "ds_read_b64_tr_b16 v[196:197], v236 offset:6144\n"
    "ds_read_b64_tr_b16 v[198:199], v236 offset:9216\n"
    "s_waitcnt lgkmcnt(2)\n"
    "v_mfma_f32_32x32x16_bf16 v[112:127], v[192:195], v[60:63], v[112:127]\n"
    "ds_read_b64_tr_b16 v[200:201], v237 offset:6144\n"
    "ds_read_b64_tr_b16 v[202:203], v237 offset:9216\n"
    "s_waitcnt lgkmcnt(2)\n"
    "v_mfma_f32_32x32x16_bf16 v[128:143], v[196:199], v[60:63], v[128:143]\n"
    "s_barrier\n"
    "v_add_u32 v232, 73728, v226\n"
    "ds_read_b64_tr_b16 v[192:193], v232 offset:0\n"
    "ds_read_b64_tr_b16 v[194:195], v232 offset:3072\n"
    "s_add_u32 m0, s69, 24576\n"
    "s_add_u32 s78, s63, 221184\n"
    "buffer_load_dwordx4 v9, s[16:19], s78 offen lds\n"
    "s_waitcnt lgkmcnt(2)\n"
    "v_mfma_f32_32x32x16_bf16 v[144:159], v[200:203], v[60:63], v[144:159]\n"
    "v_add_u32 v233, 73728, v227\n"
    "ds_read_b64_tr_b16 v[196:197], v233 offset:0\n"
    "ds_read_b64_tr_b16 v[198:199], v233 offset:3072\n"
    "s_waitcnt lgkmcnt(2)\n"
    "v_mfma_f32_32x32x16_bf16 v[64:79], v[192:195], v[40:43], v[64:79]\n"
    "v_add_u32 v234, 73728, v228\n"
    "ds_read_b64_tr_b16 v[200:201], v234 offset:0\n"
    "ds_read_b64_tr_b16 v[202:203], v234 offset:3072\n"
    "s_add_u32 m0, s69, 25600\n"
    "s_add_u32 s78, s63, 221184\n"
    "buffer_load_dwordx4 v10, s[16:19], s78 offen lds\n"
    "s_waitcnt lgkmcnt(2)\n"
    "v_mfma_f32_32x32x16_bf16 v[80:95], v[196:199], v[40:43], v[80:95]\n"
    "v_add_u32 v235, 73728, v229\n"
    "ds_read_b64_tr_b16 v[192:193], v235 offset:0\n"
    "ds_read_b64_tr_b16 v[194:195], v235 offset:3072\n"
    "s_waitcnt lgkmcnt(2)\n"
    "v_mfma_f32_32x32x16_bf16 v[96:111], v[200:203], v[40:43], v[96:111]\n"
    "v_add_u32 v236, 73728, v230\n"
    "ds_read_b64_tr_b16 v[196:197], v236 offset:0\n"
    "ds_read_b64_tr_b16 v[198:199], v236 offset:3072\n"
    "s_add_u32 m0, s69, 26624\n"
    "s_add_u32 s78, s63, 221184\n"
    "buffer_load_dwordx4 v11, s[16:19], s78 offen lds\n"
    "s_waitcnt lgkmcnt(2)\n"
    "v_mfma_f32_32x32x16_bf16 v[112:127], v[192:195], v[40:43], v[112:127]\n"
    "v_add_u32 v237, 73728, v231\n"
    "ds_read_b64_tr_b16 v[200:201], v237 offset:0\n"
    "ds_read_b64_tr_b16 v[202:203], v237 offset:3072\n"
    "s_waitcnt lgkmcnt(2)\n"
    "v_mfma_f32_32x32x16_bf16 v[128:143], v[196:199], v[40:43], v[128:143]\n"
    "ds_read_b64_tr_b16 v[192:193], v232 offset:6144\n"
    "ds_read_b64_tr_b16 v[194:195], v232 offset:9216\n"
    "s_waitcnt lgkmcnt(2)\n"
    "v_mfma_f32_32x32x16_bf16 v[144:159], v[200:203], v[40:43], v[144:159]\n"
    "ds_read_b64_tr_b16 v[196:197], v233 offset:6144\n"
    "ds_read_b64_tr_b16 v[198:199], v233 offset:9216\n"
    "s_add_u32 s78, s66, 49152\n"
    "buffer_load_dwordx4 v[48:51], v2, s[20:23], s78 offen sc1\n"
    "s_waitcnt lgkmcnt(2)\n"
    "v_mfma_f32_32x32x16_bf16 v[64:79], v[192:195], v[44:47], v[64:79]\n"
    "ds_read_b64_tr_b16 v[200:201], v234 offset:6144\n"
    "ds_read_b64_tr_b16 v[202:203], v234 offset:9216\n"
    "s_waitcnt lgkmcnt(2)\n"
    "v_mfma_f32_32x32x16_bf16 v[80:95], v[196:199], v[44:47], v[80:95]\n"
    "ds_read_b64_tr_b16 v[192:193], v235 offset:6144\n"
    "ds_read_b64_tr_b16 v[194:195], v235 offset:9216\n"
    "s_add_u32 s78, s66, 50176\n"
    "buffer_load_dwordx4 v[52:55], v2, s[20:23], s78 offen sc1\n"
    "s_waitcnt lgkmcnt(2)\n"
    "v_mfma_f32_32x32x16_bf16 v[96:111], v[200:203], v[44:47], v[96:111]\n"
    "ds_read_b64_tr_b16 v[196:197], v236 offset:6144\n"
    "ds_read_b64_tr_b16 v[198:199], v236 offset:9216\n"
    "s_waitcnt lgkmcnt(2)\n"
    "v_mfma_f32_32x32x16_bf16 v[112:127], v[192:195], v[44:47], v[112:127]\n"
    "ds_read_b64_tr_b16 v[200:201], v237 offset:6144\n"
    "ds_read_b64_tr_b16 v[202:203], v237 offset:9216\n"
    "s_waitcnt lgkmcnt(2)\n"
    "v_mfma_f32_32x32x16_bf16 v[128:143], v[196:199], v[44:47], v[128:143]\n"
    "s_waitcnt vmcnt(16)\n"
    "s_barrier\n"
    "s_add_u32 s78, s60, 64\n"
    "buffer_load_dwordx2 v[6:7], v3, s[20:23], s78 offen sc1\n"
    "v_add_u32 v232, 98304, v226\n"
    "ds_read_b64_tr_b16 v[192:193], v232 offset:0\n"
    "ds_read_b64_tr_b16 v[194:195], v232 offset:3072\n"
    "s_add_u32 m0, s69, 49152\n"
    "s_add_u32 s78, s62, 294912\n"
    "buffer_load_dwordx4 v9, s[16:19], s78 offen lds\n"
    "s_waitcnt lgkmcnt(2)\n"
    "v_mfma_f32_32x32x16_bf16 v[144:159], v[200:203], v[44:47], v[144:159]\n"
    "v_add_u32 v233, 98304, v227\n"
    "ds_read_b64_tr_b16 v[196:197], v233 offset:0\n"
    "ds_read_b64_tr_b16 v[198:199], v233 offset:3072\n"
    "s_waitcnt lgkmcnt(2)\n"
    "v_mfma_f32_32x32x16_bf16 v[64:79], v[192:195], v[32:35], v[64:79]\n"
    "v_add_u32 v234, 98304, v228\n"
    "ds_read_b64_tr_b16 v[200:201], v234 offset:0\n"
    "ds_read_b64_tr_b16 v[202:203], v234 offset:3072\n"
    "s_add_u32 m0, s69, 50176\n"
    "s_add_u32 s78, s62, 294912\n"
    "buffer_load_dwordx4 v10, s[16:19], s78 offen lds\n"
    "s_waitcnt lgkmcnt(2)\n"
    "v_mfma_f32_32x32x16_bf16 v[80:95], v[196:199], v[32:35], v[80:95]\n"
    "v_add_u32 v235, 98304, v229\n"
    "ds_read_b64_tr_b16 v[192:193], v235 offset:0\n"
    "ds_read_b64_tr_b16 v[194:195], v235 offset:3072\n"
    "s_waitcnt lgkmcnt(2)\n"
    "v_mfma_f32_32x32x16_bf16 v[96:111], v[200:203], v[32:35], v[96:111]\n"
    "v_add_u32 v236, 98304, v230\n"
    "ds_read_b64_tr_b16 v[196:197], v236 offset:0\n"
    "ds_read_b64_tr_b16 v[198:199], v236 offset:3072\n"
    "s_add_u32 m0, s69, 51200\n"
    "s_add_u32 s78, s62, 294912\n"
    "buffer_load_dwordx4 v11, s[16:19], s78 offen lds\n"
    "s_waitcnt lgkmcnt(2)\n"
    "v_mfma_f32_32x32x16_bf16 v[112:127], v[192:195], v[32:35], v[112:127]\n"
    "v_add_u32 v237, 98304, v231\n"
    "ds_read_b64_tr_b16 v[200:201], v237 offset:0\n"
    "ds_read_b64_tr_b16 v[202:203], v237 offset:3072\n"
    "s_waitcnt lgkmcnt(2)\n"
    "v_mfma_f32_32x32x16_bf16 v[128:143], v[196:199], v[32:35], v[128:143]\n"
    "ds_read_b64_tr_b16 v[192:193], v232 offset:6144\n"
    "ds_read_b64_tr_b16 v[194:195], v232 offset:9216\n"
    "s_waitcnt lgkmcnt(2)\n"
    "v_mfma_f32_32x32x16_bf16 v[144:159], v[200:203], v[32:35], v[144:159]\n"
    "ds_read_b64_tr_b16 v[196:197], v233 offset:6144\n"
    "ds_read_b64_tr_b16 v[198:199], v233 offset:9216\n"
    "s_add_u32 s78, s65, 65536\n"
    "buffer_load_dwordx4 v[56:59], v2, s[20:23], s78 offen sc1\n"
    "s_waitcnt lgkmcnt(2)\n"
    "v_mfma_f32_32x32x16_bf16 v[64:79], v[192:195], v[36:39], v[64:79]\n"
    "ds_read_b64_tr_b16 v[200:201], v234 offset:6144\n"
    "ds_read_b64_tr_b16 v[202:203], v234 offset:9216\n"
    "s_waitcnt lgkmcnt(2)\n"
    "v_mfma_f32_32x32x16_bf16 v[80:95], v[196:199], v[36:39], v[80:95]\n"
    "ds_read_b64_tr_b16 v[192:193], v235 offset:6144\n"
    "ds_read_b64_tr_b16 v[194:195], v235 offset:9216\n"
    "s_add_u32 s78, s65, 66560\n"
    "buffer_load_dwordx4 v[60:63], v2, s[20:23], s78 offen sc1\n"
    "s_waitcnt lgkmcnt(2)\n"
    "v_mfma_f32_32x32x16_bf16 v[96:111], v[200:203], v[36:39], v[96:111]\n"
    "ds_read_b64_tr_b16 v[196:197], v236 offset:6144\n"
    "ds_read_b64_tr_b16 v[198:199], v236 offset:9216\n"
    "s_waitcnt lgkmcnt(2)\n"
    "v_mfma_f32_32x32x16_bf16 v[112:127], v[192:195], v[36:39], v[112:127]\n"
    "ds_read_b64_tr_b16 v[200:201], v237 offset:6144\n"
    "ds_read_b64_tr_b16 v[202:203], v237 offset:9216\n"
    "s_waitcnt lgkmcnt(2)\n"
    "v_mfma_f32_32x32x16_bf16 v[128:143], v[196:199], v[36:39], v[128:143]\n"
    "s_waitcnt vmcnt(17)\n"
    "s_barrier\n"
    "v_add_u32 v232, 122880, v226\n"
    "ds_read_b64_tr_b16 v[192:193], v232 offset:0\n"
    "ds_read_b64_tr_b16 v[194:195], v232 offset:3072\n"
    "s_add_u32 m0, s69, 73728\n"
    "s_add_u32 s78, s63, 294912\n"
    "buffer_load_dwordx4 v9, s[16:19], s78 offen lds\n"
    "s_waitcnt lgkmcnt(2)\n"
    "v_mfma_f32_32x32x16_bf16 v[144:159], v[200:203], v[36:39], v[144:159]\n"
    "v_add_u32 v233, 122880, v227\n"
    "ds_read_b64_tr_b16 v[196:197], v233 offset:0\n"
    "ds_read_b64_tr_b16 v[198:199], v233 offset:3072\n"
    "s_waitcnt lgkmcnt(2)\n"
    "v_mfma_f32_32x32x16_bf16 v[64:79], v[192:195], v[16:19], v[64:79]\n"
    "v_add_u32 v234, 122880, v228\n"
    "ds_read_b64_tr_b16 v[200:201], v234 offset:0\n"
    "ds_read_b64_tr_b16 v[202:203], v234 offset:3072\n"
    "s_add_u32 m0, s69, 74752\n"
    "s_add_u32 s78, s63, 294912\n"
    "buffer_load_dwordx4 v10, s[16:19], s78 offen lds\n"
    "s_waitcnt lgkmcnt(2)\n"
    "v_mfma_f32_32x32x16_bf16 v[80:95], v[196:199], v[16:19], v[80:95]\n"
    "v_add_u32 v235, 122880, v229\n"
    "ds_read_b64_tr_b16 v[192:193], v235 offset:0\n"
    "ds_read_b64_tr_b16 v[194:195], v235 offset:3072\n"
    "s_waitcnt lgkmcnt(2)\n"
    "v_mfma_f32_32x32x16_bf16 v[96:111], v[200:203], v[16:19], v[96:111]\n"
    "v_add_u32 v236, 122880, v230\n"
    "ds_read_b64_tr_b16 v[196:197], v236 offset:0\n"
    "ds_read_b64_tr_b16 v[198:199], v236 offset:3072\n"
    "s_add_u32 m0, s69, 75776\n"
    "s_add_u32 s78, s63, 294912\n"
    "buffer_load_dwordx4 v11, s[16:19], s78 offen lds\n"
    "s_waitcnt lgkmcnt(2)\n"
    "v_mfma_f32_32x32x16_bf16 v[112:127], v[192:195], v[16:19], v[112:127]\n"
    "v_add_u32 v237, 122880, v231\n"
    "ds_read_b64_tr_b16 v[200:201], v237 offset:0\n"
    "ds_read_b64_tr_b16 v[202:203], v237 offset:3072\n"
    "s_waitcnt lgkmcnt(2)\n"
    "v_mfma_f32_32x32x16_bf16 v[128:143], v[196:199], v[16:19], v[128:143]\n"
    "ds_read_b64_tr_b16 v[192:193], v232 offset:6144\n"
    "ds_read_b64_tr_b16 v[194:195], v232 offset:9216\n"
    "s_waitcnt lgkmcnt(2)\n"
    "v_mfma_f32_32x32x16_bf16 v[144:159], v[200:203], v[16:19], v[144:159]\n"
    "ds_read_b64_tr_b16 v[196:197], v233 offset:6144\n"
    "ds_read_b64_tr_b16 v[198:199], v233 offset:9216\n"
    "s_add_u32 s78, s66, 65536\n"
    "buffer_load_dwordx4 v[40:43], v2, s[20:23], s78 offen sc1\n"
    "s_waitcnt lgkmcnt(2)\n"
    "v_mfma_f32_32x32x16_bf16 v[64:79], v[192:195], v[20:23], v[64:79]\n"
    "ds_read_b64_tr_b16 v[200:201], v234 offset:6144\n"
    "ds_read_b64_tr_b16 v[202:203], v234 offset:9216\n"
    "s_waitcnt lgkmcnt(2)\n"
    "v_mfma_f32_32x32x16_bf16 v[80:95], v[196:199], v[20:23], v[80:95]\n"
    "ds_read_b64_tr_b16 v[192:193], v235 offset:6144\n"
    "ds_read_b64_tr_b16 v[194:195], v235 offset:9216\n"
    "s_add_u32 s78, s66, 66560\n"
    "buffer_load_dwordx4 v[44:47], v2, s[20:23], s78 offen sc1\n"
    "s_waitcnt lgkmcnt(2)\n"
    "v_mfma_f32_32x32x16_bf16 v[96:111], v[200:203], v[20:23], v[96:111]\n"
    "ds_read_b64_tr_b16 v[196:197], v236 offset:6144\n"
    "ds_read_b64_tr_b16 v[198:199], v236 offset:9216\n"
    "s_waitcnt lgkmcnt(2)\n"
    "v_mfma_f32_32x32x16_bf16 v[112:127], v[192:195], v[20:23], v[112:127]\n"
    "ds_read_b64_tr_b16 v[200:201], v237 offset:6144\n"
    "ds_read_b64_tr_b16 v[202:203], v237 offset:9216\n"
    "s_waitcnt lgkmcnt(2)\n"
    "v_mfma_f32_32x32x16_bf16 v[128:143], v[196:199], v[20:23], v[128:143]\n"
    "s_waitcnt vmcnt(10)\n"
    "s_barrier\n"
    "v_cmp_ne_u64 vcc, v[6:7], v[4:5]\n"
    "s_nop 0\n"
    "s_and_b64 vcc, vcc, s[88:89]\n"
    "s_cbranch_vccz Lok_13\n"
    "s_mov_b32 s86, 0x40000\n"
    "s_add_u32 s78, s60, 64\n"
    "Lspin_12:\n"
    "s_sleep 1\n"
    "buffer_load_dwordx2 v[6:7], v3, s[20:23], s78 offen sc1\n"
    "s_waitcnt vmcnt(0)\n"
    "s_sub_u32 s86, s86, 1\n"
    "s_cbranch_scc1 Lok_13\n"
    "v_cmp_ne_u64 vcc, v[6:7], v[4:5]\n"
    "s_nop 1\n"
    "s_and_b64 vcc, vcc, s[88:89]\n"
    "s_cbranch_vccnz Lspin_12\n"
    "Lok_13:\n"
    "s_mov_b64 vcc, s[90:91]\n"
    "s_mov_b32 s88, 3\n"
    "v_add_u32 v232, 0, v226\n"
    "ds_read_b64_tr_b16 v[192:193], v232 offset:0\n"
    "ds_read_b64_tr_b16 v[194:195], v232 offset:3072\n"
    "s_add_u32 m0, s69, 98304\n"
    "s_add_u32 s78, s62, 368640\n"
    "buffer_load_dwordx4 v9, s[16:19], s78 offen lds\n"
    "s_waitcnt lgkmcnt(2)\n"
    "v_mfma_f32_32x32x16_bf16 v[144:159], v[200:203], v[20:23], v[144:159]\n"
    "v_add_u32 v233, 0, v227\n"
    "ds_read_b64_tr_b16 v[196:197], v233 offset:0\n"
    "ds_read_b64_tr_b16 v[198:199], v233 offset:3072\n"
    "s_waitcnt lgkmcnt(2)\n"
    "v_mfma_f32_32x32x16_bf16 v[64:79], v[192:195], v[24:27], v[64:79]\n"
    "v_add_u32 v234, 0, v228\n"
    "ds_read_b64_tr_b16 v[200:201], v234 offset:0\n"
    "ds_read_b64_tr_b16 v[202:203], v234 offset:3072\n"
    "s_add_u32 m0, s69, 99328\n"
    "s_add_u32 s78, s62, 368640\n"
    "buffer_load_dwordx4 v10, s[16:19], s78 offen lds\n"
    "s_waitcnt lgkmcnt(2)\n"
    "v_mfma_f32_32x32x16_bf16 v[80:95], v[196:199], v[24:27], v[80:95]\n"
    "v_add_u32 v235, 0, v229\n"
    "ds_read_b64_tr_b16 v[192:193], v235 offset:0\n"
    "ds_read_b64_tr_b16 v[194:195], v235 offset:3072\n"
    "s_waitcnt lgkmcnt(2)\n"
    "v_mfma_f32_32x32x16_bf16 v[96:111], v[200:203], v[24:27], v[96:111]\n"
    "v_add_u32 v236, 0, v230\n"
    "ds_read_b64_tr_b16 v[196:197], v236 offset:0\n"
    "ds_read_b64_tr_b16 v[198:199], v236 offset:3072\n"
    "s_add_u32 m0, s69, 100352\n"
    "s_add_u32 s78, s62, 368640\n"
    "buffer_load_dwordx4 v11, s[16:19], s78 offen lds\n"
    "s_waitcnt lgkmcnt(2)\n"
    "v_mfma_f32_32x32x16_bf16 v[112:127], v[192:195], v[24:27], v[112:127]\n"
    "v_add_u32 v237, 0, v231\n"
    "ds_read_b64_tr_b16 v[200:201], v237 offset:0\n"
    "ds_read_b64_tr_b16 v[202:203], v237 offset:3072\n"
    "s_waitcnt lgkmcnt(2)\n"
    "v_mfma_f32_32x32x16_bf16 v[128:143], v[196:199], v[24:27], v[128:143]\n"
    "ds_read_b64_tr_b16 v[192:193], v232 offset:6144\n"
    "ds_read_b64_tr_b16 v[194:195], v232 offset:9216\n"
    "s_waitcnt lgkmcnt(2)\n"
    "v_mfma_f32_32x32x16_bf16 v[144:159], v[200:203], v[24:27], v[144:159]\n"
    "ds_read_b64_tr_b16 v[196:197], v233 offset:6144\n"
    "ds_read_b64_tr_b16 v[198:199], v233 offset:9216\n"
    "s_add_u32 s78, s65, 81920\n"
    "buffer_load_dwordx4 v[32:35], v2, s[20:23], s78 offen sc1\n"
    "s_waitcnt lgkmcnt(2)\n"
    "v_mfma_f32_32x32x16_bf16 v[64:79], v[192:195], v[28:31], v[64:79]\n"
    "ds_read_b64_tr_b16 v[200:201], v234 offset:6144\n"
    "ds_read_b64_tr_b16 v[202:203], v234 offset:9216\n"
    "s_waitcnt lgkmcnt(2)\n"
    "v_mfma_f32_32x32x16_bf16 v[80:95], v[196:199], v[28:31], v[80:95]\n"
    "ds_read_b64_tr_b16 v[192:193], v235 offset:6144\n"
    "ds_read_b64_tr_b16 v[194:195], v235 offset:9216\n"
    "s_add_u32 s78, s65, 82944\n"
    "buffer_load_dwordx4 v[36:39], v2, s[20:23], s78 offen sc1\n"
    "s_waitcnt lgkmcnt(2)\n"
    "v_mfma_f32_32x32x16_bf16 v[96:111], v[200:203], v[28:31], v[96:111]\n"
    "ds_read_b64_tr_b16 v[196:197], v236 offset:6144\n"
    "ds_read_b64_tr_b16 v[198:199], v236 offset:9216\n"
    "s_waitcnt lgkmcnt(2)\n"
    "v_mfma_f32_32x32x16_bf16 v[112:127], v[192:195], v[28:31], v[112:127]\n"
    "ds_read_b64_tr_b16 v[200:201], v237 offset:6144\n"
    "ds_read_b64_tr_b16 v[202:203], v237 offset:9216\n"
    "s_waitcnt lgkmcnt(2)\n"
    "v_mfma_f32_32x32x16_bf16 v[128:143], v[196:199], v[28:31], v[128:143]\n"
    "s_barrier\n"
    "v_add_u32 v232, 24576, v226\n"
    "ds_read_b64_tr_b16 v[192:193], v232 offset:0\n"
    "ds_read_b64_tr_b16 v[194:195], v232 offset:3072\n"
    "s_add_u32 m0, s69, 122880\n"
    "s_add_u32 s78, s63, 368640\n"
    "buffer_load_dwordx4 v9, s[16:19], s78 offen lds\n"
    "s_waitcnt lgkmcnt(2)\n"
    "v_mfma_f32_32x32x16_bf16 v[144:159], v[200:203], v[28:31], v[144:159]\n"
    "v_add_u32 v233, 24576, v227\n"
    "ds_read_b64_tr_b16 v[196:197], v233 offset:0\n"
    "ds_read_b64_tr_b16 v[198:199], v233 offset:3072\n"
    "s_waitcnt lgkmcnt(2)\n"
    "v_mfma_f32_32x32x16_bf16 v[64:79], v[192:195], v[48:51], v[64:79]\n"
    "v_add_u32 v234, 24576, v228\n"
    "ds_read_b64_tr_b16 v[200:201], v234 offset:0\n"
    "ds_read_b64_tr_b16 v[202:203], v234 offset:3072\n"
    "s_add_u32 m0, s69, 123904\n"
    "s_add_u32 s78, s63, 368640\n"
    "buffer_load_dwordx4 v10, s[16:19], s78 offen lds\n"
    "s_waitcnt lgkmcnt(2)\n"
    "v_mfma_f32_32x32x16_bf16 v[80:95], v[196:199], v[48:51], v[80:95]\n"
    "v_add_u32 v235, 24576, v229\n"
    "ds_read_b64_tr_b16 v[192:193], v235 offset:0\n"
    "ds_read_b64_tr_b16 v[194:195], v235 offset:3072\n"
    "s_waitcnt lgkmcnt(2)\n"
    "v_mfma_f32_32x32x16_bf16 v[96:111], v[200:203], v[48:51], v[96:111]\n"
    "v_add_u32 v236, 24576, v230\n"
    "ds_read_b64_tr_b16 v[196:197], v236 offset:0\n"
    "ds_read_b64_tr_b16 v[198:199], v236 offset:3072\n"
    "s_add_u32 m0, s69, 124928\n"
    "s_add_u32 s78, s63, 368640\n"
    "buffer_load_dwordx4 v11, s[16:19], s78 offen lds\n"
    "s_waitcnt lgkmcnt(2)\n"
    "v_mfma_f32_32x32x16_bf16 v[112:127], v[192:195], v[48:51], v[112:127]\n"
    "v_add_u32 v237, 24576, v231\n"
    "ds_read_b64_tr_b16 v[200:201], v237 offset:0\n"
    "ds_read_b64_tr_b16 v[202:203], v237 offset:3072\n"
    "s_waitcnt lgkmcnt(2)\n"
    "v_mfma_f32_32x32x16_bf16 v[128:143], v[196:199], v[48:51], v[128:143]\n"
    "ds_read_b64_tr_b16 v[192:193], v232 offset:6144\n"
    "ds_read_b64_tr_b16 v[194:195], v232 offset:9216\n"
    "s_waitcnt lgkmcnt(2)\n"
    "v_mfma_f32_32x32x16_bf16 v[144:159], v[200:203], v[48:51], v[144:159]\n"
    "ds_read_b64_tr_b16 v[196:197], v233 offset:6144\n"
    "ds_read_b64_tr_b16 v[198:199], v233 offset:9216\n"
    "s_add_u32 s78, s66, 81920\n"
    "buffer_load_dwordx4 v[16:19], v2, s[20:23], s78 offen sc1\n"
    "s_waitcnt lgkmcnt(2)\n"
    "v_mfma_f32_32x32x16_bf16 v[64:79], v[192:195], v[52:55], v[64:79]\n"
    "ds_read_b64_tr_b16 v[200:201], v234 offset:6144\n"
    "ds_read_b64_tr_b16 v[202:203], v234 offset:9216\n"
    "s_waitcnt lgkmcnt(2)\n"
    "v_mfma_f32_32x32x16_bf16 v[80:95], v[196:199], v[52:55], v[80:95]\n"
    "ds_read_b64_tr_b16 v[192:193], v235 offset:6144\n"
    "ds_read_b64_tr_b16 v[194:195], v235 offset:9216\n"
    "s_add_u32 s78, s66, 82944\n"
    "buffer_load_dwordx4 v[20:23], v2, s[20:23], s78 offen sc1\n"
    "s_waitcnt lgkmcnt(2)\n"
    "v_mfma_f32_32x32x16_bf16 v[96:111], v[200:203], v[52:55], v[96:111]\n"
    "ds_read_b64_tr_b16 v[196:197], v236 offset:6144\n"
    "ds_read_b64_tr_b16 v[198:199], v236 offset:9216\n"
    "s_waitcnt lgkmcnt(2)\n"
    "v_mfma_f32_32x32x16_bf16 v[112:127], v[192:195], v[52:55], v[112:127]\n"
    "ds_read_b64_tr_b16 v[200:201], v237 offset:6144\n"
    "ds_read_b64_tr_b16 v[202:203], v237 offset:9216\n"
    "s_waitcnt lgkmcnt(2)\n"
    "v_mfma_f32_32x32x16_bf16 v[128:143], v[196:199], v[52:55], v[128:143]\n"
    "s_waitcnt vmcnt(15)\n"
    "s_barrier\n"
    "s_add_u32 s78, s98, 176128\n"
    "buffer_load_dwordx2 v[6:7], v3, s[20:23], s78 offen sc1\n"
    "v_add_u32 v232, 49152, v226\n"
    "ds_read_b64_tr_b16 v[192:193], v232 offset:0\n"
    "ds_read_b64_tr_b16 v[194:195], v232 offset:3072\n"
    "s_add_u32 m0, s69, 0\n"
    "s_nop 0\n"
    "buffer_load_dwordx4 v9, s[28:31], s68 offen lds\n"
    "s_waitcnt lgkmcnt(2)\n"
    "v_mfma_f32_32x32x16_bf16 v[144:159], v[200:203], v[52:55], v[144:159]\n"
    "v_add_u32 v233, 49152, v227\n"
    "ds_read_b64_tr_b16 v[196:197], v233 offset:0\n"
    "ds_read_b64_tr_b16 v[198:199], v233 offset:3072\n"
    "s_waitcnt lgkmcnt(2)\n"
    "v_mfma_f32_32x32x16_bf16 v[64:79], v[192:195], v[56:59], v[64:79]\n"
    "v_add_u32 v234, 49152, v228\n"
    "ds_read_b64_tr_b16 v[200:201], v234 offset:0\n"
    "ds_read_b64_tr_b16 v[202:203], v234 offset:3072\n"
    "s_waitcnt lgkmcnt(2)\n"
    "v_mfma_f32_32x32x16_bf16 v[80:95], v[196:199], v[56:59], v[80:95]\n"
    "v_add_u32 v235, 49152, v229\n"
    "ds_read_b64_tr_b16 v[192:193], v235 offset:0\n"
    "ds_read_b64_tr_b16 v[194:195], v235 offset:3072\n"
    "s_waitcnt lgkmcnt(2)\n"
    "v_mfma_f32_32x32x16_bf16 v[96:111], v[200:203], v[56:59], v[96:111]\n"
    "v_add_u32 v236, 49152, v230\n"
    "ds_read_b64_tr_b16 v[196:197], v236 offset:0\n"
    "ds_read_b64_tr_b16 v[198:199], v236 offset:3072\n"
    "s_add_u32 m0, s69, 1024\n"
    "s_nop 0\n"
    "buffer_load_dwordx4 v10, s[28:31], s68 offen lds\n"
    "s_waitcnt lgkmcnt(2)\n"
    "v_mfma_f32_32x32x16_bf16 v[112:127], v[192:195], v[56:59], v[112:127]\n"
    "v_add_u32 v237, 49152, v231\n"
    "ds_read_b64_tr_b16 v[200:201], v237 offset:0\n"
    "ds_read_b64_tr_b16 v[202:203], v237 offset:3072\n"
    "s_waitcnt lgkmcnt(2)\n"
    "v_mfma_f32_32x32x16_bf16 v[128:143], v[196:199], v[56:59], v[128:143]\n"
    "ds_read_b64_tr_b16 v[192:193], v232 offset:6144\n"
    "ds_read_b64_tr_b16 v[194:195], v232 offset:9216\n"
    "s_waitcnt lgkmcnt(2)\n"
    "v_mfma_f32_32x32x16_bf16 v[144:159], v[200:203], v[56:59], v[144:159]\n"
    "ds_read_b64_tr_b16 v[196:197], v233 offset:6144\n"
    "ds_read_b64_tr_b16 v[198:199], v233 offset:9216\n"
    "s_waitcnt lgkmcnt(2)\n"
    "v_mfma_f32_32x32x16_bf16 v[64:79], v[192:195], v[60:63], v[64:79]\n"
    "ds_read_b64_tr_b16 v[200:201], v234 offset:6144\n"
    "ds_read_b64_tr_b16 v[202:203], v234 offset:9216\n"
    "s_add_u32 m0, s69, 2048\n"
    "s_nop 0\n"
    "buffer_load_dwordx4 v11, s[28:31], s68 offen lds\n"
    "s_waitcnt lgkmcnt(2)\n"
    "v_mfma_f32_32x32x16_bf16 v[80:95], v[196:199], v[60:63], v[80:95]\n"
    "ds_read_b64_tr_b16 v[192:193], v235 offset:6144\n"
    "ds_read_b64_tr_b16 v[194:195], v235 offset:9216\n"
    "s_waitcnt lgkmcnt(2)\n"
    "v_mfma_f32_32x32x16_bf16 v[96:111], v[200:203], v[60:63], v[96:111]\n"
    "ds_read_b64_tr_b16 v[196:197], v236 offset:6144\n"
    "ds_read_b64_tr_b16 v[198:199], v236 offset:9216\n"
    "s_waitcnt lgkmcnt(2)\n"
    "v_mfma_f32_32x32x16_bf16 v[112:127], v[192:195], v[60:63], v[112:127]\n"
    "ds_read_b64_tr_b16 v[200:201], v237 offset:6144\n"
    "ds_read_b64_tr_b16 v[202:203], v237 offset:9216\n"
    "s_waitcnt lgkmcnt(2)\n"
    "v_mfma_f32_32x32x16_bf16 v[128:143], v[196:199], v[60:63], v[128:143]\n"
    "s_waitcnt vmcnt(14)\n"
    "s_barrier\n"
    "v_add_u32 v232, 73728, v226\n"
    "ds_read_b64_tr_b16 v[192:193], v232 offset:0\n"
    "ds_read_b64_tr_b16 v[194:195], v232 offset:3072\n"
    "s_add_u32 m0, s69, 24576\n"
    "s_add_u32 s78, s68, 82944\n"
    "buffer_load_dwordx4 v9, s[28:31], s78 offen lds\n"
    "s_waitcnt lgkmcnt(2)\n"
    "v_mfma_f32_32x32x16_bf16 v[144:159], v[200:203], v[60:63], v[144:159]\n"
    "v_add_u32 v233, 73728, v227\n"
    "ds_read_b64_tr_b16 v[196:197], v233 offset:0\n"
    "ds_read_b64_tr_b16 v[198:199], v233 offset:3072\n"
    "s_waitcnt lgkmcnt(2)\n"
    "v_mfma_f32_32x32x16_bf16 v[64:79], v[192:195], v[40:43], v[64:79]\n"
    "v_add_u32 v234, 73728, v228\n"
    "ds_read_b64_tr_b16 v[200:201], v234 offset:0\n"
    "ds_read_b64_tr_b16 v[202:203], v234 offset:3072\n"
    "s_waitcnt lgkmcnt(2)\n"
    "v_mfma_f32_32x32x16_bf16 v[80:95], v[196:199], v[40:43], v[80:95]\n"
    "v_add_u32 v235, 73728, v229\n"
    "ds_read_b64_tr_b16 v[192:193], v235 offset:0\n"
    "ds_read_b64_tr_b16 v[194:195], v235 offset:3072\n"
    "s_waitcnt lgkmcnt(2)\n"
    "v_mfma_f32_32x32x16_bf16 v[96:111], v[200:203], v[40:43], v[96:111]\n"
    "v_add_u32 v236, 73728, v230\n"
    "ds_read_b64_tr_b16 v[196:197], v236 offset:0\n"
    "ds_read_b64_tr_b16 v[198:199], v236 offset:3072\n"
    "s_add_u32 m0, s69, 25600\n"
    "s_add_u32 s78, s68, 82944\n"
    "buffer_load_dwordx4 v10, s[28:31], s78 offen lds\n"
    "s_waitcnt lgkmcnt(2)\n"
    "v_mfma_f32_32x32x16_bf16 v[112:127], v[192:195], v[40:43], v[112:127]\n"
    "v_add_u32 v237, 73728, v231\n"
    "ds_read_b64_tr_b16 v[200:201], v237 offset:0\n"
    "ds_read_b64_tr_b16 v[202:203], v237 offset:3072\n"
    "s_waitcnt lgkmcnt(2)\n"
    "v_mfma_f32_32x32x16_bf16 v[128:143], v[196:199], v[40:43], v[128:143]\n"
    "ds_read_b64_tr_b16 v[192:193], v232 offset:6144\n"
    "ds_read_b64_tr_b16 v[194:195], v232 offset:9216\n"
    "s_waitcnt lgkmcnt(2)\n"
    "v_mfma_f32_32x32x16_bf16 v[144:159], v[200:203], v[40:43], v[144:159]\n"
    "ds_read_b64_tr_b16 v[196:197], v233 offset:6144\n"
    "ds_read_b64_tr_b16 v[198:199], v233 offset:9216\n"
    "s_waitcnt lgkmcnt(2)\n"
    "v_mfma_f32_32x32x16_bf16 v[64:79], v[192:195], v[44:47], v[64:79]\n"
    "ds_read_b64_tr_b16 v[200:201], v234 offset:6144\n"
    "ds_read_b64_tr_b16 v[202:203], v234 offset:9216\n"
    "s_add_u32 m0, s69, 26624\n"
    "s_add_u32 s78, s68, 82944\n"
    "buffer_load_dwordx4 v11, s[28:31], s78 offen lds\n"
    "s_waitcnt lgkmcnt(2)\n"
    "v_mfma_f32_32x32x16_bf16 v[80:95], v[196:199], v[44:47], v[80:95]\n"
    "ds_read_b64_tr_b16 v[192:193], v235 offset:6144\n"
    "ds_read_b64_tr_b16 v[194:195], v235 offset:9216\n"
    "s_waitcnt lgkmcnt(2)\n"
    "v_mfma_f32_32x32x16_bf16 v[96:111], v[200:203], v[44:47], v[96:111]\n"
    "ds_read_b64_tr_b16 v[196:197], v236 offset:6144\n"
    "ds_read_b64_tr_b16 v[198:199], v236 offset:9216\n"
    "s_waitcnt lgkmcnt(2)\n"
    "v_mfma_f32_32x32x16_bf16 v[112:127], v[192:195], v[44:47], v[112:127]\n"
    "ds_read_b64_tr_b16 v[200:201], v237 offset:6144\n"
    "ds_read_b64_tr_b16 v[202:203], v237 offset:9216\n"
    "s_waitcnt lgkmcnt(2)\n"
    "v_mfma_f32_32x32x16_bf16 v[128:143], v[196:199], v[44:47], v[128:143]\n"
    "s_waitcnt vmcnt(6)\n"
    "s_barrier\n"
    "v_cmp_ne_u64 vcc, v[6:7], v[4:5]\n"
    "s_nop 0\n"
    "s_and_b64 vcc, vcc, s[88:89]\n"
    "s_cbranch_vccz Lok_15\n"
    "s_mov_b32 s86, 0x40000\n"
    "s_add_u32 s78, s98, 176128\n"
    "Lspin_14:\n"
    "s_sleep 1\n"
    "buffer_load_dwordx2 v[6:7], v3, s[20:23], s78 offen sc1\n"
    "s_waitcnt vmcnt(0)\n"
    "s_sub_u32 s86, s86, 1\n"
    "s_cbranch_scc1 Lok_15\n"
    "v_cmp_ne_u64 vcc, v[6:7], v[4:5]\n"
    "s_nop 1\n"
    "s_and_b64 vcc, vcc, s[88:89]\n"
    "s_cbranch_vccnz Lspin_14\n"
    "Lok_15:\n"
    "s_mov_b64 vcc, s[90:91]\n"
    "s_mul_i32 s85, s52, 12288\n"
    "s_lshl_b32 s84, s52, 13\n"
    "s_add_u32 s84, s84, s97\n"
    "s_and_b32 s79, s50, 6\n"
    "s_lshl_b32 s80, s50, 3\n"
    "s_add_u32 s79, s79, s80\n"
    "s_and_b32 s80, s50, 2\n"
    "s_lshl_b32 s80, s80, 1\n"
    "s_lshl_b32 s81, s52, 1\n"
    "s_add_u32 s80, s80, s81\n"
    "s_and_b32 s81, s50, 1\n"
    "s_add_u32 s80, s80, s81\n"
    "s_mul_i32 s80, s80, 18\n"
    "s_add_u32 s80, s80, 16\n"
    "s_sub_u32 s79, s80, s79\n"
    "s_mul_i32 s79, s79, 1152\n"
    "s_add_u32 s68, s68, s79\n"
    "v_and_b32 v15, 31, v1\n"
    "v_lshlrev_b32 v15, 2, v15\n"
    "s_add_u32 s78, s98, 143360\n"
    "buffer_load_dword v15, v15, s[20:23], s78 offen sc1\n"
    "s_add_u32 s78, s84, 0\n"
    "buffer_load_dwordx4 v[56:59], v2, s[20:23], s78 offen sc1\n"
    "s_add_u32 s78, s84, 4096\n"
    "buffer_load_dwordx4 v[60:63], v2, s[20:23], s78 offen sc1\n"
    "s_add_u32 s78, s84, 16384\n"
    "buffer_load_dwordx4 v[48:51], v2, s[20:23], s78 offen sc1\n"
    "s_add_u32 s78, s84, 20480\n"
    "buffer_load_dwordx4 v[52:55], v2, s[20:23], s78 offen sc1\n"
    "s_add_u32 s78, s97, 32768\n"
    "buffer_load_dwordx4 v[24:27], v2, s[20:23], s78 offen sc1\n"
    "s_add_u32 m0, s99, 49152\n"
    "s_bfm_b64 exec, 48, 0\n"
    "buffer_load_dwordx4 v204, s[28:31], s68 offen lds\n"
    "s_mov_b64 exec, -1\n"
    "v_add_u32 v232, 98304, v226\n"
    "ds_read_b64_tr_b16 v[192:193], v232 offset:0\n"
    "ds_read_b64_tr_b16 v[194:195], v232 offset:3072\n"
    "s_waitcnt lgkmcnt(2)\n"
    "v_mfma_f32_32x32x16_bf16 v[144:159], v[200:203], v[44:47], v[144:159]\n"
    "v_add_u32 v233, 98304, v227\n"
    "ds_read_b64_tr_b16 v[196:197], v233 offset:0\n"
    "ds_read_b64_tr_b16 v[198:199], v233 offset:3072\n"
    "s_waitcnt lgkmcnt(2)\n"
    "v_mfma_f32_32x32x16_bf16 v[64:79], v[192:195], v[32:35], v[64:79]\n"
    "v_add_u32 v234, 98304, v228\n"
    "ds_read_b64_tr_b16 v[200:201], v234 offset:0\n"
    "ds_read_b64_tr_b16 v[202:203], v234 offset:3072\n"
    "s_waitcnt lgkmcnt(2)\n"
    "v_mfma_f32_32x32x16_bf16 v[80:95], v[196:199], v[32:35], v[80:95]\n"
    "v_add_u32 v235, 98304, v229\n"
    "ds_read_b64_tr_b16 v[192:193], v235 offset:0\n"
    "ds_read_b64_tr_b16 v[194:195], v235 offset:3072\n"
    "s_waitcnt lgkmcnt(2)\n"
    "v_mfma_f32_32x32x16_bf16 v[96:111], v[200:203], v[32:35], v[96:111]\n"
    "v_add_u32 v236, 98304, v230\n"
    "ds_read_b64_tr_b16 v[196:197], v236 offset:0\n"
    "ds_read_b64_tr_b16 v[198:199], v236 offset:3072\n"
    "s_waitcnt lgkmcnt(2)\n"
    "v_mfma_f32_32x32x16_bf16 v[112:127], v[192:195], v[32:35], v[112:127]\n"
    "v_add_u32 v237, 98304, v231\n"
    "ds_read_b64_tr_b16 v[200:201], v237 offset:0\n"
    "ds_read_b64_tr_b16 v[202:203], v237 offset:3072\n"
    "s_waitcnt lgkmcnt(2)\n"
    "v_mfma_f32_32x32x16_bf16 v[128:143], v[196:199], v[32:35], v[128:143]\n"
    "ds_read_b64_tr_b16 v[192:193], v232 offset:6144\n"
    "ds_read_b64_tr_b16 v[194:195], v232 offset:9216\n"
    "s_waitcnt lgkmcnt(2)\n"
    "v_mfma_f32_32x32x16_bf16 v[144:159], v[200:203], v[32:35], v[144:159]\n"
    "ds_read_b64_tr_b16 v[196:197], v233 offset:6144\n"
    "ds_read_b64_tr_b16 v[198:199], v233 offset:9216\n"
    "s_waitcnt lgkmcnt(2)\n"
    "v_mfma_f32_32x32x16_bf16 v[64:79], v[192:195], v[36:39], v[64:79]\n"
    "ds_read_b64_tr_b16 v[200:201], v234 offset:6144\n"
    "ds_read_b64_tr_b16 v[202:203], v234 offset:9216\n"
    "s_waitcnt lgkmcnt(2)\n"
    "v_mfma_f32_32x32x16_bf16 v[80:95], v[196:199], v[36:39], v[80:95]\n"
    "ds_read_b64_tr_b16 v[192:193], v235 offset:6144\n"
    "ds_read_b64_tr_b16 v[194:195], v235 offset:9216\n"
    "s_waitcnt lgkmcnt(2)\n"
    "v_mfma_f32_32x32x16_bf16 v[96:111], v[200:203], v[36:39], v[96:111]\n"
    "ds_read_b64_tr_b16 v[196:197], v236 offset:6144\n"
    "ds_read_b64_tr_b16 v[198:199], v236 offset:9216\n"
    "s_waitcnt lgkmcnt(2)\n"
    "v_mfma_f32_32x32x16_bf16 v[112:127], v[192:195], v[36:39], v[112:127]\n"
    "ds_read_b64_tr_b16 v[200:201], v237 offset:6144\n"
    "ds_read_b64_tr_b16 v[202:203], v237 offset:9216\n"
    "s_waitcnt lgkmcnt(2)\n"
    "v_mfma_f32_32x32x16_bf16 v[128:143], v[196:199], v[36:39], v[128:143]\n"
    "s_waitcnt vmcnt(0) lgkmcnt(0)\n"
    "s_barrier\n"
    "v_sub_f32 v15, 1.0, v15\n"
    "s_mul_i32 s78, s50, 6144\n"
    "s_add_u32 s78, s78, 73728\n"
    "v_add_u32 v8, s78, v2\n"
    "s_xor_b32 s79, s50, 4\n"
    "s_mul_i32 s79, s79, 6144\n"
    "s_add_u32 s79, s79, 73728\n"
    "v_add_u32 v9, s79, v2\n"
    "v_add_u32 v235, 122880, v229\n"
    "ds_read_b64_tr_b16 v[192:193], v235 offset:0\n"
    "ds_read_b64_tr_b16 v[194:195], v235 offset:3072\n"
    "v_mfma_f32_32x32x16_bf16 v[144:159], v[200:203], v[36:39], v[144:159]\n"
    "ds_read_b64_tr_b16 v[196:197], v235 offset:6144\n"
    "ds_read_b64_tr_b16 v[198:199], v235 offset:9216\n"
    "s_waitcnt lgkmcnt(2)\n"
    "v_mfma_f32_32x32x16_bf16 v[112:127], v[192:195], v[16:19], v[112:127]\n"
    "v_add_u32 v236, 122880, v230\n"
    "ds_read_b64_tr_b16 v[200:201], v236 offset:0\n"
    "ds_read_b64_tr_b16 v[202:203], v236 offset:3072\n"
    "s_waitcnt lgkmcnt(2)\n"
    "v_mfma_f32_32x32x16_bf16 v[112:127], v[196:199], v[20:23], v[112:127]\n"
    "ds_read_b64_tr_b16 v[192:193], v236 offset:6144\n"
    "ds_read_b64_tr_b16 v[194:195], v236 offset:9216\n"
    "s_waitcnt lgkmcnt(2)\n"
    "v_mfma_f32_32x32x16_bf16 v[128:143], v[200:203], v[16:19], v[128:143]\n"
    "v_add_u32 v237, 122880, v231\n"
    "ds_read_b64_tr_b16 v[196:197], v237 offset:0\n"
    "ds_read_b64_tr_b16 v[198:199], v237 offset:3072\n"
    "s_waitcnt lgkmcnt(2)\n"
    "v_mfma_f32_32x32x16_bf16 v[128:143], v[192:195], v[20:23], v[128:143]\n"
    "ds_read_b64_tr_b16 v[200:201], v237 offset:6144\n"
    "ds_read_b64_tr_b16 v[202:203], v237 offset:9216\n"
    "s_nop 0\n"
    "v_mul_f32 v112, v15, v112\n"
    "v_mul_f32 v113, v15, v113\n"
    "v_mul_f32 v114, v15, v114\n"
    "v_mul_f32 v115, v15, v115\n"
    "v_mul_f32 v116, v15, v116\n"
    "v_mul_f32 v117, v15, v117\n"
    "s_waitcnt lgkmcnt(2)\n"
    "v_mfma_f32_32x32x16_bf16 v[144:159], v[196:199], v[16:19], v[144:159]\n"
    "v_add_u32 v235, 0, v229\n"
    "ds_read_b64_tr_b16 v[192:193], v235 offset:0\n"
    "ds_read_b64_tr_b16 v[194:195], v235 offset:3072\n"
    "v_mul_f32 v118, v15, v118\n"
    "v_mul_f32 v119, v15, v119\n"
    "v_mul_f32 v120, v15, v120\n"
    "v_mul_f32 v121, v15, v121\n"
    "v_mul_f32 v122, v15, v122\n"
    "v_mul_f32 v123, v15, v123\n"
    "s_waitcnt lgkmcnt(2)\n"
    "v_mfma_f32_32x32x16_bf16 v[144:159], v[200:203], v[20:23], v[144:159]\n"
    "ds_read_b64_tr_b16 v[196:197], v235 offset:6144\n"
    "ds_read_b64_tr_b16 v[198:199], v235 offset:9216\n"
    "v_mul_f32 v124, v15, v124\n"
    "v_mul_f32 v125, v15, v125\n"
    "v_mul_f32 v126, v15, v126\n"
    "v_mul_f32 v127, v15, v127\n"
    "v_mul_f32 v128, v15, v128\n"
    "v_mul_f32 v129, v15, v129\n"
    "s_waitcnt lgkmcnt(2)\n"
    "v_mfma_f32_32x32x16_bf16 v[112:127], v[192:195], v[56:59], v[112:127]\n"
    "v_add_u32 v235, 24576, v229\n"
    "ds_read_b64_tr_b16 v[200:201], v235 offset:0\n"
    "ds_read_b64_tr_b16 v[202:203], v235 offset:3072\n"
    "v_mul_f32 v130, v15, v130\n"
    "v_mul_f32 v131, v15, v131\n"
    "v_mul_f32 v132, v15, v132\n"
    "v_mul_f32 v133, v15, v133\n"
    "v_mul_f32 v134, v15, v134\n"
    "v_mul_f32 v135, v15, v135\n"
    "s_waitcnt lgkmcnt(2)\n"
    "v_mfma_f32_32x32x16_bf16 v[112:127], v[196:199], v[60:63], v[112:127]\n"
    "ds_read_b64_tr_b16 v[192:193], v235 offset:6144\n"
    "ds_read_b64_tr_b16 v[194:195], v235 offset:9216\n"
    "v_mul_f32 v136, v15, v136\n"
    "v_mul_f32 v137, v15, v137\n"
    "v_mul_f32 v138, v15, v138\n"
    "v_mul_f32 v139, v15, v139\n"
    "v_mul_f32 v140, v15, v140\n"
    "v_mul_f32 v141, v15, v141\n"
    "s_waitcnt lgkmcnt(2)\n"
    "v_mfma_f32_32x32x16_bf16 v[112:127], v[200:203], v[48:51], v[112:127]\n"
    "v_add_u32 v232, 122880, v226\n"
    "ds_read_b64_tr_b16 v[196:197], v232 offset:0\n"
    "ds_read_b64_tr_b16 v[198:199], v232 offset:3072\n"
    "v_mul_f32 v142, v15, v142\n"
    "v_mul_f32 v143, v15, v143\n"
    "v_mul_f32 v144, v15, v144\n"
    "v_mul_f32 v145, v15, v145\n"
    "v_mul_f32 v146, v15, v146\n"
    "v_mul_f32 v147, v15, v147\n"
    "s_waitcnt lgkmcnt(2)\n"
    "v_mfma_f32_32x32x16_bf16 v[112:127], v[192:195], v[52:55], v[112:127]\n"
    "ds_read_b64_tr_b16 v[200:201], v232 offset:6144\n"
    "ds_read_b64_tr_b16 v[202:203], v232 offset:9216\n"
    "v_mul_f32 v148, v15, v148\n"
    "v_mul_f32 v149, v15, v149\n"
    "v_mul_f32 v150, v15, v150\n"
    "v_mul_f32 v151, v15, v151\n"
    "v_mul_f32 v152, v15, v152\n"
    "v_mul_f32 v153, v15, v153\n"
    "s_waitcnt lgkmcnt(2)\n"
    "v_mfma_f32_32x32x16_bf16 v[64:79], v[196:199], v[16:19], v[64:79]\n"
    "v_add_u32 v236, 0, v230\n"
    "ds_read_b64_tr_b16 v[192:193], v236 offset:0\n"
    "ds_read_b64_tr_b16 v[194:195], v236 offset:3072\n"
    "v_mul_f32 v154, v15, v154\n"
    "v_mul_f32 v155, v15, v155\n"
    "v_mul_f32 v156, v15, v156\n"
    "v_mul_f32 v157, v15, v157\n"
    "v_mul_f32 v158, v15, v158\n"
    "v_mul_f32 v159, v15, v159\n"
    "s_waitcnt lgkmcnt(2)\n"
    "v_mfma_f32_32x32x16_bf16 v[64:79], v[200:203], v[20:23], v[64:79]\n"
    "ds_read_b64_tr_b16 v[196:197], v236 offset:6144\n"
    "ds_read_b64_tr_b16 v[198:199], v236 offset:9216\n"
    "v_cvt_pk_bf16_f32 v112, v112, v113\n"
    "v_cvt_pk_bf16_f32 v113, v114, v115\n"
    "v_cvt_pk_bf16_f32 v114, v116, v117\n"
    "v_cvt_pk_bf16_f32 v115, v118, v119\n"
    "s_waitcnt lgkmcnt(2)\n"
    "v_mfma_f32_32x32x16_bf16 v[128:143], v[192:195], v[56:59], v[128:143]\n"
    "v_add_u32 v236, 24576, v230\n"
    "ds_read_b64_tr_b16 v[200:201], v236 offset:0\n"
    "ds_read_b64_tr_b16 v[202:203], v236 offset:3072\n"
    "v_cvt_pk_bf16_f32 v116, v120, v121\n"
    "v_cvt_pk_bf16_f32 v117, v122, v123\n"
    "v_cvt_pk_bf16_f32 v118, v124, v125\n"
    "v_cvt_pk_bf16_f32 v119, v126, v127\n"
    "ds_write_b128 v8, v[112:115] offset:0\n"
    "s_waitcnt lgkmcnt(3)\n"
    "v_mfma_f32_32x32x16_bf16 v[128:143], v[196:199], v[60:63], v[128:143]\n"
    "ds_read_b64_tr_b16 v[192:193], v236 offset:6144\n"
    "ds_read_b64_tr_b16 v[194:195], v236 offset:9216\n"
    "v_mul_f32 v64, v15, v64\n"
    "v_mul_f32 v65, v15, v65\n"
    "v_mul_f32 v66, v15, v66\n"
    "v_mul_f32 v67, v15, v67\n"
    "v_mul_f32 v68, v15, v68\n"
    "v_mul_f32 v69, v15, v69\n"
    "ds_write_b128 v8, v[116:119] offset:1024\n"
    "s_waitcnt lgkmcnt(4)\n"
    "v_mfma_f32_32x32x16_bf16 v[128:143], v[200:203], v[48:51], v[128:143]\n"
    "v_add_u32 v233, 122880, v227\n"
    "ds_read_b64_tr_b16 v[196:197], v233 offset:0\n"
    "ds_read_b64_tr_b16 v[198:199], v233 offset:3072\n"
    "v_mul_f32 v70, v15, v70\n"
    "v_mul_f32 v71, v15, v71\n"
    "v_mul_f32 v72, v15, v72\n"
    "v_mul_f32 v73, v15, v73\n"
    "v_mul_f32 v74, v15, v74\n"
    "v_mul_f32 v75, v15, v75\n"
    "s_waitcnt lgkmcnt(3)\n"
    "v_mfma_f32_32x32x16_bf16 v[128:143], v[192:195], v[52:55], v[128:143]\n"
    "ds_read_b64_tr_b16 v[200:201], v233 offset:6144\n"
    "ds_read_b64_tr_b16 v[202:203], v233 offset:9216\n"
    "v_mul_f32 v76, v15, v76\n"
    "v_mul_f32 v77, v15, v77\n"
    "v_mul_f32 v78, v15, v78\n"
    "v_mul_f32 v79, v15, v79\n"
    "s_waitcnt lgkmcnt(2)\n"
    "v_mfma_f32_32x32x16_bf16 v[80:95], v[196:199], v[16:19], v[80:95]\n"
    "v_add_u32 v237, 0, v231\n"
    "ds_read_b64_tr_b16 v[192:193], v237 offset:0\n"
    "ds_read_b64_tr_b16 v[194:195], v237 offset:3072\n"
    "s_waitcnt lgkmcnt(2)\n"
    "v_mfma_f32_32x32x16_bf16 v[80:95], v[200:203], v[20:23], v[80:95]\n"
    "ds_read_b64_tr_b16 v[196:197], v237 offset:6144\n"
    "ds_read_b64_tr_b16 v[198:199], v237 offset:9216\n"
    "v_cvt_pk_bf16_f32 v128, v128, v129\n"
    "v_cvt_pk_bf16_f32 v129, v130, v131\n"
    "v_cvt_pk_bf16_f32 v130, v132, v133\n"
    "v_cvt_pk_bf16_f32 v131, v134, v135\n"
    "s_waitcnt lgkmcnt(2)\n"
    "v_mfma_f32_32x32x16_bf16 v[144:159], v[192:195], v[56:59], v[144:159]\n"
    "v_add_u32 v237, 24576, v231\n"
    "ds_read_b64_tr_b16 v[200:201], v237 offset:0\n"
    "ds_read_b64_tr_b16 v[202:203], v237 offset:3072\n"
    "v_cvt_pk_bf16_f32 v132, v136, v137\n"
    "v_cvt_pk_bf16_f32 v133, v138, v139\n"
    "v_cvt_pk_bf16_f32 v134, v140, v141\n"
    "v_cvt_pk_bf16_f32 v135, v142, v143\n"
    "ds_write_b128 v8, v[128:131] offset:2048\n"
    "s_waitcnt lgkmcnt(3)\n"
    "v_mfma_f32_32x32x16_bf16 v[144:159], v[196:199], v[60:63], v[144:159]\n"
    "ds_read_b64_tr_b16 v[192:193], v237 offset:6144\n"
    "ds_read_b64_tr_b16 v[194:195], v237 offset:9216\n"
    "v_mul_f32 v80, v15, v80\n"
    "v_mul_f32 v81, v15, v81\n"
    "v_mul_f32 v82, v15, v82\n"
    "v_mul_f32 v83, v15, v83\n"
    "v_mul_f32 v84, v15, v84\n"
    "v_mul_f32 v85, v15, v85\n"
    "ds_write_b128 v8, v[132:135] offset:3072\n"
    "s_waitcnt lgkmcnt(4)\n"
    "v_mfma_f32_32x32x16_bf16 v[144:159], v[200:203], v[48:51], v[144:159]\n"
    "v_add_u32 v234, 122880, v228\n"
    "ds_read_b64_tr_b16 v[196:197], v234 offset:0\n"
    "ds_read_b64_tr_b16 v[198:199], v234 offset:3072\n"
    "v_mul_f32 v86, v15, v86\n"
    "v_mul_f32 v87, v15, v87\n"
    "v_mul_f32 v88, v15, v88\n"
    "v_mul_f32 v89, v15, v89\n"
    "v_mul_f32 v90, v15, v90\n"
    "v_mul_f32 v91, v15, v91\n"
    "s_waitcnt lgkmcnt(3)\n"
    "v_mfma_f32_32x32x16_bf16 v[144:159], v[192:195], v[52:55], v[144:159]\n"
    "ds_read_b64_tr_b16 v[200:201], v234 offset:6144\n"
    "ds_read_b64_tr_b16 v[202:203], v234 offset:9216\n"
    "v_mul_f32 v92, v15, v92\n"
    "v_mul_f32 v93, v15, v93\n"
    "v_mul_f32 v94, v15, v94\n"
    "v_mul_f32 v95, v15, v95\n"
    "s_waitcnt lgkmcnt(2)\n"
    "v_mfma_f32_32x32x16_bf16 v[96:111], v[196:199], v[16:19], v[96:111]\n"
    "v_add_u32 v232, 0, v226\n"
    "ds_read_b64_tr_b16 v[192:193], v232 offset:0\n"
    "ds_read_b64_tr_b16 v[194:195], v232 offset:3072\n"
    "s_waitcnt lgkmcnt(2)\n"
    "v_mfma_f32_32x32x16_bf16 v[96:111], v[200:203], v[20:23], v[96:111]\n"
    "ds_read_b64_tr_b16 v[196:197], v232 offset:6144\n"
    "ds_read_b64_tr_b16 v[198:199], v232 offset:9216\n"
    "v_cvt_pk_bf16_f32 v144, v144, v145\n"
    "v_cvt_pk_bf16_f32 v145, v146, v147\n"
    "v_cvt_pk_bf16_f32 v146, v148, v149\n"
    "v_cvt_pk_bf16_f32 v147, v150, v151\n"
    "s_waitcnt lgkmcnt(2)\n"
    "v_mfma_f32_32x32x16_bf16 v[64:79], v[192:195], v[56:59], v[64:79]\n"
    "v_add_u32 v232, 24576, v226\n"
    "ds_read_b64_tr_b16 v[200:201], v232 offset:0\n"
    "ds_read_b64_tr_b16 v[202:203], v232 offset:3072\n"
    "v_cvt_pk_bf16_f32 v148, v152, v153\n"
    "v_cvt_pk_bf16_f32 v149, v154, v155\n"
    "v_cvt_pk_bf16_f32 v150, v156, v157\n"
    "v_cvt_pk_bf16_f32 v151, v158, v159\n"
    "ds_write_b128 v8, v[144:147] offset:4096\n"
    "s_waitcnt lgkmcnt(3)\n"
    "v_mfma_f32_32x32x16_bf16 v[64:79], v[196:199], v[60:63], v[64:79]\n"
    "ds_read_b64_tr_b16 v[192:193], v232 offset:6144\n"
    "ds_read_b64_tr_b16 v[194:195], v232 offset:9216\n"
    "v_mul_f32 v96, v15, v96\n"
    "v_mul_f32 v97, v15, v97\n"
    "v_mul_f32 v98, v15, v98\n"
    "v_mul_f32 v99, v15, v99\n"
    "v_mul_f32 v100, v15, v100\n"
    "v_mul_f32 v101, v15, v101\n"
    "ds_write_b128 v8, v[148:151] offset:5120\n"
    "s_waitcnt lgkmcnt(4)\n"
    "v_mfma_f32_32x32x16_bf16 v[64:79], v[200:203], v[48:51], v[64:79]\n"
    "v_add_u32 v232, 49152, v226\n"
    "v_subrev_u32 v232, s85, v232\n"
    "ds_read_b64_tr_b16 v[196:197], v232 offset:0\n"
    "ds_read_b64_tr_b16 v[198:199], v232 offset:3072\n"
    "v_mul_f32 v102, v15, v102\n"
    "v_mul_f32 v103, v15, v103\n"
    "v_mul_f32 v104, v15, v104\n"
    "v_mul_f32 v105, v15, v105\n"
    "v_mul_f32 v106, v15, v106\n"
    "v_mul_f32 v107, v15, v107\n"
    "s_waitcnt lgkmcnt(3)\n"
    "v_mfma_f32_32x32x16_bf16 v[64:79], v[192:195], v[52:55], v[64:79]\n"
    "v_add_u32 v233, 0, v227\n"
    "ds_read_b64_tr_b16 v[200:201], v233 offset:0\n"
    "ds_read_b64_tr_b16 v[202:203], v233 offset:3072\n"
    "v_mul_f32 v108, v15, v108\n"
    "v_mul_f32 v109, v15, v109\n"
    "v_mul_f32 v110, v15, v110\n"
    "v_mul_f32 v111, v15, v111\n"
    "s_waitcnt lgkmcnt(2)\n"
    "v_mfma_f32_32x32x16_bf16 v[64:79], v[196:199], v[24:27], v[64:79]\n"
    "ds_read_b64_tr_b16 v[192:193], v233 offset:6144\n"
    "ds_read_b64_tr_b16 v[194:195], v233 offset:9216\n"
    "s_waitcnt lgkmcnt(2)\n"
    "v_mfma_f32_32x32x16_bf16 v[80:95], v[200:203], v[56:59], v[80:95]\n"
    "v_add_u32 v233, 24576, v227\n"
    "ds_read_b64_tr_b16 v[196:197], v233 offset:0\n"
    "ds_read_b64_tr_b16 v[198:199], v233 offset:3072\n"
    "s_waitcnt lgkmcnt(2)\n"
    "v_mfma_f32_32x32x16_bf16 v[80:95], v[192:195], v[60:63], v[80:95]\n"
    "ds_read_b64_tr_b16 v[200:201], v233 offset:6144\n"
    "ds_read_b64_tr_b16 v[202:203], v233 offset:9216\n"
    "s_waitcnt lgkmcnt(2)\n"
    "v_mfma_f32_32x32x16_bf16 v[80:95], v[196:199], v[48:51], v[80:95]\n"
    "v_add_u32 v233, 49152, v227\n"
    "v_subrev_u32 v233, s85, v233\n"
    "ds_read_b64_tr_b16 v[192:193], v233 offset:0\n"
    "ds_read_b64_tr_b16 v[194:195], v233 offset:3072\n"
    "s_waitcnt lgkmcnt(2)\n"
    "v_mfma_f32_32x32x16_bf16 v[80:95], v[200:203], v[52:55], v[80:95]\n"
    "v_add_u32 v234, 0, v228\n"
    "ds_read_b64_tr_b16 v[196:197], v234 offset:0\n"
    "ds_read_b64_tr_b16 v[198:199], v234 offset:3072\n"
    "s_waitcnt lgkmcnt(2)\n"
    "v_mfma_f32_32x32x16_bf16 v[80:95], v[192:195], v[24:27], v[80:95]\n"
    "ds_read_b64_tr_b16 v[200:201], v234 offset:6144\n"
    "ds_read_b64_tr_b16 v[202:203], v234 offset:9216\n"
    "s_waitcnt lgkmcnt(2)\n"
    "v_mfma_f32_32x32x16_bf16 v[96:111], v[196:199], v[56:59], v[96:111]\n"
    "v_add_u32 v234, 24576, v228\n"
    "ds_read_b64_tr_b16 v[192:193], v234 offset:0\n"
    "ds_read_b64_tr_b16 v[194:195], v234 offset:3072\n"
    "s_waitcnt lgkmcnt(2)\n"
    "v_mfma_f32_32x32x16_bf16 v[96:111], v[200:203], v[60:63], v[96:111]\n"
    "ds_read_b64_tr_b16 v[196:197], v234 offset:6144\n"
    "ds_read_b64_tr_b16 v[198:199], v234 offset:9216\n"
    "s_waitcnt lgkmcnt(2)\n"
    "v_mfma_f32_32x32x16_bf16 v[96:111], v[192:195], v[48:51], v[96:111]\n"
    "v_add_u32 v234, 49152, v228\n"
    "v_subrev_u32 v234, s85, v234\n"
    "ds_read_b64_tr_b16 v[200:201], v234 offset:0\n"
    "ds_read_b64_tr_b16 v[202:203], v234 offset:3072\n"
    "s_waitcnt lgkmcnt(2)\n"
    "v_mfma_f32_32x32x16_bf16 v[96:111], v[196:199], v[52:55], v[96:111]\n"
    "s_waitcnt lgkmcnt(0)\n"
    "v_mfma_f32_32x32x16_bf16 v[96:111], v[200:203], v[24:27], v[96:111]\n"
    "s_mul_i32 s96, s54, 3\n"
    "s_add_u32 s96, s96, s55\n"
    "s_lshl_b32 s96, s96, 2\n"
    "s_add_u32 s96, s96, s51\n"
    "s_mul_i32 s96, s96, 6\n"
    "s_add_u32 s33, s96, s71\n"
    "s_lshl_b32 s32, s33, 13\n"
    "s_add_u32 s32, s32, 23330816\n"
    "s_lshl_b32 s33, s33, 7\n"
    "s_add_u32 s33, s33, 262144\n"
    "s_add_u32 s35, s96, s72\n"
    "s_lshl_b32 s34, s35, 13\n"
    "s_add_u32 s34, s34, 23330816\n"
    "s_lshl_b32 s35, s35, 7\n"
    "s_add_u32 s35, s35, 262144\n"
    "s_add_u32 s37, s96, s73\n"
    "s_lshl_b32 s36, s37, 13\n"
    "s_add_u32 s36, s36, 23330816\n"
    "s_lshl_b32 s37, s37, 7\n"
    "s_add_u32 s37, s37, 262144\n"
    "s_lshl_b32 s83, s53, 11\n"
    "s_lshl_b32 s84, s53, 3\n"
    "s_barrier\n"
    "ds_read_b128 v[168:171], v9 offset:2048\n"
    "ds_read_b128 v[172:175], v9 offset:3072\n"
    "ds_read_b128 v[176:179], v9 offset:4096\n"
    "ds_read_b128 v[180:183], v9 offset:5120\n"
    "ds_read_b128 v[160:163], v9 offset:0\n"
    "ds_read_b128 v[164:167], v9 offset:1024\n"
    "v_and_b32 v208, 31, v1\n"
    "v_lshrrev_b32 v209, 5, v1\n"
    "v_lshlrev_b32 v209, 2, v209\n"
    "v_sub_u32 v208, v208, v209\n"
    "v_and_b32 v209, 1, v1\n"
    "v_lshlrev_b32 v209, 4, v209\n"
    "v_mov_b32 v210, 0x3f80\n"
    "v_lshlrev_b32 v209, v209, v210\n"
    "v_and_b32 v208, -2, v208\n"
    "v_cmp_eq_u32 vcc, 0, v208\n"
    "s_nop 1\n"
    "v_cndmask_b32 v192, 0, v209, vcc\n"
    "v_cmp_eq_u32 vcc, 2, v208\n"
    "s_nop 1\n"
    "v_cndmask_b32 v193, 0, v209, vcc\n"
    "v_cmp_eq_u32 vcc, 8, v208\n"
    "s_nop 1\n"
    "v_cndmask_b32 v194, 0, v209, vcc\n"
    "v_cmp_eq_u32 vcc, 10, v208\n"
    "s_nop 1\n"
    "v_cndmask_b32 v195, 0, v209, vcc\n"
    "v_cmp_eq_u32 vcc, 16, v208\n"
    "s_nop 1\n"
    "v_cndmask_b32 v196, 0, v209, vcc\n"
    "v_cmp_eq_u32 vcc, 18, v208\n"
    "s_nop 1\n"
    "v_cndmask_b32 v197, 0, v209, vcc\n"
    "v_cmp_eq_u32 vcc, 24, v208\n"
    "s_nop 1\n"
    "v_cndmask_b32 v198, 0, v209, vcc\n"
    "v_cmp_eq_u32 vcc, 26, v208\n"
    "s_nop 1\n"
    "v_cndmask_b32 v199, 0, v209, vcc\n"
    "s_waitcnt lgkmcnt(5)\n"
    "v_mfma_f32_32x32x16_bf16 v[80:95], v[192:195], v[168:171], v[80:95]\n"
    "s_waitcnt lgkmcnt(4)\n"
    "v_mfma_f32_32x32x16_bf16 v[80:95], v[196:199], v[172:175], v[80:95]\n"
    "s_waitcnt lgkmcnt(3)\n"
    "v_mfma_f32_32x32x16_bf16 v[96:111], v[192:195], v[176:179], v[96:111]\n"
    "s_waitcnt lgkmcnt(2)\n"
    "v_mfma_f32_32x32x16_bf16 v[96:111], v[196:199], v[180:183], v[96:111]\n"
    "s_waitcnt lgkmcnt(1)\n"
    "v_mfma_f32_32x32x16_bf16 v[64:79], v[192:195], v[160:163], v[64:79]\n"
    "s_waitcnt lgkmcnt(0)\n"
    "v_mfma_f32_32x32x16_bf16 v[64:79], v[196:199], v[164:167], v[64:79]\n"
    "s_nop 3\n"
    "v_cvt_pk_bf16_f32 v80, v80, v81\n"
    "v_cvt_pk_bf16_f32 v81, v82, v83\n"
    "v_cvt_pk_bf16_f32 v82, v84, v85\n"
    "v_cvt_pk_bf16_f32 v83, v86, v87\n"
    "v_cvt_pk_bf16_f32 v84, v88, v89\n"
    "v_cvt_pk_bf16_f32 v85, v90, v91\n"
    "v_cvt_pk_bf16_f32 v86, v92, v93\n"
    "v_cvt_pk_bf16_f32 v87, v94, v95\n"
    "s_add_u32 s79, s34, s83\n"
    "buffer_store_dwordx4 v[80:83], v2, s[20:23], s79 offen sc1\n"
    "buffer_store_dwordx4 v[84:87], v2, s[20:23], s79 offen offset:1024 sc1\n"
    "v_cvt_pk_bf16_f32 v96, v96, v97\n"
    "v_cvt_pk_bf16_f32 v97, v98, v99\n"
    "v_cvt_pk_bf16_f32 v98, v100, v101\n"
    "v_cvt_pk_bf16_f32 v99, v102, v103\n"
    "v_cvt_pk_bf16_f32 v100, v104, v105\n"
    "v_cvt_pk_bf16_f32 v101, v106, v107\n"
    "v_cvt_pk_bf16_f32 v102, v108, v109\n"
    "v_cvt_pk_bf16_f32 v103, v110, v111\n"
    "s_add_u32 s79, s36, s83\n"
    "buffer_store_dwordx4 v[96:99], v2, s[20:23], s79 offen sc1\n"
    "buffer_store_dwordx4 v[100:103], v2, s[20:23], s79 offen offset:1024 sc1\n"
    "s_cmp_eq_u32 s77, 1\n"
    "s_cbranch_scc1 Lrecv_16\n"
    "v_cvt_pk_bf16_f32 v64, v64, v65\n"
    "v_cvt_pk_bf16_f32 v65, v66, v67\n"
    "v_cvt_pk_bf16_f32 v66, v68, v69\n"
    "v_cvt_pk_bf16_f32 v67, v70, v71\n"
    "v_cvt_pk_bf16_f32 v68, v72, v73\n"
    "v_cvt_pk_bf16_f32 v69, v74, v75\n"
    "v_cvt_pk_bf16_f32 v70, v76, v77\n"
    "v_cvt_pk_bf16_f32 v71, v78, v79\n"
    "s_add_u32 s79, s32, s83\n"
    "buffer_store_dwordx4 v[64:67], v2, s[20:23], s79 offen sc1\n"
    "buffer_store_dwordx4 v[68:71], v2, s[20:23], s79 offen offset:1024 sc1\n"
    "s_waitcnt vmcnt(0)\n"
    "s_add_u32 s79, s35, s84\n"
    "s_add_u32 s78, s79, 0\n"
    "s_mov_b64 exec, 1\n"
    "buffer_store_dwordx2 v[4:5], off, s[20:23], s78 sc1\n"
    "s_mov_b64 exec, -1\n"
    "s_add_u32 s79, s37, s84\n"
    "s_add_u32 s78, s79, 0\n"
    "s_mov_b64 exec, 1\n"
    "buffer_store_dwordx2 v[4:5], off, s[20:23], s78 sc1\n"
    "s_mov_b64 exec, -1\n"
    "s_add_u32 s79, s33, s84\n"
    "s_add_u32 s78, s79, 0\n"
    "s_mov_b64 exec, 1\n"
    "buffer_store_dwordx2 v[4:5], off, s[20:23], s78 sc1\n"
    "s_mov_b64 exec, -1\n"
    "s_branch Ldone_17\n"
    "Lrecv_16:\n"
    "s_lshl_b32 s79, 1, s53\n"
    "s_andn2_b32 s88, 15, s79\n"
    "s_mov_b32 s24, s46\n"
    "s_and_b32 s25, s47, 0xffff\n"
    "s_mov_b32 s26, 2359296\n"
    "s_mov_b32 s27, 0x20000\n"
    "v_and_b32 v8, 31, v1\n"
    "s_mul_i32 s79, s55, 6\n"
    "s_add_u32 s79, s79, s71\n"
    "s_lshl_b32 s80, s54, 7\n"
    "s_lshl_b32 s81, s51, 5\n"
    "s_add_u32 s80, s80, s81\n"
    "v_add_u32 v8, s80, v8\n"
    "v_mul_u32_u24 v8, 1152, v8\n"
    "s_lshl_b32 s79, s79, 6\n"
    "v_add_u32 v8, s79, v8\n"
    "v_lshrrev_b32 v9, 5, v1\n"
    "v_lshl_add_u32 v8, v9, 4, v8\n"
    "s_cmp_ge_u32 0, s53\n"
    "s_cselect_b32 s38, 2048, 0\n"
    "s_add_u32 s38, s38, s32\n"
    "s_add_u32 s38, s38, 0\n"
    "s_cmp_ge_u32 1, s53\n"
    "s_cselect_b32 s39, 2048, 0\n"
    "s_add_u32 s39, s39, s32\n"
    "s_add_u32 s39, s39, 2048\n"
    "s_cmp_ge_u32 2, s53\n"
    "s_cselect_b32 s40, 2048, 0\n"
    "s_add_u32 s40, s40, s32\n"
    "s_add_u32 s40, s40, 4096\n"
    "s_waitcnt vmcnt(0)\n"
    "s_add_u32 s79, s35, s84\n"
    "s_add_u32 s78, s79, 0\n"
    "s_mov_b64 exec, 1\n"
    "buffer_store_dwordx2 v[4:5], off, s[20:23], s78 sc1\n"
    "s_mov_b64 exec, -1\n"
    "s_add_u32 s79, s37, s84\n"
    "s_add_u32 s78, s79, 0\n"
    "s_mov_b64 exec, 1\n"
    "buffer_store_dwordx2 v[4:5], off, s[20:23], s78 sc1\n"
    "s_mov_b64 exec, -1\n"
    "s_add_u32 s78, s33, 0\n"
    "buffer_load_dwordx2 v[6:7], v3, s[20:23], s78 offen sc1\n"
    "s_waitcnt vmcnt(0)\n"
    "v_cmp_ne_u64 vcc, v[6:7], v[4:5]\n"
    "s_nop 0\n"
    "s_and_b64 vcc, vcc, s[88:89]\n"
    "s_cbranch_vccz Lok_19\n"
    "s_mov_b32 s86, 0x40000\n"
    "s_add_u32 s78, s33, 0\n"
    "Lspin_18:\n"
    "s_sleep 1\n"
    "buffer_load_dwordx2 v[6:7], v3, s[20:23], s78 offen sc1\n"
    "s_waitcnt vmcnt(0)\n"
    "s_sub_u32 s86, s86, 1\n"
    "s_cbranch_scc1 Lok_19\n"
    "v_cmp_ne_u64 vcc, v[6:7], v[4:5]\n"
    "s_nop 1\n"
    "s_and_b64 vcc, vcc, s[88:89]\n"
    "s_cbranch_vccnz Lspin_18\n"
    "Lok_19:\n"
    "s_mov_b64 vcc, s[90:91]\n"
    "buffer_load_dwordx4 v[160:163], v2, s[20:23], s38 offen offset:0 sc1\n"
    "buffer_load_dwordx4 v[164:167], v2, s[20:23], s38 offen offset:1024 sc1\n"
    "buffer_load_dwordx4 v[168:171], v2, s[20:23], s39 offen offset:0 sc1\n"
    "buffer_load_dwordx4 v[172:175], v2, s[20:23], s39 offen offset:1024 sc1\n"
    "buffer_load_dwordx4 v[176:179], v2, s[20:23], s40 offen offset:0 sc1\n"
    "buffer_load_dwordx4 v[180:183], v2, s[20:23], s40 offen offset:1024 sc1\n"
    "s_waitcnt vmcnt(5)\n"
    "v_mfma_f32_32x32x16_bf16 v[64:79], v[192:195], v[160:163], v[64:79]\n"
    "s_waitcnt vmcnt(4)\n"
    "v_mfma_f32_32x32x16_bf16 v[64:79], v[196:199], v[164:167], v[64:79]\n"
    "s_waitcnt vmcnt(3)\n"
    "v_mfma_f32_32x32x16_bf16 v[64:79], v[192:195], v[168:171], v[64:79]\n"
    "s_waitcnt vmcnt(2)\n"
    "v_mfma_f32_32x32x16_bf16 v[64:79], v[196:199], v[172:175], v[64:79]\n"
    "s_waitcnt vmcnt(1)\n"
    "v_mfma_f32_32x32x16_bf16 v[64:79], v[192:195], v[176:179], v[64:79]\n"
    "s_waitcnt vmcnt(0)\n"
    "v_mfma_f32_32x32x16_bf16 v[64:79], v[196:199], v[180:183], v[64:79]\n"
    "s_nop 11\n"
    "v_cvt_pk_bf16_f32 v64, v64, v65\n"
    "v_cvt_pk_bf16_f32 v65, v66, v67\n"
    "v_cvt_pk_bf16_f32 v66, v68, v69\n"
    "v_cvt_pk_bf16_f32 v67, v70, v71\n"
    "v_cvt_pk_bf16_f32 v68, v72, v73\n"
    "v_cvt_pk_bf16_f32 v69, v74, v75\n"
    "v_cvt_pk_bf16_f32 v70, v76, v77\n"
    "v_cvt_pk_bf16_f32 v71, v78, v79\n"
    "v_permlane32_swap_b32 v64, v66\n"
    "v_permlane32_swap_b32 v65, v67\n"
    "buffer_store_dwordx4 v[64:67], v8, s[24:27], 0 offen offset:0\n"
    "v_permlane32_swap_b32 v68, v70\n"
    "v_permlane32_swap_b32 v69, v71\n"
    "buffer_store_dwordx4 v[68:71], v8, s[24:27], 0 offen offset:32\n"
    "s_or_b32 s79, s53, s55\n"
    "s_or_b32 s79, s79, s50\n"
    "s_cmp_eq_u32 s79, 0\n"
    "s_cbranch_scc0 Lnoep_20\n"
    "s_lshl_b32 s79, s54, 8\n"
    "s_add_u32 s78, s79, 0\n"
    "s_mov_b64 exec, 1\n"
    "buffer_store_dwordx2 v[4:5], off, s[20:23], s78 sc1\n"
    "s_mov_b64 exec, -1\n"
    "Lnoep_20:\n"
    "Ldone_17:\n"
    "s_branch Lend_2\n"
    "Latt_1:\n"
    "v_mov_b32 v11, v224\n"
    "v_mov_b32 v12, v225\n"
    "s_mov_b32 s12, s34\n"
    "s_and_b32 s13, s35, 0xffff\n"
    "s_mov_b32 s14, 1990656\n"
    "s_mov_b32 s15, 0x20000\n"
    "s_mov_b32 s28, s44\n"
    "s_and_b32 s29, s45, 0xffff\n"
    "s_mov_b32 s30, 1152\n"
    "s_mov_b32 s31, 0x20000\n"
    "s_lshl_b32 s79, s53, 8\n"
    "s_add_u32 s79, s79, 4096\n"
    "buffer_load_dwordx2 v[170:171], off, s[20:23], s79 sc1\n"
    "v_lshlrev_b32 v160, 1, v0\n"
    "buffer_load_ushort v254, v160, s[28:31], 0 offen\n"
    "buffer_load_ushort v255, v160, s[28:31], 0 offen offset:1024\n"
    "v_mov_b32 v161, v0\n"
    "v_mul_u32_u24 v162, 57, v1\n"
    "v_lshrrev_b32 v162, 10, v162\n"
    "v_mul_u32_u24 v163, 18, v162\n"
    "v_sub_u32 v163, v1, v163\n"
    "v_lshlrev_b32 v163, 4, v163\n"
    "s_lshl_b32 s79, s50, 3\n"
    "v_add_u32 v164, s79, v162\n"
    "v_lshrrev_b32 v165, 1, v164\n"
    "v_mul_u32_u24 v165, 3456, v165\n"
    "v_and_b32 v164, 1, v164\n"
    "v_mul_u32_u24 v164, 1152, v164\n"
    "v_add3_u32 v8, v164, v165, v163\n"
    "v_add_u32 v164, s79, v162\n"
    "v_add_u32 v164, 3, v164\n"
    "v_lshrrev_b32 v165, 1, v164\n"
    "v_mul_u32_u24 v165, 3456, v165\n"
    "v_and_b32 v164, 1, v164\n"
    "v_mul_u32_u24 v164, 1152, v164\n"
    "v_add3_u32 v9, v164, v165, v163\n"
    "v_add_u32 v164, s79, v162\n"
    "v_add_u32 v164, 6, v164\n"
    "v_lshrrev_b32 v165, 1, v164\n"
    "v_mul_u32_u24 v165, 3456, v165\n"
    "v_and_b32 v164, 1, v164\n"
    "v_mul_u32_u24 v164, 1152, v164\n"
    "v_add3_u32 v10, v164, v165, v163\n"
    "s_mul_i32 s65, s53, 288\n"
    "s_add_u32 s57, s65, 1152\n"
    "s_mul_i32 s69, s50, 2304\n"
    "s_add_u32 m0, s67, 92160\n"
    "s_nop 0\n"
    "buffer_load_dwordx4 v11, s[8:11], s56 offen lds\n"
    "s_add_u32 m0, s67, 93184\n"
    "s_nop 0\n"
    "buffer_load_dwordx4 v12, s[8:11], s56 offen lds\n"
    "s_add_u32 m0, s69, 0\n"
    "s_bfm_b64 exec, 54, 0\n"
    "buffer_load_dwordx4 v8, s[12:15], s57 offen lds\n"
    "s_mov_b64 exec, -1\n"
    "s_add_u32 m0, s69, 864\n"
    "s_bfm_b64 exec, 54, 0\n"
    "buffer_load_dwordx4 v9, s[12:15], s57 offen lds\n"
    "s_mov_b64 exec, -1\n"
    "s_add_u32 m0, s69, 1728\n"
    "s_bfm_b64 exec, 36, 0\n"
    "buffer_load_dwordx4 v10, s[12:15], s57 offen lds\n"
    "s_mov_b64 exec, -1\n"
    "s_add_u32 m0, s69, 18432\n"
    "s_add_u32 s78, s57, 110592\n"
    "s_bfm_b64 exec, 54, 0\n"
    "buffer_load_dwordx4 v8, s[12:15], s78 offen lds\n"
    "s_mov_b64 exec, -1\n"
    "s_add_u32 m0, s69, 19296\n"
    "s_add_u32 s78, s57, 110592\n"
    "s_bfm_b64 exec, 54, 0\n"
    "buffer_load_dwordx4 v9, s[12:15], s78 offen lds\n"
    "s_mov_b64 exec, -1\n"
    "s_add_u32 m0, s69, 20160\n"
    "s_add_u32 s78, s57, 110592\n"
    "s_bfm_b64 exec, 36, 0\n"
    "buffer_load_dwordx4 v10, s[12:15], s78 offen lds\n"
    "s_mov_b64 exec, -1\n"
    "s_add_u32 m0, s67, 108544\n"
    "s_add_u32 s78, s56, 128\n"
    "buffer_load_dwordx4 v11, s[8:11], s78 offen lds\n"
    "s_add_u32 m0, s67, 109568\n"
    "s_add_u32 s78, s56, 128\n"
    "buffer_load_dwordx4 v12, s[8:11], s78 offen lds\n"
    "s_add_u32 m0, s69, 36864\n"
    "s_add_u32 s78, s57, 221184\n"
    "s_bfm_b64 exec, 54, 0\n"
    "buffer_load_dwordx4 v8, s[12:15], s78 offen lds\n"
    "s_mov_b64 exec, -1\n"
    "s_add_u32 m0, s69, 37728\n"
    "s_add_u32 s78, s57, 221184\n"
    "s_bfm_b64 exec, 54, 0\n"
    "buffer_load_dwordx4 v9, s[12:15], s78 offen lds\n"
    "s_mov_b64 exec, -1\n"
    "s_add_u32 m0, s69, 38592\n"
    "s_add_u32 s78, s57, 221184\n"
    "s_bfm_b64 exec, 36, 0\n"
    "buffer_load_dwordx4 v10, s[12:15], s78 offen lds\n"
    "s_mov_b64 exec, -1\n"
    "s_add_u32 m0, s69, 55296\n"
    "s_add_u32 s78, s57, 331776\n"
    "s_bfm_b64 exec, 54, 0\n"
    "buffer_load_dwordx4 v8, s[12:15], s78 offen lds\n"
    "s_mov_b64 exec, -1\n"
    "s_add_u32 m0, s69, 56160\n"
    "s_add_u32 s78, s57, 331776\n"
    "s_bfm_b64 exec, 54, 0\n"
    "buffer_load_dwordx4 v9, s[12:15], s78 offen lds\n"
    "s_mov_b64 exec, -1\n"
    "s_add_u32 m0, s69, 57024\n"
    "s_add_u32 s78, s57, 331776\n"
    "s_bfm_b64 exec, 36, 0\n"
    "buffer_load_dwordx4 v10, s[12:15], s78 offen lds\n"
    "s_mov_b64 exec, -1\n"
    "s_add_u32 m0, s67, 124928\n"
    "s_add_u32 s78, s56, 256\n"
    "buffer_load_dwordx4 v11, s[8:11], s78 offen lds\n"
    "s_add_u32 m0, s67, 125952\n"
    "s_add_u32 s78, s56, 256\n"
    "buffer_load_dwordx4 v12, s[8:11], s78 offen lds\n"
    "s_waitcnt vmcnt(21)\n"
    "v_add_co_u32 v4, vcc, 1, v4\n"
    "s_nop 1\n"
    "v_addc_co_u32 v5, vcc, 0, v5, vcc\n"
    "s_waitcnt vmcnt(20)\n"
    "v_add_co_u32 v170, vcc, 1, v170\n"
    "s_nop 1\n"
    "v_addc_co_u32 v171, vcc, 0, v171, vcc\n"
    "v_readfirstlane_b32 s61, v4\n"
    "v_readfirstlane_b32 s62, v5\n"
    "v_readfirstlane_b32 s63, v170\n"
    "v_readfirstlane_b32 s64, v171\n"
    "v_bfe_u32 v164, v161, 4, 2\n"
    "v_lshlrev_b32 v164, 4, v164\n"
    "v_bfe_u32 v165, v161, 2, 1\n"
    "v_or_b32 v164, v164, v165\n"
    "v_mul_u32_u24 v164, 288, v164\n"
    "v_bfe_u32 v165, v161, 6, 2\n"
    "v_lshl_add_u32 v164, v165, 6, v164\n"
    "v_lshrrev_b32 v165, 8, v161\n"
    "v_mul_u32_u24 v165, 576, v165\n"
    "v_add_u32 v164, v164, v165\n"
    "v_bfe_u32 v165, v161, 3, 1\n"
    "v_lshl_add_u32 v164, v165, 5, v164\n"
    "v_and_b32 v165, 3, v161\n"
    "v_lshl_add_u32 v164, v165, 3, v164\n"
    "v_add_u32 v164, 147968, v164\n"
    "s_waitcnt vmcnt(19)\n"
    "ds_write_b16 v164, v254\n"
    "v_add_u32 v161, 512, v161\n"
    "v_bfe_u32 v164, v161, 4, 2\n"
    "v_lshlrev_b32 v164, 4, v164\n"
    "v_bfe_u32 v165, v161, 2, 1\n"
    "v_or_b32 v164, v164, v165\n"
    "v_mul_u32_u24 v164, 288, v164\n"
    "v_bfe_u32 v165, v161, 6, 2\n"
    "v_lshl_add_u32 v164, v165, 6, v164\n"
    "v_lshrrev_b32 v165, 8, v161\n"
    "v_mul_u32_u24 v165, 576, v165\n"
    "v_add_u32 v164, v164, v165\n"
    "v_bfe_u32 v165, v161, 3, 1\n"
    "v_lshl_add_u32 v164, v165, 5, v164\n"
    "v_and_b32 v165, 3, v161\n"
    "v_lshl_add_u32 v164, v165, 3, v164\n"
    "v_add_u32 v164, 147968, v164\n"
    "s_cmp_eq_u32 s50, 0\n"
    "s_cbranch_scc0 Lgw_21\n"
    "s_waitcnt vmcnt(18)\n"
    "ds_write_b16 v164, v255\n"
    "v_lshlrev_b32 v164, 2, v1\n"
    "v_add_u32 v164, 147456, v164\n"
    "v_mov_b32 v165, 0\n"
    "v_mov_b32 v166, 0x3f803f80\n"
    "ds_write2_b32 v164, v165, v166 offset0:0 offset1:64\n"
    "Lgw_21:\n"
    "v_and_b32 v164, 31, v1\n"
    "s_lshl_b32 s78, s51, 5\n"
    "v_or_b32 v164, s78, v164\n"
    "v_bfe_u32 v165, v164, 1, 3\n"
    "v_lshrrev_b32 v166, 5, v1\n"
    "v_xor_b32 v165, v165, v166\n"
    "v_lshlrev_b32 v165, 4, v165\n"
    "v_lshl_or_b32 v252, v164, 7, v165\n"
    "v_add_u32 v252, 92160, v252\n"
    "v_bfe_u32 v164, v1, 5, 1\n"
    "v_bfe_u32 v165, v1, 2, 2\n"
    "v_lshl_or_b32 v164, v164, 3, v165\n"
    "v_mul_u32_u24 v164, 576, v164\n"
    "s_mul_i32 s78, s52, 144\n"
    "v_add_u32 v164, s78, v164\n"
    "v_bfe_u32 v165, v1, 4, 1\n"
    "v_and_b32 v166, 3, v1\n"
    "v_lshl_or_b32 v165, v165, 2, v166\n"
    "v_add_u32 v166, 0, v165\n"
    "v_lshlrev_b32 v167, 3, v166\n"
    "v_add_u32 v13, v164, v167\n"
    "v_add_u32 v166, 8, v165\n"
    "v_lshlrev_b32 v167, 3, v166\n"
    "v_add_u32 v14, v164, v167\n"
    "v_add_u32 v166, 16, v165\n"
    "v_lshlrev_b32 v167, 3, v166\n"
    "v_cmp_lt_u32 vcc, 17, v166\n"
    "v_add_u32 v168, 144, v167\n"
    "s_nop 0\n"
    "v_cndmask_b32 v167, v167, v168, vcc\n"
    "v_add_u32 v15, v164, v167\n"
    "v_add_u32 v166, 24, v165\n"
    "v_lshlrev_b32 v167, 3, v166\n"
    "v_cmp_lt_u32 vcc, 17, v166\n"
    "v_add_u32 v168, 144, v167\n"
    "s_nop 0\n"
    "v_cndmask_b32 v167, v167, v168, vcc\n"
    "v_add_u32 v0, v164, v167\n"
    "v_add_u32 v166, 32, v165\n"
    "v_min_u32 v166, 35, v166\n"
    "v_lshlrev_b32 v167, 3, v166\n"
    "v_cmp_lt_u32 vcc, 17, v166\n"
    "v_add_u32 v168, 144, v167\n"
    "s_nop 0\n"
    "v_cndmask_b32 v167, v167, v168, vcc\n"
    "v_add_u32 v3, v164, v167\n"
    "s_mov_b32 s74, 0xff\n"
    "s_mov_b32 s75, 0xff\n"
    "s_mov_b32 s76, 0xffff\n"
    "s_mov_b32 s77, 0xffff\n"
    "s_mov_b32 s59, 0x358637bd\n"
    "s_mov_b32 s71, 0x3f80\n"
    "s_mov_b32 s72, 0x3f800000\n"
    "s_lshl_b32 s78, s53, 3\n"
    "s_add_u32 s78, s78, s50\n"
    "s_lshl_b32 s68, s78, 14\n"
    "s_add_u32 s68, s68, 524288\n"
    "s_lshl_b32 s60, s78, 7\n"
    "s_add_u32 s60, s60, 139264\n"
    "s_waitcnt vmcnt(13) lgkmcnt(0)\n"
    "s_barrier\n"
    "s_and_b32 s79, %2, 1\n"
    "s_lshl_b32 s79, s79, 5\n"
    "s_lshr_b32 s80, %2, 3\n"
    "s_add_u32 s79, s79, s80\n"
    "s_lshl_b32 s80, s50, 6\n"
    "s_add_u32 s79, s79, s80\n"
    "s_mov_b32 s27, 0x20000\n"
    "s_cmp_ge_u32 s79, 162\n"
    "s_cbranch_scc1 Lpq_23\n"
    "s_sub_u32 s79, s79, 0\n"
    "s_lshl_b32 s79, s79, 6\n"
    "v_add_u32 v7, s79, v1\n"
    "s_mov_b32 s24, s38\n"
    "s_and_b32 s25, s39, 0xffff\n"
    "s_mov_b32 s26, 5308416\n"
    "v_mul_u32_u24 v4, 3641, v7\n"
    "v_lshrrev_b32 v4, 16, v4\n"
    "v_mul_u32_u24 v5, 18, v4\n"
    "v_sub_u32 v7, v7, v5\n"
    "v_mul_u32_u24 v4, 9216, v4\n"
    "v_lshl_add_u32 v7, v7, 7, v4\n"
    "s_mul_i32 s79, s53, 2304\n"
    "s_branch Lpgo_25\n"
    "Lpq_23:\n"
    "s_cmp_ge_u32 s79, 243\n"
    "s_cbranch_scc1 Lpw1_22\n"
    "s_sub_u32 s79, s79, 162\n"
    "s_lshl_b32 s79, s79, 6\n"
    "v_add_u32 v7, s79, v1\n"
    "s_mov_b32 s24, s34\n"
    "s_and_b32 s25, s35, 0xffff\n"
    "s_mov_b32 s26, 1990656\n"
    "v_mul_u32_u24 v4, 21846, v7\n"
    "v_lshrrev_b32 v4, 16, v4\n"
    "v_mul_u32_u24 v5, 3, v4\n"
    "v_sub_u32 v7, v7, v5\n"
    "v_mul_u32_u24 v4, 1152, v4\n"
    "v_lshl_add_u32 v7, v7, 7, v4\n"
    "s_lshl_b32 s79, s53, 8\n"
    "s_branch Lpgo_25\n"
    "Lpw1_22:\n"
    "s_cmp_ge_u32 s79, 405\n"
    "s_cbranch_scc1 Lpo_24\n"
    "s_sub_u32 s79, s79, 243\n"
    "s_lshl_b32 s79, s79, 6\n"
    "v_add_u32 v7, s79, v1\n"
    "s_mov_b32 s24, s42\n"
    "s_and_b32 s25, s43, 0xffff\n"
    "s_mov_b32 s26, 5308416\n"
    "v_lshlrev_b32 v7, 7, v7\n"
    "s_mul_i32 s79, s53, 1327104\n"
    "s_branch Lpgo_25\n"
    "Lpo_24:\n"
    "s_cmp_ge_u32 s79, 426\n"
    "s_cbranch_scc1 Lpskip_26\n"
    "s_sub_u32 s79, s79, 405\n"
    "s_lshl_b32 s79, s79, 6\n"
    "v_add_u32 v7, s79, v1\n"
    "s_mov_b32 s24, s36\n"
    "s_and_b32 s25, s37, 0xffff\n"
    "s_mov_b32 s26, 663552\n"
    "v_lshlrev_b32 v7, 7, v7\n"
    "s_mul_i32 s79, s53, 165888\n"
    "Lpgo_25:\n"
    "buffer_load_dword v6, v7, s[24:27], s79 offen\n"
    "Lpskip_26:\n"
    "ds_read_b128 v[16:19], v252\n"
    "ds_read_b64_tr_b16 v[240:241], v13 offset:0\n"
    "ds_read_b64_tr_b16 v[242:243], v13 offset:2304\n"
    "ds_read_b64_tr_b16 v[244:245], v14 offset:0\n"
    "ds_read_b64_tr_b16 v[246:247], v14 offset:2304\n"
    "s_waitcnt lgkmcnt(2)\n"
    "v_mfma_f32_32x32x16_bf16 v[160:175], v[16:19], v[240:243], 0\n"
    "ds_read_b64_tr_b16 v[248:249], v15 offset:0\n"
    "ds_read_b64_tr_b16 v[250:251], v15 offset:2304\n"
    "s_waitcnt lgkmcnt(2)\n"
    "v_mfma_f32_32x32x16_bf16 v[176:191], v[16:19], v[244:247], 0\n"
    "ds_read_b64_tr_b16 v[240:241], v0 offset:0\n"
    "ds_read_b64_tr_b16 v[242:243], v0 offset:2304\n"
    "s_waitcnt lgkmcnt(2)\n"
    "v_mfma_f32_32x32x16_bf16 v[192:207], v[16:19], v[248:251], 0\n"
    "ds_read_b64_tr_b16 v[244:245], v3 offset:0\n"
    "ds_read_b64_tr_b16 v[246:247], v3 offset:2304\n"
    "s_waitcnt lgkmcnt(2)\n"
    "v_mfma_f32_32x32x16_bf16 v[208:223], v[16:19], v[240:243], 0\n"
    "v_xor_b32 v253, 32, v252\n"
    "ds_read_b128 v[20:23], v253\n"
    "ds_read_b64_tr_b16 v[248:249], v13 offset:9216\n"
    "ds_read_b64_tr_b16 v[250:251], v13 offset:11520\n"
    "s_waitcnt lgkmcnt(3)\n"
    "v_mfma_f32_32x32x16_bf16 v[224:239], v[16:19], v[244:247], 0\n"
    "ds_read_b64_tr_b16 v[240:241], v14 offset:9216\n"
    "ds_read_b64_tr_b16 v[242:243], v14 offset:11520\n"
    "s_waitcnt lgkmcnt(2)\n"
    "v_mfma_f32_32x32x16_bf16 v[160:175], v[20:23], v[248:251], v[160:175]\n"
    "ds_read_b64_tr_b16 v[244:245], v15 offset:9216\n"
    "ds_read_b64_tr_b16 v[246:247], v15 offset:11520\n"
    "s_waitcnt lgkmcnt(2)\n"
    "v_mfma_f32_32x32x16_bf16 v[176:191], v[20:23], v[240:243], v[176:191]\n"
    "ds_read_b64_tr_b16 v[248:249], v0 offset:9216\n"
    "ds_read_b64_tr_b16 v[250:251], v0 offset:11520\n"
    "s_waitcnt lgkmcnt(2)\n"
    "v_mfma_f32_32x32x16_bf16 v[192:207], v[20:23], v[244:247], v[192:207]\n"
    "ds_read_b64_tr_b16 v[240:241], v3 offset:9216\n"
    "ds_read_b64_tr_b16 v[242:243], v3 offset:11520\n"
    "s_waitcnt lgkmcnt(2)\n"
    "v_mfma_f32_32x32x16_bf16 v[208:223], v[20:23], v[248:251], v[208:223]\n"
    "s_waitcnt vmcnt(10)\n"
    "s_barrier\n"
    "v_xor_b32 v253, 64, v252\n"
    "ds_read_b128 v[24:27], v253\n"
    "v_add_u32 v13, 18432, v13\n"
    "ds_read_b64_tr_b16 v[244:245], v13 offset:0\n"
    "ds_read_b64_tr_b16 v[246:247], v13 offset:2304\n"
    "s_add_u32 m0, s69, 73728\n"
    "s_add_u32 s78, s57, 442368\n"
    "s_bfm_b64 exec, 54, 0\n"
    "buffer_load_dwordx4 v8, s[12:15], s78 offen lds\n"
    "s_mov_b64 exec, -1\n"
    "s_waitcnt lgkmcnt(3)\n"
    "v_mfma_f32_32x32x16_bf16 v[224:239], v[20:23], v[240:243], v[224:239]\n"
    "v_add_u32 v14, 18432, v14\n"
    "ds_read_b64_tr_b16 v[248:249], v14 offset:0\n"
    "ds_read_b64_tr_b16 v[250:251], v14 offset:2304\n"
    "s_waitcnt lgkmcnt(2)\n"
    "v_mfma_f32_32x32x16_bf16 v[160:175], v[24:27], v[244:247], v[160:175]\n"
    "v_add_u32 v15, 18432, v15\n"
    "ds_read_b64_tr_b16 v[240:241], v15 offset:0\n"
    "ds_read_b64_tr_b16 v[242:243], v15 offset:2304\n"
    "s_waitcnt lgkmcnt(2)\n"
    "v_mfma_f32_32x32x16_bf16 v[176:191], v[24:27], v[248:251], v[176:191]\n"
    "v_add_u32 v0, 18432, v0\n"
    "ds_read_b64_tr_b16 v[244:245], v0 offset:0\n"
    "ds_read_b64_tr_b16 v[246:247], v0 offset:2304\n"
    "s_add_u32 m0, s69, 74592\n"
    "s_add_u32 s78, s57, 442368\n"
    "s_bfm_b64 exec, 54, 0\n"
    "buffer_load_dwordx4 v9, s[12:15], s78 offen lds\n"
    "s_mov_b64 exec, -1\n"
    "s_waitcnt lgkmcnt(2)\n"
    "v_mfma_f32_32x32x16_bf16 v[192:207], v[24:27], v[240:243], v[192:207]\n"
    "v_add_u32 v3, 18432, v3\n"
    "ds_read_b64_tr_b16 v[248:249], v3 offset:0\n"
    "ds_read_b64_tr_b16 v[250:251], v3 offset:2304\n"
    "s_waitcnt lgkmcnt(2)\n"
    "v_mfma_f32_32x32x16_bf16 v[208:223], v[24:27], v[244:247], v[208:223]\n"
    "v_xor_b32 v253, 96, v252\n"
    "ds_read_b128 v[28:31], v253\n"
    "ds_read_b64_tr_b16 v[240:241], v13 offset:9216\n"
    "ds_read_b64_tr_b16 v[242:243], v13 offset:11520\n"
    "s_waitcnt lgkmcnt(3)\n"
    "v_mfma_f32_32x32x16_bf16 v[224:239], v[24:27], v[248:251], v[224:239]\n"
    "ds_read_b64_tr_b16 v[244:245], v14 offset:9216\n"
    "ds_read_b64_tr_b16 v[246:247], v14 offset:11520\n"
    "s_add_u32 m0, s69, 75456\n"
    "s_add_u32 s78, s57, 442368\n"
    "s_bfm_b64 exec, 36, 0\n"
    "buffer_load_dwordx4 v10, s[12:15], s78 offen lds\n"
    "s_mov_b64 exec, -1\n"
    "s_waitcnt lgkmcnt(2)\n"
    "v_mfma_f32_32x32x16_bf16 v[160:175], v[28:31], v[240:243], v[160:175]\n"
    "ds_read_b64_tr_b16 v[248:249], v15 offset:9216\n"
    "ds_read_b64_tr_b16 v[250:251], v15 offset:11520\n"
    "s_waitcnt lgkmcnt(2)\n"
    "v_mfma_f32_32x32x16_bf16 v[176:191], v[28:31], v[244:247], v[176:191]\n"
    "ds_read_b64_tr_b16 v[240:241], v0 offset:9216\n"
    "ds_read_b64_tr_b16 v[242:243], v0 offset:11520\n"
    "s_waitcnt lgkmcnt(2)\n"
    "v_mfma_f32_32x32x16_bf16 v[192:207], v[28:31], v[248:251], v[192:207]\n"
    "ds_read_b64_tr_b16 v[244:245], v3 offset:9216\n"
    "ds_read_b64_tr_b16 v[246:247], v3 offset:11520\n"
    "s_waitcnt lgkmcnt(2)\n"
    "v_mfma_f32_32x32x16_bf16 v[208:223], v[28:31], v[240:243], v[208:223]\n"
    "s_waitcnt vmcnt(8)\n"
    "s_barrier\n"
    "v_add_u32 v252, 16384, v252\n"
    "ds_read_b128 v[32:35], v252\n"
    "v_add_u32 v13, 18432, v13\n"
    "ds_read_b64_tr_b16 v[248:249], v13 offset:0\n"
    "ds_read_b64_tr_b16 v[250:251], v13 offset:2304\n"
    "s_add_u32 m0, s69, 0\n"
    "s_add_u32 s78, s57, 552960\n"
    "s_bfm_b64 exec, 54, 0\n"
    "buffer_load_dwordx4 v8, s[12:15], s78 offen lds\n"
    "s_mov_b64 exec, -1\n"
    "s_waitcnt lgkmcnt(3)\n"
    "v_mfma_f32_32x32x16_bf16 v[224:239], v[28:31], v[244:247], v[224:239]\n"
    "v_add_u32 v14, 18432, v14\n"
    "ds_read_b64_tr_b16 v[240:241], v14 offset:0\n"
    "ds_read_b64_tr_b16 v[242:243], v14 offset:2304\n"
    "s_waitcnt lgkmcnt(2)\n"
    "v_mfma_f32_32x32x16_bf16 v[160:175], v[32:35], v[248:251], v[160:175]\n"
    "v_add_u32 v15, 18432, v15\n"
    "ds_read_b64_tr_b16 v[244:245], v15 offset:0\n"
    "ds_read_b64_tr_b16 v[246:247], v15 offset:2304\n"
    "s_waitcnt lgkmcnt(2)\n"
    "v_mfma_f32_32x32x16_bf16 v[176:191], v[32:35], v[240:243], v[176:191]\n"
    "v_add_u32 v0, 18432, v0\n"
    "ds_read_b64_tr_b16 v[248:249], v0 offset:0\n"
    "ds_read_b64_tr_b16 v[250:251], v0 offset:2304\n"
    "s_add_u32 m0, s69, 864\n"
    "s_add_u32 s78, s57, 552960\n"
    "s_bfm_b64 exec, 54, 0\n"
    "buffer_load_dwordx4 v9, s[12:15], s78 offen lds\n"
    "s_mov_b64 exec, -1\n"
    "s_waitcnt lgkmcnt(2)\n"
    "v_mfma_f32_32x32x16_bf16 v[192:207], v[32:35], v[244:247], v[192:207]\n"
    "v_add_u32 v3, 18432, v3\n"
    "ds_read_b64_tr_b16 v[240:241], v3 offset:0\n"
    "ds_read_b64_tr_b16 v[242:243], v3 offset:2304\n"
    "s_waitcnt lgkmcnt(2)\n"
    "v_mfma_f32_32x32x16_bf16 v[208:223], v[32:35], v[248:251], v[208:223]\n"
    "v_xor_b32 v253, 32, v252\n"
    "ds_read_b128 v[36:39], v253\n"
    "ds_read_b64_tr_b16 v[244:245], v13 offset:9216\n"
    "ds_read_b64_tr_b16 v[246:247], v13 offset:11520\n"
    "s_waitcnt lgkmcnt(3)\n"
    "v_mfma_f32_32x32x16_bf16 v[224:239], v[32:35], v[240:243], v[224:239]\n"
    "ds_read_b64_tr_b16 v[248:249], v14 offset:9216\n"
    "ds_read_b64_tr_b16 v[250:251], v14 offset:11520\n"
    "s_add_u32 m0, s69, 1728\n"
    "s_add_u32 s78, s57, 552960\n"
    "s_bfm_b64 exec, 36, 0\n"
    "buffer_load_dwordx4 v10, s[12:15], s78 offen lds\n"
    "s_mov_b64 exec, -1\n"
    "s_waitcnt lgkmcnt(2)\n"
    "v_mfma_f32_32x32x16_bf16 v[160:175], v[36:39], v[244:247], v[160:175]\n"
    "ds_read_b64_tr_b16 v[240:241], v15 offset:9216\n"
    "ds_read_b64_tr_b16 v[242:243], v15 offset:11520\n"
    "s_waitcnt lgkmcnt(2)\n"
    "v_mfma_f32_32x32x16_bf16 v[176:191], v[36:39], v[248:251], v[176:191]\n"
    "ds_read_b64_tr_b16 v[244:245], v0 offset:9216\n"
    "ds_read_b64_tr_b16 v[246:247], v0 offset:11520\n"
    "s_waitcnt lgkmcnt(2)\n"
    "v_mfma_f32_32x32x16_bf16 v[192:207], v[36:39], v[240:243], v[192:207]\n"
    "ds_read_b64_tr_b16 v[248:249], v3 offset:9216\n"
    "ds_read_b64_tr_b16 v[250:251], v3 offset:11520\n"
    "s_waitcnt lgkmcnt(2)\n"
    "v_mfma_f32_32x32x16_bf16 v[208:223], v[36:39], v[244:247], v[208:223]\n"
    "s_waitcnt vmcnt(8)\n"
    "s_barrier\n"
    "v_xor_b32 v253, 64, v252\n"
    "ds_read_b128 v[40:43], v253\n"
    "v_add_u32 v13, 18432, v13\n"
    "ds_read_b64_tr_b16 v[240:241], v13 offset:0\n"
    "ds_read_b64_tr_b16 v[242:243], v13 offset:2304\n"
    "s_add_u32 m0, s69, 18432\n"
    "s_add_u32 s78, s57, 663552\n"
    "s_bfm_b64 exec, 54, 0\n"
    "buffer_load_dwordx4 v8, s[12:15], s78 offen lds\n"
    "s_mov_b64 exec, -1\n"
    "s_waitcnt lgkmcnt(3)\n"
    "v_mfma_f32_32x32x16_bf16 v[224:239], v[36:39], v[248:251], v[224:239]\n"
    "v_add_u32 v14, 18432, v14\n"
    "ds_read_b64_tr_b16 v[244:245], v14 offset:0\n"
    "ds_read_b64_tr_b16 v[246:247], v14 offset:2304\n"
    "s_waitcnt lgkmcnt(2)\n"
    "v_mfma_f32_32x32x16_bf16 v[160:175], v[40:43], v[240:243], v[160:175]\n"
    "v_add_u32 v15, 18432, v15\n"
    "ds_read_b64_tr_b16 v[248:249], v15 offset:0\n"
    "ds_read_b64_tr_b16 v[250:251], v15 offset:2304\n"
    "s_add_u32 m0, s69, 19296\n"
    "s_add_u32 s78, s57, 663552\n"
    "s_bfm_b64 exec, 54, 0\n"
    "buffer_load_dwordx4 v9, s[12:15], s78 offen lds\n"
    "s_mov_b64 exec, -1\n"
    "s_waitcnt lgkmcnt(2)\n"
    "v_mfma_f32_32x32x16_bf16 v[176:191], v[40:43], v[244:247], v[176:191]\n"
    "v_add_u32 v0, 18432, v0\n"
    "ds_read_b64_tr_b16 v[240:241], v0 offset:0\n"
    "ds_read_b64_tr_b16 v[242:243], v0 offset:2304\n"
    "s_waitcnt lgkmcnt(2)\n"
    "v_mfma_f32_32x32x16_bf16 v[192:207], v[40:43], v[248:251], v[192:207]\n"
    "v_add_u32 v3, 18432, v3\n"
    "ds_read_b64_tr_b16 v[244:245], v3 offset:0\n"
    "ds_read_b64_tr_b16 v[246:247], v3 offset:2304\n"
    "s_add_u32 m0, s69, 20160\n"
    "s_add_u32 s78, s57, 663552\n"
    "s_bfm_b64 exec, 36, 0\n"
    "buffer_load_dwordx4 v10, s[12:15], s78 offen lds\n"
    "s_mov_b64 exec, -1\n"
    "s_waitcnt lgkmcnt(2)\n"
    "v_mfma_f32_32x32x16_bf16 v[208:223], v[40:43], v[240:243], v[208:223]\n"
    "v_xor_b32 v253, 96, v252\n"
    "ds_read_b128 v[44:47], v253\n"
    "ds_read_b64_tr_b16 v[248:249], v13 offset:9216\n"
    "ds_read_b64_tr_b16 v[250:251], v13 offset:11520\n"
    "s_waitcnt lgkmcnt(3)\n"
    "v_mfma_f32_32x32x16_bf16 v[224:239], v[40:43], v[244:247], v[224:239]\n"
    "ds_read_b64_tr_b16 v[240:241], v14 offset:9216\n"
    "ds_read_b64_tr_b16 v[242:243], v14 offset:11520\n"
    "s_add_u32 m0, s67, 92160\n"
    "s_add_u32 s78, s56, 384\n"
    "buffer_load_dwordx4 v11, s[8:11], s78 offen lds\n"
    "s_waitcnt lgkmcnt(2)\n"
    "v_mfma_f32_32x32x16_bf16 v[160:175], v[44:47], v[248:251], v[160:175]\n"
    "ds_read_b64_tr_b16 v[244:245], v15 offset:9216\n"
    "ds_read_b64_tr_b16 v[246:247], v15 offset:11520\n"
    "s_waitcnt lgkmcnt(2)\n"
    "v_mfma_f32_32x32x16_bf16 v[176:191], v[44:47], v[240:243], v[176:191]\n"
    "ds_read_b64_tr_b16 v[248:249], v0 offset:9216\n"
    "ds_read_b64_tr_b16 v[250:251], v0 offset:11520\n"
    "s_add_u32 m0, s67, 93184\n"
    "s_add_u32 s78, s56, 384\n"
    "buffer_load_dwordx4 v12, s[8:11], s78 offen lds\n"
    "s_waitcnt lgkmcnt(2)\n"
    "v_mfma_f32_32x32x16_bf16 v[192:207], v[44:47], v[244:247], v[192:207]\n"
    "ds_read_b64_tr_b16 v[240:241], v3 offset:9216\n"
    "ds_read_b64_tr_b16 v[242:243], v3 offset:11520\n"
    "s_waitcnt lgkmcnt(2)\n"
    "v_mfma_f32_32x32x16_bf16 v[208:223], v[44:47], v[248:251], v[208:223]\n"
    "s_waitcnt vmcnt(8)\n"
    "s_barrier\n"
    "v_add_u32 v252, 16384, v252\n"
    "ds_read_b128 v[48:51], v252\n"
    "v_add_u32 v13, 18432, v13\n"
    "ds_read_b64_tr_b16 v[244:245], v13 offset:0\n"
    "ds_read_b64_tr_b16 v[246:247], v13 offset:2304\n"
    "s_add_u32 m0, s69, 36864\n"
    "s_add_u32 s78, s57, 774144\n"
    "s_bfm_b64 exec, 54, 0\n"
    "buffer_load_dwordx4 v8, s[12:15], s78 offen lds\n"
    "s_mov_b64 exec, -1\n"
    "s_waitcnt lgkmcnt(3)\n"
    "v_mfma_f32_32x32x16_bf16 v[224:239], v[44:47], v[240:243], v[224:239]\n"
    "v_add_u32 v14, 18432, v14\n"
    "ds_read_b64_tr_b16 v[248:249], v14 offset:0\n"
    "ds_read_b64_tr_b16 v[250:251], v14 offset:2304\n"
    "s_waitcnt lgkmcnt(2)\n"
    "v_mfma_f32_32x32x16_bf16 v[160:175], v[48:51], v[244:247], v[160:175]\n"
    "v_add_u32 v15, 18432, v15\n"
    "ds_read_b64_tr_b16 v[240:241], v15 offset:0\n"
    "ds_read_b64_tr_b16 v[242:243], v15 offset:2304\n"
    "s_waitcnt lgkmcnt(2)\n"
    "v_mfma_f32_32x32x16_bf16 v[176:191], v[48:51], v[248:251], v[176:191]\n"
    "v_add_u32 v0, 18432, v0\n"
    "ds_read_b64_tr_b16 v[244:245], v0 offset:0\n"
    "ds_read_b64_tr_b16 v[246:247], v0 offset:2304\n"
    "s_add_u32 m0, s69, 37728\n"
    "s_add_u32 s78, s57, 774144\n"
    "s_bfm_b64 exec, 54, 0\n"
    "buffer_load_dwordx4 v9, s[12:15], s78 offen lds\n"
    "s_mov_b64 exec, -1\n"
    "s_waitcnt lgkmcnt(2)\n"
    "v_mfma_f32_32x32x16_bf16 v[192:207], v[48:51], v[240:243], v[192:207]\n"
    "v_add_u32 v3, 18432, v3\n"
    "ds_read_b64_tr_b16 v[248:249], v3 offset:0\n"
    "ds_read_b64_tr_b16 v[250:251], v3 offset:2304\n"
    "s_waitcnt lgkmcnt(2)\n"
    "v_mfma_f32_32x32x16_bf16 v[208:223], v[48:51], v[244:247], v[208:223]\n"
    "v_xor_b32 v253, 32, v252\n"
    "ds_read_b128 v[52:55], v253\n"
    "ds_read_b64_tr_b16 v[240:241], v13 offset:9216\n"
    "ds_read_b64_tr_b16 v[242:243], v13 offset:11520\n"
    "s_waitcnt lgkmcnt(3)\n"
    "v_mfma_f32_32x32x16_bf16 v[224:239], v[48:51], v[248:251], v[224:239]\n"
    "ds_read_b64_tr_b16 v[244:245], v14 offset:9216\n"
    "ds_read_b64_tr_b16 v[246:247], v14 offset:11520\n"
    "s_add_u32 m0, s69, 38592\n"
    "s_add_u32 s78, s57, 774144\n"
    "s_bfm_b64 exec, 36, 0\n"
    "buffer_load_dwordx4 v10, s[12:15], s78 offen lds\n"
    "s_mov_b64 exec, -1\n"
    "s_waitcnt lgkmcnt(2)\n"
    "v_mfma_f32_32x32x16_bf16 v[160:175], v[52:55], v[240:243], v[160:175]\n"
    "ds_read_b64_tr_b16 v[248:249], v15 offset:9216\n"
    "ds_read_b64_tr_b16 v[250:251], v15 offset:11520\n"
    "s_waitcnt lgkmcnt(2)\n"
    "v_mfma_f32_32x32x16_bf16 v[176:191], v[52:55], v[244:247], v[176:191]\n"
    "ds_read_b64_tr_b16 v[240:241], v0 offset:9216\n"
    "ds_read_b64_tr_b16 v[242:243], v0 offset:11520\n"
    "s_waitcnt lgkmcnt(2)\n"
    "v_mfma_f32_32x32x16_bf16 v[192:207], v[52:55], v[248:251], v[192:207]\n"
    "ds_read_b64_tr_b16 v[244:245], v3 offset:9216\n"
    "ds_read_b64_tr_b16 v[246:247], v3 offset:11520\n"
    "s_waitcnt lgkmcnt(2)\n"
    "v_mfma_f32_32x32x16_bf16 v[208:223], v[52:55], v[240:243], v[208:223]\n"
    "s_waitcnt vmcnt(8)\n"
    "s_barrier\n"
    "v_xor_b32 v253, 64, v252\n"
    "ds_read_b128 v[56:59], v253\n"
    "v_add_u32 v13, 4294893568, v13\n"
    "ds_read_b64_tr_b16 v[248:249], v13 offset:0\n"
    "ds_read_b64_tr_b16 v[250:251], v13 offset:2304\n"
    "s_add_u32 m0, s69, 55296\n"
    "s_add_u32 s78, s57, 884736\n"
    "s_bfm_b64 exec, 54, 0\n"
    "buffer_load_dwordx4 v8, s[12:15], s78 offen lds\n"
    "s_mov_b64 exec, -1\n"
    "s_waitcnt lgkmcnt(3)\n"
    "v_mfma_f32_32x32x16_bf16 v[224:239], v[52:55], v[244:247], v[224:239]\n"
    "v_add_u32 v14, 4294893568, v14\n"
    "ds_read_b64_tr_b16 v[240:241], v14 offset:0\n"
    "ds_read_b64_tr_b16 v[242:243], v14 offset:2304\n"
    "s_waitcnt lgkmcnt(2)\n"
    "v_mfma_f32_32x32x16_bf16 v[160:175], v[56:59], v[248:251], v[160:175]\n"
    "v_add_u32 v15, 4294893568, v15\n"
    "ds_read_b64_tr_b16 v[244:245], v15 offset:0\n"
    "ds_read_b64_tr_b16 v[246:247], v15 offset:2304\n"
    "s_add_u32 m0, s69, 56160\n"
    "s_add_u32 s78, s57, 884736\n"
    "s_bfm_b64 exec, 54, 0\n"
    "buffer_load_dwordx4 v9, s[12:15], s78 offen lds\n"
    "s_mov_b64 exec, -1\n"
    "s_waitcnt lgkmcnt(2)\n"
    "v_mfma_f32_32x32x16_bf16 v[176:191], v[56:59], v[240:243], v[176:191]\n"
    "v_add_u32 v0, 4294893568, v0\n"
    "ds_read_b64_tr_b16 v[248:249], v0 offset:0\n"
    "ds_read_b64_tr_b16 v[250:251], v0 offset:2304\n"
    "s_waitcnt lgkmcnt(2)\n"
    "v_mfma_f32_32x32x16_bf16 v[192:207], v[56:59], v[244:247], v[192:207]\n"
    "v_add_u32 v3, 4294893568, v3\n"
    "ds_read_b64_tr_b16 v[240:241], v3 offset:0\n"
    "ds_read_b64_tr_b16 v[242:243], v3 offset:2304\n"
    "s_add_u32 m0, s69, 57024\n"
    "s_add_u32 s78, s57, 884736\n"
    "s_bfm_b64 exec, 36, 0\n"
    "buffer_load_dwordx4 v10, s[12:15], s78 offen lds\n"
    "s_mov_b64 exec, -1\n"
    "s_waitcnt lgkmcnt(2)\n"
    "v_mfma_f32_32x32x16_bf16 v[208:223], v[56:59], v[248:251], v[208:223]\n"
    "v_xor_b32 v253, 96, v252\n"
    "ds_read_b128 v[60:63], v253\n"
    "ds_read_b64_tr_b16 v[244:245], v13 offset:9216\n"
    "ds_read_b64_tr_b16 v[246:247], v13 offset:11520\n"
    "s_waitcnt lgkmcnt(3)\n"
    "v_mfma_f32_32x32x16_bf16 v[224:239], v[56:59], v[240:243], v[224:239]\n"
    "ds_read_b64_tr_b16 v[248:249], v14 offset:9216\n"
    "ds_read_b64_tr_b16 v[250:251], v14 offset:11520\n"
    "s_add_u32 m0, s67, 108544\n"
    "s_add_u32 s78, s56, 512\n"
    "buffer_load_dwordx4 v11, s[8:11], s78 offen lds\n"
    "s_waitcnt lgkmcnt(2)\n"
    "v_mfma_f32_32x32x16_bf16 v[160:175], v[60:63], v[244:247], v[160:175]\n"
    "ds_read_b64_tr_b16 v[240:241], v15 offset:9216\n"
    "ds_read_b64_tr_b16 v[242:243], v15 offset:11520\n"
    "s_waitcnt lgkmcnt(2)\n"
    "v_mfma_f32_32x32x16_bf16 v[176:191], v[60:63], v[248:251], v[176:191]\n"
    "ds_read_b64_tr_b16 v[244:245], v0 offset:9216\n"
    "ds_read_b64_tr_b16 v[246:247], v0 offset:11520\n"
    "s_add_u32 m0, s67, 109568\n"
    "s_add_u32 s78, s56, 512\n"
    "buffer_load_dwordx4 v12, s[8:11], s78 offen lds\n"
    "s_waitcnt lgkmcnt(2)\n"
    "v_mfma_f32_32x32x16_bf16 v[192:207], v[60:63], v[240:243], v[192:207]\n"
    "ds_read_b64_tr_b16 v[248:249], v3 offset:9216\n"
    "ds_read_b64_tr_b16 v[250:251], v3 offset:11520\n"
    "s_waitcnt lgkmcnt(2)\n"
    "v_mfma_f32_32x32x16_bf16 v[208:223], v[60:63], v[244:247], v[208:223]\n"
    "s_waitcnt vmcnt(8)\n"
    "s_barrier\n"
    "v_add_u32 v252, 4294934528, v252\n"
    "ds_read_b128 v[64:67], v252\n"
    "v_add_u32 v13, 18432, v13\n"
    "ds_read_b64_tr_b16 v[240:241], v13 offset:0\n"
    "ds_read_b64_tr_b16 v[242:243], v13 offset:2304\n"
    "s_add_u32 m0, s69, 73728\n"
    "s_add_u32 s78, s57, 995328\n"
    "s_bfm_b64 exec, 54, 0\n"
    "buffer_load_dwordx4 v8, s[12:15], s78 offen lds\n"
    "s_mov_b64 exec, -1\n"
    "s_waitcnt lgkmcnt(3)\n"
    "v_mfma_f32_32x32x16_bf16 v[224:239], v[60:63], v[248:251], v[224:239]\n"
    "v_add_u32 v14, 18432, v14\n"
    "ds_read_b64_tr_b16 v[244:245], v14 offset:0\n"
    "ds_read_b64_tr_b16 v[246:247], v14 offset:2304\n"
    "s_waitcnt lgkmcnt(2)\n"
    "v_mfma_f32_32x32x16_bf16 v[160:175], v[64:67], v[240:243], v[160:175]\n"
    "v_add_u32 v15, 18432, v15\n"
    "ds_read_b64_tr_b16 v[248:249], v15 offset:0\n"
    "ds_read_b64_tr_b16 v[250:251], v15 offset:2304\n"
    "s_waitcnt lgkmcnt(2)\n"
    "v_mfma_f32_32x32x16_bf16 v[176:191], v[64:67], v[244:247], v[176:191]\n"
    "v_add_u32 v0, 18432, v0\n"
    "ds_read_b64_tr_b16 v[240:241], v0 offset:0\n"
    "ds_read_b64_tr_b16 v[242:243], v0 offset:2304\n"
    "s_add_u32 m0, s69, 74592\n"
    "s_add_u32 s78, s57, 995328\n"
    "s_bfm_b64 exec, 54, 0\n"
    "buffer_load_dwordx4 v9, s[12:15], s78 offen lds\n"
    "s_mov_b64 exec, -1\n"
    "s_waitcnt lgkmcnt(2)\n"
    "v_mfma_f32_32x32x16_bf16 v[192:207], v[64:67], v[248:251], v[192:207]\n"
    "v_add_u32 v3, 18432, v3\n"
    "ds_read_b64_tr_b16 v[244:245], v3 offset:0\n"
    "ds_read_b64_tr_b16 v[246:247], v3 offset:2304\n"
    "s_waitcnt lgkmcnt(2)\n"
    "v_mfma_f32_32x32x16_bf16 v[208:223], v[64:67], v[240:243], v[208:223]\n"
    "v_xor_b32 v253, 32, v252\n"
    "ds_read_b128 v[68:71], v253\n"
    "ds_read_b64_tr_b16 v[248:249], v13 offset:9216\n"
    "ds_read_b64_tr_b16 v[250:251], v13 offset:11520\n"
    "s_waitcnt lgkmcnt(3)\n"
    "v_mfma_f32_32x32x16_bf16 v[224:239], v[64:67], v[244:247], v[224:239]\n"
    "ds_read_b64_tr_b16 v[240:241], v14 offset:9216\n"
    "ds_read_b64_tr_b16 v[242:243], v14 offset:11520\n"
    "s_add_u32 m0, s69, 75456\n"
    "s_add_u32 s78, s57, 995328\n"
    "s_bfm_b64 exec, 36, 0\n"
    "buffer_load_dwordx4 v10, s[12:15], s78 offen lds\n"
    "s_mov_b64 exec, -1\n"
    "s_waitcnt lgkmcnt(2)\n"
    "v_mfma_f32_32x32x16_bf16 v[160:175], v[68:71], v[248:251], v[160:175]\n"
    "ds_read_b64_tr_b16 v[244:245], v15 offset:9216\n"
    "ds_read_b64_tr_b16 v[246:247], v15 offset:11520\n"
    "s_waitcnt lgkmcnt(2)\n"
    "v_mfma_f32_32x32x16_bf16 v[176:191], v[68:71], v[240:243], v[176:191]\n"
    "ds_read_b64_tr_b16 v[248:249], v0 offset:9216\n"
    "ds_read_b64_tr_b16 v[250:251], v0 offset:11520\n"
    "s_waitcnt lgkmcnt(2)\n"
    "v_mfma_f32_32x32x16_bf16 v[192:207], v[68:71], v[244:247], v[192:207]\n"
    "ds_read_b64_tr_b16 v[240:241], v3 offset:9216\n"
    "ds_read_b64_tr_b16 v[242:243], v3 offset:11520\n"
    "s_waitcnt lgkmcnt(2)\n"
    "v_mfma_f32_32x32x16_bf16 v[208:223], v[68:71], v[248:251], v[208:223]\n"
    "s_waitcnt vmcnt(8)\n"
    "s_barrier\n"
    "v_xor_b32 v253, 64, v252\n"
    "ds_read_b128 v[72:75], v253\n"
    "v_add_u32 v13, 18432, v13\n"
    "ds_read_b64_tr_b16 v[244:245], v13 offset:0\n"
    "ds_read_b64_tr_b16 v[246:247], v13 offset:2304\n"
    "s_add_u32 m0, s69, 0\n"
    "s_add_u32 s78, s57, 1105920\n"
    "s_bfm_b64 exec, 54, 0\n"
    "buffer_load_dwordx4 v8, s[12:15], s78 offen lds\n"
    "s_mov_b64 exec, -1\n"
    "s_waitcnt lgkmcnt(3)\n"
    "v_mfma_f32_32x32x16_bf16 v[224:239], v[68:71], v[240:243], v[224:239]\n"
    "v_add_u32 v14, 18432, v14\n"
    "ds_read_b64_tr_b16 v[248:249], v14 offset:0\n"
    "ds_read_b64_tr_b16 v[250:251], v14 offset:2304\n"
    "s_waitcnt lgkmcnt(2)\n"
    "v_mfma_f32_32x32x16_bf16 v[160:175], v[72:75], v[244:247], v[160:175]\n"
    "v_add_u32 v15, 18432, v15\n"
    "ds_read_b64_tr_b16 v[240:241], v15 offset:0\n"
    "ds_read_b64_tr_b16 v[242:243], v15 offset:2304\n"
    "s_add_u32 m0, s69, 864\n"
    "s_add_u32 s78, s57, 1105920\n"
    "s_bfm_b64 exec, 54, 0\n"
    "buffer_load_dwordx4 v9, s[12:15], s78 offen lds\n"
    "s_mov_b64 exec, -1\n"
    "s_waitcnt lgkmcnt(2)\n"
    "v_mfma_f32_32x32x16_bf16 v[176:191], v[72:75], v[248:251], v[176:191]\n"
    "v_add_u32 v0, 18432, v0\n"
    "ds_read_b64_tr_b16 v[244:245], v0 offset:0\n"
    "ds_read_b64_tr_b16 v[246:247], v0 offset:2304\n"
    "s_waitcnt lgkmcnt(2)\n"
    "v_mfma_f32_32x32x16_bf16 v[192:207], v[72:75], v[240:243], v[192:207]\n"
    "v_add_u32 v3, 18432, v3\n"
    "ds_read_b64_tr_b16 v[248:249], v3 offset:0\n"
    "ds_read_b64_tr_b16 v[250:251], v3 offset:2304\n"
    "s_add_u32 m0, s69, 1728\n"
    "s_add_u32 s78, s57, 1105920\n"
    "s_bfm_b64 exec, 36, 0\n"
    "buffer_load_dwordx4 v10, s[12:15], s78 offen lds\n"
    "s_mov_b64 exec, -1\n"
    "s_waitcnt lgkmcnt(2)\n"
    "v_mfma_f32_32x32x16_bf16 v[208:223], v[72:75], v[244:247], v[208:223]\n"
    "v_xor_b32 v253, 96, v252\n"
    "ds_read_b128 v[76:79], v253\n"
    "ds_read_b64_tr_b16 v[240:241], v13 offset:9216\n"
    "ds_read_b64_tr_b16 v[242:243], v13 offset:11520\n"
    "s_waitcnt lgkmcnt(3)\n"
    "v_mfma_f32_32x32x16_bf16 v[224:239], v[72:75], v[248:251], v[224:239]\n"
    "ds_read_b64_tr_b16 v[244:245], v14 offset:9216\n"
    "ds_read_b64_tr_b16 v[246:247], v14 offset:11520\n"
    "s_add_u32 m0, s67, 124928\n"
    "s_add_u32 s78, s56, 640\n"
    "buffer_load_dwordx4 v11, s[8:11], s78 offen lds\n"
    "s_waitcnt lgkmcnt(2)\n"
    "v_mfma_f32_32x32x16_bf16 v[160:175], v[76:79], v[240:243], v[160:175]\n"
    "ds_read_b64_tr_b16 v[248:249], v15 offset:9216\n"
    "ds_read_b64_tr_b16 v[250:251], v15 offset:11520\n"
    "s_waitcnt lgkmcnt(2)\n"
    "v_mfma_f32_32x32x16_bf16 v[176:191], v[76:79], v[244:247], v[176:191]\n"
    "ds_read_b64_tr_b16 v[240:241], v0 offset:9216\n"
    "ds_read_b64_tr_b16 v[242:243], v0 offset:11520\n"
    "s_add_u32 m0, s67, 125952\n"
    "s_add_u32 s78, s56, 640\n"
    "buffer_load_dwordx4 v12, s[8:11], s78 offen lds\n"
    "s_waitcnt lgkmcnt(2)\n"
    "v_mfma_f32_32x32x16_bf16 v[192:207], v[76:79], v[248:251], v[192:207]\n"
    "ds_read_b64_tr_b16 v[244:245], v3 offset:9216\n"
    "ds_read_b64_tr_b16 v[246:247], v3 offset:11520\n"
    "s_waitcnt lgkmcnt(2)\n"
    "v_mfma_f32_32x32x16_bf16 v[208:223], v[76:79], v[240:243], v[208:223]\n"
    "s_waitcnt vmcnt(8)\n"
    "s_barrier\n"
    "v_add_u32 v252, 16384, v252\n"
    "ds_read_b128 v[80:83], v252\n"
    "v_add_u32 v13, 18432, v13\n"
    "ds_read_b64_tr_b16 v[248:249], v13 offset:0\n"
    "ds_read_b64_tr_b16 v[250:251], v13 offset:2304\n"
    "s_add_u32 m0, s69, 18432\n"
    "s_add_u32 s78, s57, 1216512\n"
    "s_bfm_b64 exec, 54, 0\n"
    "buffer_load_dwordx4 v8, s[12:15], s78 offen lds\n"
    "s_mov_b64 exec, -1\n"
    "s_waitcnt lgkmcnt(3)\n"
    "v_mfma_f32_32x32x16_bf16 v[224:239], v[76:79], v[244:247], v[224:239]\n"
    "v_add_u32 v14, 18432, v14\n"
    "ds_read_b64_tr_b16 v[240:241], v14 offset:0\n"
    "ds_read_b64_tr_b16 v[242:243], v14 offset:2304\n"
    "s_waitcnt lgkmcnt(2)\n"
    "v_mfma_f32_32x32x16_bf16 v[160:175], v[80:83], v[248:251], v[160:175]\n"
    "v_add_u32 v15, 18432, v15\n"
    "ds_read_b64_tr_b16 v[244:245], v15 offset:0\n"
    "ds_read_b64_tr_b16 v[246:247], v15 offset:2304\n"
    "s_waitcnt lgkmcnt(2)\n"
    "v_mfma_f32_32x32x16_bf16 v[176:191], v[80:83], v[240:243], v[176:191]\n"
    "v_add_u32 v0, 18432, v0\n"
    "ds_read_b64_tr_b16 v[248:249], v0 offset:0\n"
    "ds_read_b64_tr_b16 v[250:251], v0 offset:2304\n"
    "s_add_u32 m0, s69, 19296\n"
    "s_add_u32 s78, s57, 1216512\n"
    "s_bfm_b64 exec, 54, 0\n"
    "buffer_load_dwordx4 v9, s[12:15], s78 offen lds\n"
    "s_mov_b64 exec, -1\n"
    "s_waitcnt lgkmcnt(2)\n"
    "v_mfma_f32_32x32x16_bf16 v[192:207], v[80:83], v[244:247], v[192:207]\n"
    "v_add_u32 v3, 18432, v3\n"
    "ds_read_b64_tr_b16 v[240:241], v3 offset:0\n"
    "ds_read_b64_tr_b16 v[242:243], v3 offset:2304\n"
    "s_waitcnt lgkmcnt(2)\n"
    "v_mfma_f32_32x32x16_bf16 v[208:223], v[80:83], v[248:251], v[208:223]\n"
    "v_xor_b32 v253, 32, v252\n"
    "ds_read_b128 v[84:87], v253\n"
    "ds_read_b64_tr_b16 v[244:245], v13 offset:9216\n"
    "ds_read_b64_tr_b16 v[246:247], v13 offset:11520\n"
    "s_waitcnt lgkmcnt(3)\n"
    "v_mfma_f32_32x32x16_bf16 v[224:239], v[80:83], v[240:243], v[224:239]\n"
    "ds_read_b64_tr_b16 v[248:249], v14 offset:9216\n"
    "ds_read_b64_tr_b16 v[250:251], v14 offset:11520\n"
    "s_add_u32 m0, s69, 20160\n"
    "s_add_u32 s78, s57, 1216512\n"
    "s_bfm_b64 exec, 36, 0\n"
    "buffer_load_dwordx4 v10, s[12:15], s78 offen lds\n"
    "s_mov_b64 exec, -1\n"
    "s_waitcnt lgkmcnt(2)\n"
    "v_mfma_f32_32x32x16_bf16 v[160:175], v[84:87], v[244:247], v[160:175]\n"
    "ds_read_b64_tr_b16 v[240:241], v15 offset:9216\n"
    "ds_read_b64_tr_b16 v[242:243], v15 offset:11520\n"
    "s_waitcnt lgkmcnt(2)\n"
    "v_mfma_f32_32x32x16_bf16 v[176:191], v[84:87], v[248:251], v[176:191]\n"
    "ds_read_b64_tr_b16 v[244:245], v0 offset:9216\n"
    "ds_read_b64_tr_b16 v[246:247], v0 offset:11520\n"
    "s_waitcnt lgkmcnt(2)\n"
    "v_mfma_f32_32x32x16_bf16 v[192:207], v[84:87], v[240:243], v[192:207]\n"
    "ds_read_b64_tr_b16 v[248:249], v3 offset:9216\n"
    "ds_read_b64_tr_b16 v[250:251], v3 offset:11520\n"
    "s_waitcnt lgkmcnt(2)\n"
    "v_mfma_f32_32x32x16_bf16 v[208:223], v[84:87], v[244:247], v[208:223]\n"
    "s_waitcnt vmcnt(8)\n"
    "s_barrier\n"
    "v_xor_b32 v253, 64, v252\n"
    "ds_read_b128 v[88:91], v253\n"
    "v_add_u32 v13, 18432, v13\n"
    "ds_read_b64_tr_b16 v[240:241], v13 offset:0\n"
    "ds_read_b64_tr_b16 v[242:243], v13 offset:2304\n"
    "s_add_u32 m0, s69, 36864\n"
    "s_add_u32 s78, s57, 1327104\n"
    "s_bfm_b64 exec, 54, 0\n"
    "buffer_load_dwordx4 v8, s[12:15], s78 offen lds\n"
    "s_mov_b64 exec, -1\n"
    "s_waitcnt lgkmcnt(3)\n"
    "v_mfma_f32_32x32x16_bf16 v[224:239], v[84:87], v[248:251], v[224:239]\n"
    "v_add_u32 v14, 18432, v14\n"
    "ds_read_b64_tr_b16 v[244:245], v14 offset:0\n"
    "ds_read_b64_tr_b16 v[246:247], v14 offset:2304\n"
    "s_waitcnt lgkmcnt(2)\n"
    "v_mfma_f32_32x32x16_bf16 v[160:175], v[88:91], v[240:243], v[160:175]\n"
    "v_add_u32 v15, 18432, v15\n"
    "ds_read_b64_tr_b16 v[248:249], v15 offset:0\n"
    "ds_read_b64_tr_b16 v[250:251], v15 offset:2304\n"
    "s_add_u32 m0, s69, 37728\n"
    "s_add_u32 s78, s57, 1327104\n"
    "s_bfm_b64 exec, 54, 0\n"
    "buffer_load_dwordx4 v9, s[12:15], s78 offen lds\n"
    "s_mov_b64 exec, -1\n"
    "s_waitcnt lgkmcnt(2)\n"
    "v_mfma_f32_32x32x16_bf16 v[176:191], v[88:91], v[244:247], v[176:191]\n"
    "v_add_u32 v0, 18432, v0\n"
    "ds_read_b64_tr_b16 v[240:241], v0 offset:0\n"
    "ds_read_b64_tr_b16 v[242:243], v0 offset:2304\n"
    "s_waitcnt lgkmcnt(2)\n"
    "v_mfma_f32_32x32x16_bf16 v[192:207], v[88:91], v[248:251], v[192:207]\n"
    "v_add_u32 v3, 18432, v3\n"
    "ds_read_b64_tr_b16 v[244:245], v3 offset:0\n"
    "ds_read_b64_tr_b16 v[246:247], v3 offset:2304\n"
    "s_add_u32 m0, s69, 38592\n"
    "s_add_u32 s78, s57, 1327104\n"
    "s_bfm_b64 exec, 36, 0\n"
    "buffer_load_dwordx4 v10, s[12:15], s78 offen lds\n"
    "s_mov_b64 exec, -1\n"
    "s_waitcnt lgkmcnt(2)\n"
    "v_mfma_f32_32x32x16_bf16 v[208:223], v[88:91], v[240:243], v[208:223]\n"
    "v_xor_b32 v253, 96, v252\n"
    "ds_read_b128 v[92:95], v253\n"
    "ds_read_b64_tr_b16 v[248:249], v13 offset:9216\n"
    "ds_read_b64_tr_b16 v[250:251], v13 offset:11520\n"
    "s_waitcnt lgkmcnt(3)\n"
    "v_mfma_f32_32x32x16_bf16 v[224:239], v[88:91], v[244:247], v[224:239]\n"
    "ds_read_b64_tr_b16 v[240:241], v14 offset:9216\n"
    "ds_read_b64_tr_b16 v[242:243], v14 offset:11520\n"
    "s_add_u32 m0, s67, 92160\n"
    "s_add_u32 s78, s56, 768\n"
    "buffer_load_dwordx4 v11, s[8:11], s78 offen lds\n"
    "s_waitcnt lgkmcnt(2)\n"
    "v_mfma_f32_32x32x16_bf16 v[160:175], v[92:95], v[248:251], v[160:175]\n"
    "ds_read_b64_tr_b16 v[244:245], v15 offset:9216\n"
    "ds_read_b64_tr_b16 v[246:247], v15 offset:11520\n"
    "s_waitcnt lgkmcnt(2)\n"
    "v_mfma_f32_32x32x16_bf16 v[176:191], v[92:95], v[240:243], v[176:191]\n"
    "ds_read_b64_tr_b16 v[248:249], v0 offset:9216\n"
    "ds_read_b64_tr_b16 v[250:251], v0 offset:11520\n"
    "s_add_u32 m0, s67, 93184\n"
    "s_add_u32 s78, s56, 768\n"
    "buffer_load_dwordx4 v12, s[8:11], s78 offen lds\n"
    "s_waitcnt lgkmcnt(2)\n"
    "v_mfma_f32_32x32x16_bf16 v[192:207], v[92:95], v[244:247], v[192:207]\n"
    "ds_read_b64_tr_b16 v[240:241], v3 offset:9216\n"
    "ds_read_b64_tr_b16 v[242:243], v3 offset:11520\n"
    "s_waitcnt lgkmcnt(2)\n"
    "v_mfma_f32_32x32x16_bf16 v[208:223], v[92:95], v[248:251], v[208:223]\n"
    "s_waitcnt vmcnt(8)\n"
    "s_barrier\n"
    "v_add_u32 v252, 16384, v252\n"
    "ds_read_b128 v[96:99], v252\n"
    "v_add_u32 v13, 4294893568, v13\n"
    "ds_read_b64_tr_b16 v[244:245], v13 offset:0\n"
    "ds_read_b64_tr_b16 v[246:247], v13 offset:2304\n"
    "s_add_u32 m0, s69, 55296\n"
    "s_add_u32 s78, s57, 1437696\n"
    "s_bfm_b64 exec, 54, 0\n"
    "buffer_load_dwordx4 v8, s[12:15], s78 offen lds\n"
    "s_mov_b64 exec, -1\n"
    "s_waitcnt lgkmcnt(3)\n"
    "v_mfma_f32_32x32x16_bf16 v[224:239], v[92:95], v[240:243], v[224:239]\n"
    "v_add_u32 v14, 4294893568, v14\n"
    "ds_read_b64_tr_b16 v[248:249], v14 offset:0\n"
    "ds_read_b64_tr_b16 v[250:251], v14 offset:2304\n"
    "s_waitcnt lgkmcnt(2)\n"
    "v_mfma_f32_32x32x16_bf16 v[160:175], v[96:99], v[244:247], v[160:175]\n"
    "v_add_u32 v15, 4294893568, v15\n"
    "ds_read_b64_tr_b16 v[240:241], v15 offset:0\n"
    "ds_read_b64_tr_b16 v[242:243], v15 offset:2304\n"
    "s_waitcnt lgkmcnt(2)\n"
    "v_mfma_f32_32x32x16_bf16 v[176:191], v[96:99], v[248:251], v[176:191]\n"
    "v_add_u32 v0, 4294893568, v0\n"
    "ds_read_b64_tr_b16 v[244:245], v0 offset:0\n"
    "ds_read_b64_tr_b16 v[246:247], v0 offset:2304\n"
    "s_add_u32 m0, s69, 56160\n"
    "s_add_u32 s78, s57, 1437696\n"
    "s_bfm_b64 exec, 54, 0\n"
    "buffer_load_dwordx4 v9, s[12:15], s78 offen lds\n"
    "s_mov_b64 exec, -1\n"
    "s_waitcnt lgkmcnt(2)\n"
    "v_mfma_f32_32x32x16_bf16 v[192:207], v[96:99], v[240:243], v[192:207]\n"
    "v_add_u32 v3, 4294893568, v3\n"
    "ds_read_b64_tr_b16 v[248:249], v3 offset:0\n"
    "ds_read_b64_tr_b16 v[250:251], v3 offset:2304\n"
    "s_waitcnt lgkmcnt(2)\n"
    "v_mfma_f32_32x32x16_bf16 v[208:223], v[96:99], v[244:247], v[208:223]\n"
    "v_xor_b32 v253, 32, v252\n"
    "ds_read_b128 v[100:103], v253\n"
    "ds_read_b64_tr_b16 v[240:241], v13 offset:9216\n"
    "ds_read_b64_tr_b16 v[242:243], v13 offset:11520\n"
    "s_waitcnt lgkmcnt(3)\n"
    "v_mfma_f32_32x32x16_bf16 v[224:239], v[96:99], v[248:251], v[224:239]\n"
    "ds_read_b64_tr_b16 v[244:245], v14 offset:9216\n"
    "ds_read_b64_tr_b16 v[246:247], v14 offset:11520\n"
    "s_add_u32 m0, s69, 57024\n"
    "s_add_u32 s78, s57, 1437696\n"
    "s_bfm_b64 exec, 36, 0\n"
    "buffer_load_dwordx4 v10, s[12:15], s78 offen lds\n"
    "s_mov_b64 exec, -1\n"
    "s_waitcnt lgkmcnt(2)\n"
    "v_mfma_f32_32x32x16_bf16 v[160:175], v[100:103], v[240:243], v[160:175]\n"
    "ds_read_b64_tr_b16 v[248:249], v15 offset:9216\n"
    "ds_read_b64_tr_b16 v[250:251], v15 offset:11520\n"
    "s_waitcnt lgkmcnt(2)\n"
    "v_mfma_f32_32x32x16_bf16 v[176:191], v[100:103], v[244:247], v[176:191]\n"
    "ds_read_b64_tr_b16 v[240:241], v0 offset:9216\n"
    "ds_read_b64_tr_b16 v[242:243], v0 offset:11520\n"
    "s_waitcnt lgkmcnt(2)\n"
    "v_mfma_f32_32x32x16_bf16 v[192:207], v[100:103], v[248:251], v[192:207]\n"
    "ds_read_b64_tr_b16 v[244:245], v3 offset:9216\n"
    "ds_read_b64_tr_b16 v[246:247], v3 offset:11520\n"
    "s_waitcnt lgkmcnt(2)\n"
    "v_mfma_f32_32x32x16_bf16 v[208:223], v[100:103], v[240:243], v[208:223]\n"
    "s_waitcnt vmcnt(8)\n"
    "s_barrier\n"
    "v_xor_b32 v253, 64, v252\n"
    "ds_read_b128 v[104:107], v253\n"
    "v_add_u32 v13, 18432, v13\n"
    "ds_read_b64_tr_b16 v[248:249], v13 offset:0\n"
    "ds_read_b64_tr_b16 v[250:251], v13 offset:2304\n"
    "s_add_u32 m0, s69, 73728\n"
    "s_add_u32 s78, s57, 1548288\n"
    "s_bfm_b64 exec, 54, 0\n"
    "buffer_load_dwordx4 v8, s[12:15], s78 offen lds\n"
    "s_mov_b64 exec, -1\n"
    "s_waitcnt lgkmcnt(3)\n"
    "v_mfma_f32_32x32x16_bf16 v[224:239], v[100:103], v[244:247], v[224:239]\n"
    "v_add_u32 v14, 18432, v14\n"
    "ds_read_b64_tr_b16 v[240:241], v14 offset:0\n"
    "ds_read_b64_tr_b16 v[242:243], v14 offset:2304\n"
    "s_waitcnt lgkmcnt(2)\n"
    "v_mfma_f32_32x32x16_bf16 v[160:175], v[104:107], v[248:251], v[160:175]\n"
    "v_add_u32 v15, 18432, v15\n"
    "ds_read_b64_tr_b16 v[244:245], v15 offset:0\n"
    "ds_read_b64_tr_b16 v[246:247], v15 offset:2304\n"
    "s_add_u32 m0, s69, 74592\n"
    "s_add_u32 s78, s57, 1548288\n"
    "s_bfm_b64 exec, 54, 0\n"
    "buffer_load_dwordx4 v9, s[12:15], s78 offen lds\n"
    "s_mov_b64 exec, -1\n"
    "s_waitcnt lgkmcnt(2)\n"
    "v_mfma_f32_32x32x16_bf16 v[176:191], v[104:107], v[240:243], v[176:191]\n"
    "v_add_u32 v0, 18432, v0\n"
    "ds_read_b64_tr_b16 v[248:249], v0 offset:0\n"
    "ds_read_b64_tr_b16 v[250:251], v0 offset:2304\n"
    "s_waitcnt lgkmcnt(2)\n"
    "v_mfma_f32_32x32x16_bf16 v[192:207], v[104:107], v[244:247], v[192:207]\n"
    "v_add_u32 v3, 18432, v3\n"
    "ds_read_b64_tr_b16 v[240:241], v3 offset:0\n"
    "ds_read_b64_tr_b16 v[242:243], v3 offset:2304\n"
    "s_add_u32 m0, s69, 75456\n"
    "s_add_u32 s78, s57, 1548288\n"
    "s_bfm_b64 exec, 36, 0\n"
    "buffer_load_dwordx4 v10, s[12:15], s78 offen lds\n"
    "s_mov_b64 exec, -1\n"
    "s_waitcnt lgkmcnt(2)\n"
    "v_mfma_f32_32x32x16_bf16 v[208:223], v[104:107], v[248:251], v[208:223]\n"
    "v_xor_b32 v253, 96, v252\n"
    "ds_read_b128 v[108:111], v253\n"
    "ds_read_b64_tr_b16 v[244:245], v13 offset:9216\n"
    "ds_read_b64_tr_b16 v[246:247], v13 offset:11520\n"
    "s_waitcnt lgkmcnt(3)\n"
    "v_mfma_f32_32x32x16_bf16 v[224:239], v[104:107], v[240:243], v[224:239]\n"
    "ds_read_b64_tr_b16 v[248:249], v14 offset:9216\n"
    "ds_read_b64_tr_b16 v[250:251], v14 offset:11520\n"
    "s_add_u32 m0, s67, 108544\n"
    "s_add_u32 s78, s56, 896\n"
    "buffer_load_dwordx4 v11, s[8:11], s78 offen lds\n"
    "s_waitcnt lgkmcnt(2)\n"
    "v_mfma_f32_32x32x16_bf16 v[160:175], v[108:111], v[244:247], v[160:175]\n"
    "ds_read_b64_tr_b16 v[240:241], v15 offset:9216\n"
    "ds_read_b64_tr_b16 v[242:243], v15 offset:11520\n"
    "s_waitcnt lgkmcnt(2)\n"
    "v_mfma_f32_32x32x16_bf16 v[176:191], v[108:111], v[248:251], v[176:191]\n"
    "ds_read_b64_tr_b16 v[244:245], v0 offset:9216\n"
    "ds_read_b64_tr_b16 v[246:247], v0 offset:11520\n"
    "s_add_u32 m0, s67, 109568\n"
    "s_add_u32 s78, s56, 896\n"
    "buffer_load_dwordx4 v12, s[8:11], s78 offen lds\n"
    "s_waitcnt lgkmcnt(2)\n"
    "v_mfma_f32_32x32x16_bf16 v[192:207], v[108:111], v[240:243], v[192:207]\n"
    "ds_read_b64_tr_b16 v[248:249], v3 offset:9216\n"
    "ds_read_b64_tr_b16 v[250:251], v3 offset:11520\n"
    "s_waitcnt lgkmcnt(2)\n"
    "v_mfma_f32_32x32x16_bf16 v[208:223], v[108:111], v[244:247], v[208:223]\n"
    "s_waitcnt vmcnt(8)\n"
    "s_barrier\n"
    "v_add_u32 v252, 4294934528, v252\n"
    "ds_read_b128 v[112:115], v252\n"
    "v_add_u32 v13, 18432, v13\n"
    "ds_read_b64_tr_b16 v[240:241], v13 offset:0\n"
    "ds_read_b64_tr_b16 v[242:243], v13 offset:2304\n"
    "s_add_u32 m0, s69, 0\n"
    "s_add_u32 s78, s57, 1658880\n"
    "s_bfm_b64 exec, 54, 0\n"
    "buffer_load_dwordx4 v8, s[12:15], s78 offen lds\n"
    "s_mov_b64 exec, -1\n"
    "s_waitcnt lgkmcnt(3)\n"
    "v_mfma_f32_32x32x16_bf16 v[224:239], v[108:111], v[248:251], v[224:239]\n"
    "v_add_u32 v14, 18432, v14\n"
    "ds_read_b64_tr_b16 v[244:245], v14 offset:0\n"
    "ds_read_b64_tr_b16 v[246:247], v14 offset:2304\n"
    "s_waitcnt lgkmcnt(2)\n"
    "v_mfma_f32_32x32x16_bf16 v[160:175], v[112:115], v[240:243], v[160:175]\n"
    "v_add_u32 v15, 18432, v15\n"
    "ds_read_b64_tr_b16 v[248:249], v15 offset:0\n"
    "ds_read_b64_tr_b16 v[250:251], v15 offset:2304\n"
    "s_waitcnt lgkmcnt(2)\n"
    "v_mfma_f32_32x32x16_bf16 v[176:191], v[112:115], v[244:247], v[176:191]\n"
    "v_add_u32 v0, 18432, v0\n"
    "ds_read_b64_tr_b16 v[240:241], v0 offset:0\n"
    "ds_read_b64_tr_b16 v[242:243], v0 offset:2304\n"
    "s_add_u32 m0, s69, 864\n"
    "s_add_u32 s78, s57, 1658880\n"
    "s_bfm_b64 exec, 54, 0\n"
    "buffer_load_dwordx4 v9, s[12:15], s78 offen lds\n"
    "s_mov_b64 exec, -1\n"
    "s_waitcnt lgkmcnt(2)\n"
    "v_mfma_f32_32x32x16_bf16 v[192:207], v[112:115], v[248:251], v[192:207]\n"
    "v_add_u32 v3, 18432, v3\n"
    "ds_read_b64_tr_b16 v[244:245], v3 offset:0\n"
    "ds_read_b64_tr_b16 v[246:247], v3 offset:2304\n"
    "s_waitcnt lgkmcnt(2)\n"
    "v_mfma_f32_32x32x16_bf16 v[208:223], v[112:115], v[240:243], v[208:223]\n"
    "v_xor_b32 v253, 32, v252\n"
    "ds_read_b128 v[116:119], v253\n"
    "ds_read_b64_tr_b16 v[248:249], v13 offset:9216\n"
    "ds_read_b64_tr_b16 v[250:251], v13 offset:11520\n"
    "s_waitcnt lgkmcnt(3)\n"
    "v_mfma_f32_32x32x16_bf16 v[224:239], v[112:115], v[244:247], v[224:239]\n"
    "ds_read_b64_tr_b16 v[240:241], v14 offset:9216\n"
    "ds_read_b64_tr_b16 v[242:243], v14 offset:11520\n"
    "s_add_u32 m0, s69, 1728\n"
    "s_add_u32 s78, s57, 1658880\n"
    "s_bfm_b64 exec, 36, 0\n"
    "buffer_load_dwordx4 v10, s[12:15], s78 offen lds\n"
    "s_mov_b64 exec, -1\n"
    "s_waitcnt lgkmcnt(2)\n"
    "v_mfma_f32_32x32x16_bf16 v[160:175], v[116:119], v[248:251], v[160:175]\n"
    "ds_read_b64_tr_b16 v[244:245], v15 offset:9216\n"
    "ds_read_b64_tr_b16 v[246:247], v15 offset:11520\n"
    "s_waitcnt lgkmcnt(2)\n"
    "v_mfma_f32_32x32x16_bf16 v[176:191], v[116:119], v[240:243], v[176:191]\n"
    "ds_read_b64_tr_b16 v[248:249], v0 offset:9216\n"
    "ds_read_b64_tr_b16 v[250:251], v0 offset:11520\n"
    "s_waitcnt lgkmcnt(2)\n"
    "v_mfma_f32_32x32x16_bf16 v[192:207], v[116:119], v[244:247], v[192:207]\n"
    "ds_read_b64_tr_b16 v[240:241], v3 offset:9216\n"
    "ds_read_b64_tr_b16 v[242:243], v3 offset:11520\n"
    "s_waitcnt lgkmcnt(2)\n"
    "v_mfma_f32_32x32x16_bf16 v[208:223], v[116:119], v[248:251], v[208:223]\n"
    "s_waitcnt vmcnt(8)\n"
    "s_barrier\n"
    "v_xor_b32 v253, 64, v252\n"
    "ds_read_b128 v[120:123], v253\n"
    "v_add_u32 v13, 18432, v13\n"
    "ds_read_b64_tr_b16 v[244:245], v13 offset:0\n"
    "ds_read_b64_tr_b16 v[246:247], v13 offset:2304\n"
    "s_add_u32 m0, s69, 18432\n"
    "s_add_u32 s78, s57, 1769472\n"
    "s_bfm_b64 exec, 54, 0\n"
    "buffer_load_dwordx4 v8, s[12:15], s78 offen lds\n"
    "s_mov_b64 exec, -1\n"
    "s_waitcnt lgkmcnt(3)\n"
    "v_mfma_f32_32x32x16_bf16 v[224:239], v[116:119], v[240:243], v[224:239]\n"
    "v_add_u32 v14, 18432, v14\n"
    "ds_read_b64_tr_b16 v[248:249], v14 offset:0\n"
    "ds_read_b64_tr_b16 v[250:251], v14 offset:2304\n"
    "s_waitcnt lgkmcnt(2)\n"
    "v_mfma_f32_32x32x16_bf16 v[160:175], v[120:123], v[244:247], v[160:175]\n"
    "v_add_u32 v15, 18432, v15\n"
    "ds_read_b64_tr_b16 v[240:241], v15 offset:0\n"
    "ds_read_b64_tr_b16 v[242:243], v15 offset:2304\n"
    "s_add_u32 m0, s69, 19296\n"
    "s_add_u32 s78, s57, 1769472\n"
    "s_bfm_b64 exec, 54, 0\n"
    "buffer_load_dwordx4 v9, s[12:15], s78 offen lds\n"
    "s_mov_b64 exec, -1\n"
    "s_waitcnt lgkmcnt(2)\n"
    "v_mfma_f32_32x32x16_bf16 v[176:191], v[120:123], v[248:251], v[176:191]\n"
    "v_add_u32 v0, 18432, v0\n"
    "ds_read_b64_tr_b16 v[244:245], v0 offset:0\n"
    "ds_read_b64_tr_b16 v[246:247], v0 offset:2304\n"
    "s_waitcnt lgkmcnt(2)\n"
    "v_mfma_f32_32x32x16_bf16 v[192:207], v[120:123], v[240:243], v[192:207]\n"
    "v_add_u32 v3, 18432, v3\n"
    "ds_read_b64_tr_b16 v[248:249], v3 offset:0\n"
    "ds_read_b64_tr_b16 v[250:251], v3 offset:2304\n"
    "s_add_u32 m0, s69, 20160\n"
    "s_add_u32 s78, s57, 1769472\n"
    "s_bfm_b64 exec, 36, 0\n"
    "buffer_load_dwordx4 v10, s[12:15], s78 offen lds\n"
    "s_mov_b64 exec, -1\n"
    "s_waitcnt lgkmcnt(2)\n"
    "v_mfma_f32_32x32x16_bf16 v[208:223], v[120:123], v[244:247], v[208:223]\n"
    "v_xor_b32 v253, 96, v252\n"
    "ds_read_b128 v[124:127], v253\n"
    "ds_read_b64_tr_b16 v[240:241], v13 offset:9216\n"
    "ds_read_b64_tr_b16 v[242:243], v13 offset:11520\n"
    "s_waitcnt lgkmcnt(3)\n"
    "v_mfma_f32_32x32x16_bf16 v[224:239], v[120:123], v[248:251], v[224:239]\n"
    "ds_read_b64_tr_b16 v[244:245], v14 offset:9216\n"
    "ds_read_b64_tr_b16 v[246:247], v14 offset:11520\n"
    "s_add_u32 m0, s67, 124928\n"
    "s_add_u32 s78, s56, 1024\n"
    "buffer_load_dwordx4 v11, s[8:11], s78 offen lds\n"
    "s_waitcnt lgkmcnt(2)\n"
    "v_mfma_f32_32x32x16_bf16 v[160:175], v[124:127], v[240:243], v[160:175]\n"
    "ds_read_b64_tr_b16 v[248:249], v15 offset:9216\n"
    "ds_read_b64_tr_b16 v[250:251], v15 offset:11520\n"
    "s_waitcnt lgkmcnt(2)\n"
    "v_mfma_f32_32x32x16_bf16 v[176:191], v[124:127], v[244:247], v[176:191]\n"
    "ds_read_b64_tr_b16 v[240:241], v0 offset:9216\n"
    "ds_read_b64_tr_b16 v[242:243], v0 offset:11520\n"
    "s_add_u32 m0, s67, 125952\n"
    "s_add_u32 s78, s56, 1024\n"
    "buffer_load_dwordx4 v12, s[8:11], s78 offen lds\n"
    "s_waitcnt lgkmcnt(2)\n"
    "v_mfma_f32_32x32x16_bf16 v[192:207], v[124:127], v[248:251], v[192:207]\n"
    "ds_read_b64_tr_b16 v[244:245], v3 offset:9216\n"
    "ds_read_b64_tr_b16 v[246:247], v3 offset:11520\n"
    "s_waitcnt lgkmcnt(2)\n"
    "v_mfma_f32_32x32x16_bf16 v[208:223], v[124:127], v[240:243], v[208:223]\n"
    "s_waitcnt vmcnt(8)\n"
    "s_barrier\n"
    "v_add_u32 v252, 16384, v252\n"
    "ds_read_b128 v[128:131], v252\n"
    "v_add_u32 v13, 18432, v13\n"
    "ds_read_b64_tr_b16 v[248:249], v13 offset:0\n"
    "ds_read_b64_tr_b16 v[250:251], v13 offset:2304\n"
    "s_add_u32 m0, s69, 36864\n"
    "s_add_u32 s78, s57, 1880064\n"
    "s_bfm_b64 exec, 54, 0\n"
    "buffer_load_dwordx4 v8, s[12:15], s78 offen lds\n"
    "s_mov_b64 exec, -1\n"
    "s_waitcnt lgkmcnt(3)\n"
    "v_mfma_f32_32x32x16_bf16 v[224:239], v[124:127], v[244:247], v[224:239]\n"
    "v_add_u32 v14, 18432, v14\n"
    "ds_read_b64_tr_b16 v[240:241], v14 offset:0\n"
    "ds_read_b64_tr_b16 v[242:243], v14 offset:2304\n"
    "s_waitcnt lgkmcnt(2)\n"
    "v_mfma_f32_32x32x16_bf16 v[160:175], v[128:131], v[248:251], v[160:175]\n"
    "v_add_u32 v15, 18432, v15\n"
    "ds_read_b64_tr_b16 v[244:245], v15 offset:0\n"
    "ds_read_b64_tr_b16 v[246:247], v15 offset:2304\n"
    "s_waitcnt lgkmcnt(2)\n"
    "v_mfma_f32_32x32x16_bf16 v[176:191], v[128:131], v[240:243], v[176:191]\n"
    "v_add_u32 v0, 18432, v0\n"
    "ds_read_b64_tr_b16 v[248:249], v0 offset:0\n"
    "ds_read_b64_tr_b16 v[250:251], v0 offset:2304\n"
    "s_add_u32 m0, s69, 37728\n"
    "s_add_u32 s78, s57, 1880064\n"
    "s_bfm_b64 exec, 54, 0\n"
    "buffer_load_dwordx4 v9, s[12:15], s78 offen lds\n"
    "s_mov_b64 exec, -1\n"
    "s_waitcnt lgkmcnt(2)\n"
    "v_mfma_f32_32x32x16_bf16 v[192:207], v[128:131], v[244:247], v[192:207]\n"
    "v_add_u32 v3, 18432, v3\n"
    "ds_read_b64_tr_b16 v[240:241], v3 offset:0\n"
    "ds_read_b64_tr_b16 v[242:243], v3 offset:2304\n"
    "s_waitcnt lgkmcnt(2)\n"
    "v_mfma_f32_32x32x16_bf16 v[208:223], v[128:131], v[248:251], v[208:223]\n"
    "v_xor_b32 v253, 32, v252\n"
    "ds_read_b128 v[132:135], v253\n"
    "ds_read_b64_tr_b16 v[244:245], v13 offset:9216\n"
    "ds_read_b64_tr_b16 v[246:247], v13 offset:11520\n"
    "s_waitcnt lgkmcnt(3)\n"
    "v_mfma_f32_32x32x16_bf16 v[224:239], v[128:131], v[240:243], v[224:239]\n"
    "ds_read_b64_tr_b16 v[248:249], v14 offset:9216\n"
    "ds_read_b64_tr_b16 v[250:251], v14 offset:11520\n"
    "s_add_u32 m0, s69, 38592\n"
    "s_add_u32 s78, s57, 1880064\n"
    "s_bfm_b64 exec, 36, 0\n"
    "buffer_load_dwordx4 v10, s[12:15], s78 offen lds\n"
    "s_mov_b64 exec, -1\n"
    "s_waitcnt lgkmcnt(2)\n"
    "v_mfma_f32_32x32x16_bf16 v[160:175], v[132:135], v[244:247], v[160:175]\n"
    "ds_read_b64_tr_b16 v[240:241], v15 offset:9216\n"
    "ds_read_b64_tr_b16 v[242:243], v15 offset:11520\n"
    "s_waitcnt lgkmcnt(2)\n"
    "v_mfma_f32_32x32x16_bf16 v[176:191], v[132:135], v[248:251], v[176:191]\n"
    "ds_read_b64_tr_b16 v[244:245], v0 offset:9216\n"
    "ds_read_b64_tr_b16 v[246:247], v0 offset:11520\n"
    "s_waitcnt lgkmcnt(2)\n"
    "v_mfma_f32_32x32x16_bf16 v[192:207], v[132:135], v[240:243], v[192:207]\n"
    "ds_read_b64_tr_b16 v[248:249], v3 offset:9216\n"
    "ds_read_b64_tr_b16 v[250:251], v3 offset:11520\n"
    "s_waitcnt lgkmcnt(2)\n"
    "v_mfma_f32_32x32x16_bf16 v[208:223], v[132:135], v[244:247], v[208:223]\n"
    "s_waitcnt vmcnt(8)\n"
    "s_barrier\n"
    "v_xor_b32 v253, 64, v252\n"
    "ds_read_b128 v[136:139], v253\n"
    "v_add_u32 v13, 4294893568, v13\n"
    "ds_read_b64_tr_b16 v[240:241], v13 offset:0\n"
    "ds_read_b64_tr_b16 v[242:243], v13 offset:2304\n"
    "v_mul_u32_u24 v4, 57, v1\n"
    "v_lshrrev_b32 v4, 10, v4\n"
    "v_mul_u32_u24 v5, 18, v4\n"
    "v_sub_u32 v5, v1, v5\n"
    "v_lshlrev_b32 v5, 4, v5\n"
    "s_lshl_b32 s79, s50, 3\n"
    "v_add_u32 v6, 0, v4\n"
    "v_and_b32 v7, 1, v6\n"
    "v_bfe_u32 v6, v6, 1, 2\n"
    "v_lshl_or_b32 v6, v7, 2, v6\n"
    "v_add_u32 v6, s79, v6\n"
    "v_mul_u32_u24 v6, 3456, v6\n"
    "v_add_u32 v8, v6, v5\n"
    "v_add_u32 v6, 3, v4\n"
    "v_and_b32 v7, 1, v6\n"
    "v_bfe_u32 v6, v6, 1, 2\n"
    "v_lshl_or_b32 v6, v7, 2, v6\n"
    "v_add_u32 v6, s79, v6\n"
    "v_mul_u32_u24 v6, 3456, v6\n"
    "v_add_u32 v9, v6, v5\n"
    "v_add_u32 v6, 6, v4\n"
    "v_and_b32 v7, 1, v6\n"
    "v_bfe_u32 v6, v6, 1, 2\n"
    "v_lshl_or_b32 v6, v7, 2, v6\n"
    "v_add_u32 v6, s79, v6\n"
    "v_mul_u32_u24 v6, 3456, v6\n"
    "v_add_u32 v10, v6, v5\n"
    "s_waitcnt lgkmcnt(3)\n"
    "v_mfma_f32_32x32x16_bf16 v[224:239], v[132:135], v[248:251], v[224:239]\n"
    "v_add_u32 v14, 4294893568, v14\n"
    "ds_read_b64_tr_b16 v[244:245], v14 offset:0\n"
    "ds_read_b64_tr_b16 v[246:247], v14 offset:2304\n"
    "s_waitcnt lgkmcnt(2)\n"
    "v_mfma_f32_32x32x16_bf16 v[160:175], v[136:139], v[240:243], v[160:175]\n"
    "v_add_u32 v15, 4294893568, v15\n"
    "ds_read_b64_tr_b16 v[248:249], v15 offset:0\n"
    "ds_read_b64_tr_b16 v[250:251], v15 offset:2304\n"
    "s_add_u32 m0, s69, 55296\n"
    "s_bfm_b64 exec, 54, 0\n"
    "buffer_load_dwordx4 v8, s[12:15], s65 offen lds\n"
    "s_mov_b64 exec, -1\n"
    "s_waitcnt lgkmcnt(2)\n"
    "v_mfma_f32_32x32x16_bf16 v[176:191], v[136:139], v[244:247], v[176:191]\n"
    "v_add_u32 v0, 4294893568, v0\n"
    "ds_read_b64_tr_b16 v[240:241], v0 offset:0\n"
    "ds_read_b64_tr_b16 v[242:243], v0 offset:2304\n"
    "s_waitcnt lgkmcnt(2)\n"
    "v_mfma_f32_32x32x16_bf16 v[192:207], v[136:139], v[248:251], v[192:207]\n"
    "v_add_u32 v3, 4294893568, v3\n"
    "ds_read_b64_tr_b16 v[244:245], v3 offset:0\n"
    "ds_read_b64_tr_b16 v[246:247], v3 offset:2304\n"
    "s_waitcnt lgkmcnt(2)\n"
    "v_mfma_f32_32x32x16_bf16 v[208:223], v[136:139], v[240:243], v[208:223]\n"
    "v_xor_b32 v253, 96, v252\n"
    "ds_read_b128 v[140:143], v253\n"
    "ds_read_b64_tr_b16 v[248:249], v13 offset:9216\n"
    "ds_read_b64_tr_b16 v[250:251], v13 offset:11520\n"
    "s_add_u32 m0, s69, 56160\n"
    "s_bfm_b64 exec, 54, 0\n"
    "buffer_load_dwordx4 v9, s[12:15], s65 offen lds\n"
    "s_mov_b64 exec, -1\n"
    "s_waitcnt lgkmcnt(3)\n"
    "v_mfma_f32_32x32x16_bf16 v[224:239], v[136:139], v[244:247], v[224:239]\n"
    "ds_read_b64_tr_b16 v[240:241], v14 offset:9216\n"
    "ds_read_b64_tr_b16 v[242:243], v14 offset:11520\n"
    "s_waitcnt lgkmcnt(2)\n"
    "v_mfma_f32_32x32x16_bf16 v[160:175], v[140:143], v[248:251], v[160:175]\n"
    "ds_read_b64_tr_b16 v[244:245], v15 offset:9216\n"
    "ds_read_b64_tr_b16 v[246:247], v15 offset:11520\n"
    "s_add_u32 m0, s69, 57024\n"
    "s_bfm_b64 exec, 36, 0\n"
    "buffer_load_dwordx4 v10, s[12:15], s65 offen lds\n"
    "s_mov_b64 exec, -1\n"
    "s_waitcnt lgkmcnt(2)\n"
    "v_mfma_f32_32x32x16_bf16 v[176:191], v[140:143], v[240:243], v[176:191]\n"
    "ds_read_b64_tr_b16 v[248:249], v0 offset:9216\n"
    "ds_read_b64_tr_b16 v[250:251], v0 offset:11520\n"
    "s_waitcnt lgkmcnt(2)\n"
    "v_mfma_f32_32x32x16_bf16 v[192:207], v[140:143], v[244:247], v[192:207]\n"
    "ds_read_b64_tr_b16 v[240:241], v3 offset:9216\n"
    "ds_read_b64_tr_b16 v[242:243], v3 offset:11520\n"
    "s_waitcnt lgkmcnt(2)\n"
    "v_mfma_f32_32x32x16_bf16 v[208:223], v[140:143], v[248:251], v[208:223]\n"
    "s_waitcnt vmcnt(6)\n"
    "s_barrier\n"
    "v_add_u32 v252, 16384, v252\n"
    "ds_read_b128 v[144:147], v252\n"
    "v_add_u32 v13, 18432, v13\n"
    "ds_read_b64_tr_b16 v[244:245], v13 offset:0\n"
    "ds_read_b64_tr_b16 v[246:247], v13 offset:2304\n"
    "s_waitcnt lgkmcnt(3)\n"
    "v_mfma_f32_32x32x16_bf16 v[224:239], v[140:143], v[240:243], v[224:239]\n"
    "v_add_u32 v14, 18432, v14\n"
    "ds_read_b64_tr_b16 v[248:249], v14 offset:0\n"
    "ds_read_b64_tr_b16 v[250:251], v14 offset:2304\n"
    "s_waitcnt lgkmcnt(2)\n"
    "v_mfma_f32_32x32x16_bf16 v[160:175], v[144:147], v[244:247], v[160:175]\n"
    "v_add_u32 v15, 18432, v15\n"
    "ds_read_b64_tr_b16 v[240:241], v15 offset:0\n"
    "ds_read_b64_tr_b16 v[242:243], v15 offset:2304\n"
    "s_waitcnt lgkmcnt(2)\n"
    "v_mfma_f32_32x32x16_bf16 v[176:191], v[144:147], v[248:251], v[176:191]\n"
    "v_add_u32 v0, 18432, v0\n"
    "ds_read_b64_tr_b16 v[244:245], v0 offset:0\n"
    "ds_read_b64_tr_b16 v[246:247], v0 offset:2304\n"
    "s_waitcnt lgkmcnt(2)\n"
    "v_mfma_f32_32x32x16_bf16 v[192:207], v[144:147], v[240:243], v[192:207]\n"
    "v_add_u32 v3, 18432, v3\n"
    "ds_read_b64_tr_b16 v[248:249], v3 offset:0\n"
    "ds_read_b64_tr_b16 v[250:251], v3 offset:2304\n"
    "s_waitcnt lgkmcnt(2)\n"
    "v_mfma_f32_32x32x16_bf16 v[208:223], v[144:147], v[244:247], v[208:223]\n"
    "v_xor_b32 v253, 32, v252\n"
    "ds_read_b128 v[148:151], v253\n"
    "ds_read_b64_tr_b16 v[240:241], v13 offset:9216\n"
    "ds_read_b64_tr_b16 v[242:243], v13 offset:11520\n"
    "s_waitcnt lgkmcnt(3)\n"
    "v_mfma_f32_32x32x16_bf16 v[224:239], v[144:147], v[248:251], v[224:239]\n"
    "ds_read_b64_tr_b16 v[244:245], v14 offset:9216\n"
    "ds_read_b64_tr_b16 v[246:247], v14 offset:11520\n"
    "s_waitcnt lgkmcnt(2)\n"
    "v_mfma_f32_32x32x16_bf16 v[160:175], v[148:151], v[240:243], v[160:175]\n"
    "ds_read_b64_tr_b16 v[248:249], v15 offset:9216\n"
    "ds_read_b64_tr_b16 v[250:251], v15 offset:11520\n"
    "s_waitcnt lgkmcnt(2)\n"
    "v_mfma_f32_32x32x16_bf16 v[176:191], v[148:151], v[244:247], v[176:191]\n"
    "ds_read_b64_tr_b16 v[240:241], v0 offset:9216\n"
    "ds_read_b64_tr_b16 v[242:243], v0 offset:11520\n"
    "s_waitcnt lgkmcnt(2)\n"
    "v_mfma_f32_32x32x16_bf16 v[192:207], v[148:151], v[248:251], v[192:207]\n"
    "ds_read_b64_tr_b16 v[244:245], v3 offset:9216\n"
    "ds_read_b64_tr_b16 v[246:247], v3 offset:11520\n"
    "s_waitcnt lgkmcnt(2)\n"
    "v_mfma_f32_32x32x16_bf16 v[208:223], v[148:151], v[240:243], v[208:223]\n"
    "s_waitcnt vmcnt(3)\n"
    "s_barrier\n"
    "v_xor_b32 v253, 64, v252\n"
    "ds_read_b128 v[152:155], v253\n"
    "v_add_u32 v13, 18432, v13\n"
    "ds_read_b64_tr_b16 v[248:249], v13 offset:0\n"
    "ds_read_b64_tr_b16 v[250:251], v13 offset:2304\n"
    "s_add_u32 m0, s69, 0\n"
    "s_add_u32 s78, s65, 221184\n"
    "s_bfm_b64 exec, 54, 0\n"
    "buffer_load_dwordx4 v8, s[12:15], s78 offen lds\n"
    "s_mov_b64 exec, -1\n"
    "s_waitcnt lgkmcnt(3)\n"
    "v_mfma_f32_32x32x16_bf16 v[224:239], v[148:151], v[244:247], v[224:239]\n"
    "v_add_u32 v14, 18432, v14\n"
    "ds_read_b64_tr_b16 v[240:241], v14 offset:0\n"
    "ds_read_b64_tr_b16 v[242:243], v14 offset:2304\n"
    "s_waitcnt lgkmcnt(2)\n"
    "v_mfma_f32_32x32x16_bf16 v[160:175], v[152:155], v[248:251], v[160:175]\n"
    "v_add_u32 v15, 18432, v15\n"
    "ds_read_b64_tr_b16 v[244:245], v15 offset:0\n"
    "ds_read_b64_tr_b16 v[246:247], v15 offset:2304\n"
    "s_add_u32 m0, s69, 864\n"
    "s_add_u32 s78, s65, 221184\n"
    "s_bfm_b64 exec, 54, 0\n"
    "buffer_load_dwordx4 v9, s[12:15], s78 offen lds\n"
    "s_mov_b64 exec, -1\n"
    "s_waitcnt lgkmcnt(2)\n"
    "v_mfma_f32_32x32x16_bf16 v[176:191], v[152:155], v[240:243], v[176:191]\n"
    "v_add_u32 v0, 18432, v0\n"
    "ds_read_b64_tr_b16 v[248:249], v0 offset:0\n"
    "ds_read_b64_tr_b16 v[250:251], v0 offset:2304\n"
    "s_waitcnt lgkmcnt(2)\n"
    "v_mfma_f32_32x32x16_bf16 v[192:207], v[152:155], v[244:247], v[192:207]\n"
    "v_add_u32 v3, 18432, v3\n"
    "ds_read_b64_tr_b16 v[240:241], v3 offset:0\n"
    "ds_read_b64_tr_b16 v[242:243], v3 offset:2304\n"
    "s_add_u32 m0, s69, 1728\n"
    "s_add_u32 s78, s65, 221184\n"
    "s_bfm_b64 exec, 36, 0\n"
    "buffer_load_dwordx4 v10, s[12:15], s78 offen lds\n"
    "s_mov_b64 exec, -1\n"
    "s_waitcnt lgkmcnt(2)\n"
    "v_mfma_f32_32x32x16_bf16 v[208:223], v[152:155], v[248:251], v[208:223]\n"
    "v_xor_b32 v253, 96, v252\n"
    "ds_read_b128 v[156:159], v253\n"
    "ds_read_b64_tr_b16 v[244:245], v13 offset:9216\n"
    "ds_read_b64_tr_b16 v[246:247], v13 offset:11520\n"
    "s_waitcnt lgkmcnt(3)\n"
    "v_mfma_f32_32x32x16_bf16 v[224:239], v[152:155], v[240:243], v[224:239]\n"
    "ds_read_b64_tr_b16 v[248:249], v14 offset:9216\n"
    "ds_read_b64_tr_b16 v[250:251], v14 offset:11520\n"
    "s_waitcnt lgkmcnt(2)\n"
    "v_mfma_f32_32x32x16_bf16 v[160:175], v[156:159], v[244:247], v[160:175]\n"
    "ds_read_b64_tr_b16 v[240:241], v15 offset:9216\n"
    "ds_read_b64_tr_b16 v[242:243], v15 offset:11520\n"
    "s_waitcnt lgkmcnt(2)\n"
    "v_mfma_f32_32x32x16_bf16 v[176:191], v[156:159], v[248:251], v[176:191]\n"
    "ds_read_b64_tr_b16 v[244:245], v0 offset:9216\n"
    "ds_read_b64_tr_b16 v[246:247], v0 offset:11520\n"
    "s_waitcnt lgkmcnt(2)\n"
    "v_mfma_f32_32x32x16_bf16 v[192:207], v[156:159], v[240:243], v[192:207]\n"
    "ds_read_b64_tr_b16 v[248:249], v3 offset:9216\n"
    "ds_read_b64_tr_b16 v[250:251], v3 offset:11520\n"
    "s_waitcnt lgkmcnt(2)\n"
    "v_mfma_f32_32x32x16_bf16 v[208:223], v[156:159], v[244:247], v[208:223]\n"
    "s_waitcnt lgkmcnt(0)\n"
    "v_mfma_f32_32x32x16_bf16 v[224:239], v[156:159], v[248:251], v[224:239]\n"
    "s_barrier\n"
    "s_add_u32 m0, s69, 18432\n"
    "s_add_u32 s78, s65, 442368\n"
    "s_bfm_b64 exec, 54, 0\n"
    "buffer_load_dwordx4 v8, s[12:15], s78 offen lds\n"
    "s_mov_b64 exec, -1\n"
    "s_add_u32 m0, s69, 19296\n"
    "s_add_u32 s78, s65, 442368\n"
    "s_bfm_b64 exec, 54, 0\n"
    "buffer_load_dwordx4 v9, s[12:15], s78 offen lds\n"
    "s_mov_b64 exec, -1\n"
    "s_add_u32 m0, s69, 20160\n"
    "s_add_u32 s78, s65, 442368\n"
    "s_bfm_b64 exec, 36, 0\n"
    "buffer_load_dwordx4 v10, s[12:15], s78 offen lds\n"
    "s_mov_b64 exec, -1\n"
    "s_add_u32 m0, s69, 36864\n"
    "s_add_u32 s78, s65, 663552\n"
    "s_bfm_b64 exec, 54, 0\n"
    "buffer_load_dwordx4 v8, s[12:15], s78 offen lds\n"
    "s_mov_b64 exec, -1\n"
    "s_add_u32 m0, s69, 37728\n"
    "s_add_u32 s78, s65, 663552\n"
    "s_bfm_b64 exec, 54, 0\n"
    "buffer_load_dwordx4 v9, s[12:15], s78 offen lds\n"
    "s_mov_b64 exec, -1\n"
    "s_add_u32 m0, s69, 38592\n"
    "s_add_u32 s78, s65, 663552\n"
    "s_bfm_b64 exec, 36, 0\n"
    "buffer_load_dwordx4 v10, s[12:15], s78 offen lds\n"
    "s_mov_b64 exec, -1\n"
    "v_and_b32 v13, 31, v1\n"
    "s_mul_i32 s79, s52, 72\n"
    "v_add_u32 v13, s79, v13\n"
    "v_lshrrev_b32 v15, 5, v1\n"
    "v_lshlrev_b32 v15, 3, v15\n"
    "v_add_u32 v15, 73728, v15\n"
    "s_lshl_b32 s80, s51, 2\n"
    "v_add_u32 v14, 168, v13\n"
    "v_and_b32 v0, 15, v14\n"
    "v_xor_b32 v0, s80, v0\n"
    "v_lshlrev_b32 v0, 4, v0\n"
    "v_lshl_add_u32 v14, v14, 8, v15\n"
    "v_add_u32 v14, v14, v0\n"
    "v_cvt_pk_bf16_f32 v208, v208, v209\n"
    "v_cvt_pk_bf16_f32 v209, v210, v211\n"
    "v_cvt_pk_bf16_f32 v210, v212, v213\n"
    "v_cvt_pk_bf16_f32 v211, v214, v215\n"
    "v_cvt_pk_bf16_f32 v212, v216, v217\n"
    "v_cvt_pk_bf16_f32 v213, v218, v219\n"
    "v_cvt_pk_bf16_f32 v214, v220, v221\n"
    "v_cvt_pk_bf16_f32 v215, v222, v223\n"
    "ds_write_b64 v14, v[208:209]\n"
    "v_xor_b32 v0, 16, v14\n"
    "ds_write_b64 v0, v[210:211]\n"
    "v_xor_b32 v0, 32, v14\n"
    "ds_write_b64 v0, v[212:213]\n"
    "v_xor_b32 v0, 48, v14\n"
    "ds_write_b64 v0, v[214:215]\n"
    "v_add_u32 v14, 200, v13\n"
    "v_and_b32 v0, 15, v14\n"
    "v_xor_b32 v0, s80, v0\n"
    "v_lshlrev_b32 v0, 4, v0\n"
    "v_lshl_add_u32 v14, v14, 8, v15\n"
    "v_add_u32 v14, v14, v0\n"
    "v_cvt_pk_bf16_f32 v224, v224, v225\n"
    "v_cvt_pk_bf16_f32 v225, v226, v227\n"
    "v_cvt_pk_bf16_f32 v226, v228, v229\n"
    "v_cvt_pk_bf16_f32 v227, v230, v231\n"
    "v_cvt_pk_bf16_f32 v228, v232, v233\n"
    "v_cvt_pk_bf16_f32 v229, v234, v235\n"
    "v_cvt_pk_bf16_f32 v230, v236, v237\n"
    "v_cvt_pk_bf16_f32 v231, v238, v239\n"
    "s_mov_b64 exec, s[76:77]\n"
    "ds_write_b64 v14, v[224:225]\n"
    "v_xor_b32 v0, 16, v14\n"
    "ds_write_b64 v0, v[226:227]\n"
    "v_xor_b32 v0, 32, v14\n"
    "ds_write_b64 v0, v[228:229]\n"
    "v_xor_b32 v0, 48, v14\n"
    "ds_write_b64 v0, v[230:231]\n"
    "s_mov_b64 exec, -1\n"
    "v_mul_f32 v4, s70, v160\n"
    "v_mul_f32 v6, s70, v161\n"
    "v_exp_f32_e64 v4, v4 clamp\n"
    "v_exp_f32_e64 v6, v6 clamp\n"
    "v_max_f32 v5, s59, v160\n"
    "v_max_f32 v7, s59, v161\n"
    "v_add_f32 v160, v4, v5\n"
    "v_add_f32 v161, v6, v7\n"
    "v_mul_f32 v4, s70, v162\n"
    "v_mul_f32 v6, s70, v163\n"
    "v_exp_f32_e64 v4, v4 clamp\n"
    "v_exp_f32_e64 v6, v6 clamp\n"
    "v_max_f32 v5, s59, v162\n"
    "v_max_f32 v7, s59, v163\n"
    "v_add_f32 v162, v4, v5\n"
    "v_add_f32 v163, v6, v7\n"
    "v_mul_f32 v4, s70, v164\n"
    "v_mul_f32 v6, s70, v165\n"
    "v_exp_f32_e64 v4, v4 clamp\n"
    "v_exp_f32_e64 v6, v6 clamp\n"
    "v_max_f32 v5, s59, v164\n"
    "v_max_f32 v7, s59, v165\n"
    "v_add_f32 v164, v4, v5\n"
    "v_add_f32 v165, v6, v7\n"
    "v_mul_f32 v4, s70, v166\n"
    "v_mul_f32 v6, s70, v167\n"
    "v_exp_f32_e64 v4, v4 clamp\n"
    "v_exp_f32_e64 v6, v6 clamp\n"
    "v_max_f32 v5, s59, v166\n"
    "v_max_f32 v7, s59, v167\n"
    "v_add_f32 v166, v4, v5\n"
    "v_add_f32 v167, v6, v7\n"
    "v_mul_f32 v4, s70, v168\n"
    "v_mul_f32 v6, s70, v169\n"
    "v_exp_f32_e64 v4, v4 clamp\n"
    "v_exp_f32_e64 v6, v6 clamp\n"
    "v_max_f32 v5, s59, v168\n"
    "v_max_f32 v7, s59, v169\n"
    "v_add_f32 v168, v4, v5\n"
    "v_add_f32 v169, v6, v7\n"
    "v_mul_f32 v4, s70, v170\n"
    "v_mul_f32 v6, s70, v171\n"
    "v_exp_f32_e64 v4, v4 clamp\n"
    "v_exp_f32_e64 v6, v6 clamp\n"
    "v_max_f32 v5, s59, v170\n"
    "v_max_f32 v7, s59, v171\n"
    "v_add_f32 v170, v4, v5\n"
    "v_add_f32 v171, v6, v7\n"
    "v_mul_f32 v4, s70, v172\n"
    "v_mul_f32 v6, s70, v173\n"
    "v_exp_f32_e64 v4, v4 clamp\n"
    "v_exp_f32_e64 v6, v6 clamp\n"
    "v_max_f32 v5, s59, v172\n"
    "v_max_f32 v7, s59, v173\n"
    "v_add_f32 v172, v4, v5\n"
    "v_add_f32 v173, v6, v7\n"
    "v_mul_f32 v4, s70, v174\n"
    "v_mul_f32 v6, s70, v175\n"
    "v_exp_f32_e64 v4, v4 clamp\n"
    "v_exp_f32_e64 v6, v6 clamp\n"
    "v_max_f32 v5, s59, v174\n"
    "v_max_f32 v7, s59, v175\n"
    "v_add_f32 v174, v4, v5\n"
    "v_add_f32 v175, v6, v7\n"
    "v_add_u32 v14, 0, v13\n"
    "v_and_b32 v0, 15, v14\n"
    "v_xor_b32 v0, s80, v0\n"
    "v_lshlrev_b32 v0, 4, v0\n"
    "v_lshl_add_u32 v14, v14, 8, v15\n"
    "v_add_u32 v14, v14, v0\n"
    "v_cvt_pk_bf16_f32 v160, v160, v161\n"
    "v_cvt_pk_bf16_f32 v161, v162, v163\n"
    "v_cvt_pk_bf16_f32 v162, v164, v165\n"
    "v_cvt_pk_bf16_f32 v163, v166, v167\n"
    "v_cvt_pk_bf16_f32 v164, v168, v169\n"
    "v_cvt_pk_bf16_f32 v165, v170, v171\n"
    "v_cvt_pk_bf16_f32 v166, v172, v173\n"
    "v_cvt_pk_bf16_f32 v167, v174, v175\n"
    "ds_write_b64 v14, v[160:161]\n"
    "v_xor_b32 v0, 16, v14\n"
    "ds_write_b64 v0, v[162:163]\n"
    "v_xor_b32 v0, 32, v14\n"
    "ds_write_b64 v0, v[164:165]\n"
    "v_xor_b32 v0, 48, v14\n"
    "ds_write_b64 v0, v[166:167]\n"
    "v_mul_f32 v4, s70, v176\n"
    "v_mul_f32 v6, s70, v177\n"
    "v_exp_f32_e64 v4, v4 clamp\n"
    "v_exp_f32_e64 v6, v6 clamp\n"
    "v_max_f32 v5, s59, v176\n"
    "v_max_f32 v7, s59, v177\n"
    "v_add_f32 v176, v4, v5\n"
    "v_add_f32 v177, v6, v7\n"
    "v_mul_f32 v4, s70, v178\n"
    "v_mul_f32 v6, s70, v179\n"
    "v_exp_f32_e64 v4, v4 clamp\n"
    "v_exp_f32_e64 v6, v6 clamp\n"
    "v_max_f32 v5, s59, v178\n"
    "v_max_f32 v7, s59, v179\n"
    "v_add_f32 v178, v4, v5\n"
    "v_add_f32 v179, v6, v7\n"
    "v_mul_f32 v4, s70, v180\n"
    "v_mul_f32 v6, s70, v181\n"
    "v_exp_f32_e64 v4, v4 clamp\n"
    "v_exp_f32_e64 v6, v6 clamp\n"
    "v_max_f32 v5, s59, v180\n"
    "v_max_f32 v7, s59, v181\n"
    "v_add_f32 v180, v4, v5\n"
    "v_add_f32 v181, v6, v7\n"
    "v_mul_f32 v4, s70, v182\n"
    "v_mul_f32 v6, s70, v183\n"
    "v_exp_f32_e64 v4, v4 clamp\n"
    "v_exp_f32_e64 v6, v6 clamp\n"
    "v_max_f32 v5, s59, v182\n"
    "v_max_f32 v7, s59, v183\n"
    "v_add_f32 v182, v4, v5\n"
    "v_add_f32 v183, v6, v7\n"
    "v_mul_f32 v4, s70, v184\n"
    "v_mul_f32 v6, s70, v185\n"
    "v_exp_f32_e64 v4, v4 clamp\n"
    "v_exp_f32_e64 v6, v6 clamp\n"
    "v_max_f32 v5, s59, v184\n"
    "v_max_f32 v7, s59, v185\n"
    "v_add_f32 v184, v4, v5\n"
    "v_add_f32 v185, v6, v7\n"
    "v_mul_f32 v4, s70, v186\n"
    "v_mul_f32 v6, s70, v187\n"
    "v_exp_f32_e64 v4, v4 clamp\n"
    "v_exp_f32_e64 v6, v6 clamp\n"
    "v_max_f32 v5, s59, v186\n"
    "v_max_f32 v7, s59, v187\n"
    "v_add_f32 v186, v4, v5\n"
    "v_add_f32 v187, v6, v7\n"
    "v_mul_f32 v4, s70, v188\n"
    "v_mul_f32 v6, s70, v189\n"
    "v_exp_f32_e64 v4, v4 clamp\n"
    "v_exp_f32_e64 v6, v6 clamp\n"
    "v_max_f32 v5, s59, v188\n"
    "v_max_f32 v7, s59, v189\n"
    "v_add_f32 v188, v4, v5\n"
    "v_add_f32 v189, v6, v7\n"
    "v_mul_f32 v4, s70, v190\n"
    "v_mul_f32 v6, s70, v191\n"
    "v_exp_f32_e64 v4, v4 clamp\n"
    "v_exp_f32_e64 v6, v6 clamp\n"
    "v_max_f32 v5, s59, v190\n"
    "v_max_f32 v7, s59, v191\n"
    "v_add_f32 v190, v4, v5\n"
    "v_add_f32 v191, v6, v7\n"
    "v_add_u32 v14, 32, v13\n"
    "v_and_b32 v0, 15, v14\n"
    "v_xor_b32 v0, s80, v0\n"
    "v_lshlrev_b32 v0, 4, v0\n"
    "v_lshl_add_u32 v14, v14, 8, v15\n"
    "v_add_u32 v14, v14, v0\n"
    "v_cvt_pk_bf16_f32 v176, v176, v177\n"
    "v_cvt_pk_bf16_f32 v177, v178, v179\n"
    "v_cvt_pk_bf16_f32 v178, v180, v181\n"
    "v_cvt_pk_bf16_f32 v179, v182, v183\n"
    "v_cvt_pk_bf16_f32 v180, v184, v185\n"
    "v_cvt_pk_bf16_f32 v181, v186, v187\n"
    "v_cvt_pk_bf16_f32 v182, v188, v189\n"
    "v_cvt_pk_bf16_f32 v183, v190, v191\n"
    "ds_write_b64 v14, v[176:177]\n"
    "v_xor_b32 v0, 16, v14\n"
    "ds_write_b64 v0, v[178:179]\n"
    "v_xor_b32 v0, 32, v14\n"
    "ds_write_b64 v0, v[180:181]\n"
    "v_xor_b32 v0, 48, v14\n"
    "ds_write_b64 v0, v[182:183]\n"
    "v_mul_f32 v4, s70, v192\n"
    "v_mul_f32 v6, s70, v193\n"
    "v_exp_f32_e64 v4, v4 clamp\n"
    "v_exp_f32_e64 v6, v6 clamp\n"
    "v_max_f32 v5, s59, v192\n"
    "v_max_f32 v7, s59, v193\n"
    "v_add_f32 v4, v4, v5\n"
    "v_add_f32 v6, v6, v7\n"
    "v_cndmask_b32 v192, v192, v4, s[74:75]\n"
    "v_cndmask_b32 v193, v193, v6, s[74:75]\n"
    "v_mul_f32 v4, s70, v194\n"
    "v_mul_f32 v6, s70, v195\n"
    "v_exp_f32_e64 v4, v4 clamp\n"
    "v_exp_f32_e64 v6, v6 clamp\n"
    "v_max_f32 v5, s59, v194\n"
    "v_max_f32 v7, s59, v195\n"
    "v_add_f32 v4, v4, v5\n"
    "v_add_f32 v6, v6, v7\n"
    "v_cndmask_b32 v194, v194, v4, s[74:75]\n"
    "v_cndmask_b32 v195, v195, v6, s[74:75]\n"
    "v_mul_f32 v4, s70, v196\n"
    "v_mul_f32 v6, s70, v197\n"
    "v_exp_f32_e64 v4, v4 clamp\n"
    "v_exp_f32_e64 v6, v6 clamp\n"
    "v_max_f32 v5, s59, v196\n"
    "v_max_f32 v7, s59, v197\n"
    "v_add_f32 v4, v4, v5\n"
    "v_add_f32 v6, v6, v7\n"
    "v_cndmask_b32 v196, v196, v4, s[74:75]\n"
    "v_cndmask_b32 v197, v197, v6, s[74:75]\n"
    "v_mul_f32 v4, s70, v198\n"
    "v_mul_f32 v6, s70, v199\n"
    "v_exp_f32_e64 v4, v4 clamp\n"
    "v_exp_f32_e64 v6, v6 clamp\n"
    "v_max_f32 v5, s59, v198\n"
    "v_max_f32 v7, s59, v199\n"
    "v_add_f32 v4, v4, v5\n"
    "v_add_f32 v6, v6, v7\n"
    "v_cndmask_b32 v198, v198, v4, s[74:75]\n"
    "v_cndmask_b32 v199, v199, v6, s[74:75]\n"
    "v_mul_f32 v4, s70, v200\n"
    "v_mul_f32 v6, s70, v201\n"
    "v_exp_f32_e64 v4, v4 clamp\n"
    "v_exp_f32_e64 v6, v6 clamp\n"
    "v_max_f32 v5, s59, v200\n"
    "v_max_f32 v7, s59, v201\n"
    "v_add_f32 v4, v4, v5\n"
    "v_add_f32 v6, v6, v7\n"
    "v_cndmask_b32 v200, v200, v4, s[74:75]\n"
    "v_cndmask_b32 v201, v201, v6, s[74:75]\n"
    "v_mul_f32 v4, s70, v202\n"
    "v_mul_f32 v6, s70, v203\n"
    "v_exp_f32_e64 v4, v4 clamp\n"
    "v_exp_f32_e64 v6, v6 clamp\n"
    "v_max_f32 v5, s59, v202\n"
    "v_max_f32 v7, s59, v203\n"
    "v_add_f32 v4, v4, v5\n"
    "v_add_f32 v6, v6, v7\n"
    "v_cndmask_b32 v202, v202, v4, s[74:75]\n"
    "v_cndmask_b32 v203, v203, v6, s[74:75]\n"
    "v_mul_f32 v4, s70, v204\n"
    "v_mul_f32 v6, s70, v205\n"
    "v_exp_f32_e64 v4, v4 clamp\n"
    "v_exp_f32_e64 v6, v6 clamp\n"
    "v_max_f32 v5, s59, v204\n"
    "v_max_f32 v7, s59, v205\n"
    "v_add_f32 v4, v4, v5\n"
    "v_add_f32 v6, v6, v7\n"
    "v_cndmask_b32 v204, v204, v4, s[74:75]\n"
    "v_cndmask_b32 v205, v205, v6, s[74:75]\n"
    "v_mul_f32 v4, s70, v206\n"
    "v_mul_f32 v6, s70, v207\n"
    "v_exp_f32_e64 v4, v4 clamp\n"
    "v_exp_f32_e64 v6, v6 clamp\n"
    "v_max_f32 v5, s59, v206\n"
    "v_max_f32 v7, s59, v207\n"
    "v_add_f32 v4, v4, v5\n"
    "v_add_f32 v6, v6, v7\n"
    "v_cndmask_b32 v206, v206, v4, s[74:75]\n"
    "v_cndmask_b32 v207, v207, v6, s[74:75]\n"
    "v_add_u32 v14, 64, v13\n"
    "v_add_u32 v0, 136, v13\n"
    "v_cndmask_b32 v14, v0, v14, s[74:75]\n"
    "v_and_b32 v0, 15, v14\n"
    "v_xor_b32 v0, s80, v0\n"
    "v_lshlrev_b32 v0, 4, v0\n"
    "v_lshl_add_u32 v14, v14, 8, v15\n"
    "v_add_u32 v14, v14, v0\n"
    "v_cvt_pk_bf16_f32 v192, v192, v193\n"
    "v_cvt_pk_bf16_f32 v193, v194, v195\n"
    "v_cvt_pk_bf16_f32 v194, v196, v197\n"
    "v_cvt_pk_bf16_f32 v195, v198, v199\n"
    "v_cvt_pk_bf16_f32 v196, v200, v201\n"
    "v_cvt_pk_bf16_f32 v197, v202, v203\n"
    "v_cvt_pk_bf16_f32 v198, v204, v205\n"
    "v_cvt_pk_bf16_f32 v199, v206, v207\n"
    "ds_write_b64 v14, v[192:193]\n"
    "v_xor_b32 v0, 16, v14\n"
    "ds_write_b64 v0, v[194:195]\n"
    "v_xor_b32 v0, 32, v14\n"
    "ds_write_b64 v0, v[196:197]\n"
    "v_xor_b32 v0, 48, v14\n"
    "ds_write_b64 v0, v[198:199]\n"
    "v_and_b32 v4, 31, v1\n"
    "v_lshrrev_b32 v7, 5, v1\n"
    "s_and_b32 s79, s50, 1\n"
    "s_lshl_b32 s79, s79, 1\n"
    "s_mul_i32 s80, s50, 18\n"
    "v_add_u32 v5, s79, v4\n"
    "v_cmp_lt_u32 vcc, 17, v5\n"
    "v_subrev_u32 v0, 18, v5\n"
    "s_nop 0\n"
    "v_cndmask_b32 v5, v5, v0, vcc\n"
    "v_add_u32 v6, s80, v5\n"
    "v_and_b32 v0, 15, v6\n"
    "v_xor_b32 v0, v0, v7\n"
    "v_lshlrev_b32 v0, 4, v0\n"
    "v_lshl_add_u32 v6, v6, 8, v0\n"
    "v_add_u32 v6, 73728, v6\n"
    "v_cmp_gt_u32 vcc, 18, v4\n"
    "v_mov_b32 v0, 147456\n"
    "s_nop 0\n"
    "v_cndmask_b32 v13, v0, v6, vcc\n"
    "s_add_u32 s80, s80, 144\n"
    "v_add_u32 v6, s80, v4\n"
    "v_and_b32 v0, 15, v6\n"
    "v_xor_b32 v0, v0, v7\n"
    "v_lshlrev_b32 v0, 4, v0\n"
    "v_lshl_add_u32 v6, v6, 8, v0\n"
    "v_add_u32 v6, 73728, v6\n"
    "v_cmp_gt_u32 vcc, 18, v4\n"
    "v_mov_b32 v0, 147712\n"
    "s_nop 0\n"
    "v_cndmask_b32 v14, v0, v6, vcc\n"
    "s_waitcnt lgkmcnt(0)\n"
    "s_barrier\n"
    "ds_read_b128 v[176:179], v13\n"
    "ds_read_b128 v[208:211], v14\n"
    "v_xor_b32 v4, 32, v13\n"
    "v_xor_b32 v5, 32, v14\n"
    "ds_read_b128 v[180:183], v4\n"
    "ds_read_b128 v[212:215], v5\n"
    "v_xor_b32 v4, 64, v13\n"
    "v_xor_b32 v5, 64, v14\n"
    "ds_read_b128 v[184:187], v4\n"
    "ds_read_b128 v[216:219], v5\n"
    "v_xor_b32 v4, 96, v13\n"
    "v_xor_b32 v5, 96, v14\n"
    "ds_read_b128 v[188:191], v4\n"
    "ds_read_b128 v[220:223], v5\n"
    "v_xor_b32 v4, 128, v13\n"
    "v_xor_b32 v5, 128, v14\n"
    "ds_read_b128 v[192:195], v4\n"
    "ds_read_b128 v[224:227], v5\n"
    "v_xor_b32 v4, 160, v13\n"
    "v_xor_b32 v5, 160, v14\n"
    "ds_read_b128 v[196:199], v4\n"
    "ds_read_b128 v[228:231], v5\n"
    "v_xor_b32 v4, 192, v13\n"
    "v_xor_b32 v5, 192, v14\n"
    "ds_read_b128 v[200:203], v4\n"
    "ds_read_b128 v[232:235], v5\n"
    "v_xor_b32 v4, 224, v13\n"
    "v_xor_b32 v5, 224, v14\n"
    "ds_read_b128 v[204:207], v4\n"
    "ds_read_b128 v[236:239], v5\n"
    "v_bfe_u32 v4, v1, 5, 1\n"
    "v_bfe_u32 v5, v1, 2, 2\n"
    "v_lshl_or_b32 v3, v4, 2, v5\n"
    "v_lshlrev_b32 v0, 3, v3\n"
    "v_add_u32 v0, 147968, v0\n"
    "v_mul_u32_u24 v3, 576, v3\n"
    "s_mul_i32 s79, s52, 144\n"
    "s_add_u32 s79, s79, 55296\n"
    "v_add_u32 v3, s79, v3\n"
    "v_bfe_u32 v4, v1, 4, 1\n"
    "v_and_b32 v5, 3, v1\n"
    "v_lshl_or_b32 v4, v4, 2, v5\n"
    "v_add_u32 v5, 0, v4\n"
    "v_mul_u32_u24 v6, 52, v5\n"
    "v_lshrrev_b32 v6, 8, v6\n"
    "v_lshlrev_b32 v5, 3, v5\n"
    "v_add_u32 v7, 1, v6\n"
    "v_lshrrev_b32 v7, 1, v7\n"
    "v_lshlrev_b32 v7, 3, v7\n"
    "v_sub_u32 v5, v5, v7\n"
    "v_add_u32 v13, v3, v5\n"
    "v_add_u32 v5, 8, v4\n"
    "v_mul_u32_u24 v6, 52, v5\n"
    "v_lshrrev_b32 v6, 8, v6\n"
    "v_lshlrev_b32 v5, 3, v5\n"
    "v_add_u32 v7, 1, v6\n"
    "v_lshrrev_b32 v7, 1, v7\n"
    "v_lshlrev_b32 v7, 3, v7\n"
    "v_sub_u32 v5, v5, v7\n"
    "v_add_u32 v14, v3, v5\n"
    "v_add_u32 v5, 16, v4\n"
    "v_mul_u32_u24 v6, 52, v5\n"
    "v_lshrrev_b32 v6, 8, v6\n"
    "v_lshlrev_b32 v5, 3, v5\n"
    "v_add_u32 v7, 1, v6\n"
    "v_lshrrev_b32 v7, 1, v7\n"
    "v_lshlrev_b32 v7, 3, v7\n"
    "v_sub_u32 v5, v5, v7\n"
    "v_add_u32 v15, v3, v5\n"
    "s_mov_b32 s98, 0xffff0000\n"
    "s_mov_b32 s99, 0xffff0000\n"
    "v_cndmask_b32 v15, v15, v0, s[98:99]\n"
    "s_waitcnt lgkmcnt(14)\n"
    "v_mfma_f32_32x32x16_bf16 v[160:175], v[176:179], v[208:211], 0\n"
    "s_waitcnt lgkmcnt(12)\n"
    "v_mfma_f32_32x32x16_bf16 v[160:175], v[180:183], v[212:215], v[160:175]\n"
    "s_waitcnt lgkmcnt(10)\n"
    "v_mfma_f32_32x32x16_bf16 v[160:175], v[184:187], v[216:219], v[160:175]\n"
    "s_waitcnt lgkmcnt(8)\n"
    "v_mfma_f32_32x32x16_bf16 v[160:175], v[188:191], v[220:223], v[160:175]\n"
    "s_waitcnt lgkmcnt(6)\n"
    "v_mfma_f32_32x32x16_bf16 v[160:175], v[192:195], v[224:227], v[160:175]\n"
    "s_waitcnt lgkmcnt(4)\n"
    "v_mfma_f32_32x32x16_bf16 v[160:175], v[196:199], v[228:231], v[160:175]\n"
    "s_waitcnt lgkmcnt(2)\n"
    "v_mfma_f32_32x32x16_bf16 v[160:175], v[200:203], v[232:235], v[160:175]\n"
    "s_waitcnt lgkmcnt(0)\n"
    "v_mfma_f32_32x32x16_bf16 v[160:175], v[204:207], v[236:239], v[160:175]\n"
    "s_nop 11\n"
    "v_cvt_pk_bf16_f32 v176, v160, v161\n"
    "v_cvt_pk_bf16_f32 v177, v162, v163\n"
    "v_cvt_pk_bf16_f32 v178, v164, v165\n"
    "v_cvt_pk_bf16_f32 v179, v166, v167\n"
    "v_cvt_pk_bf16_f32 v180, v168, v169\n"
    "s_lshl_b32 s79, s54, 10\n"
    "s_add_u32 s79, s79, s68\n"
    "v_lshrrev_b32 v3, 2, v2\n"
    "s_mov_b32 exec_lo, 0xfffff\n"
    "s_mov_b32 exec_hi, 0xfffff\n"
    "buffer_store_dwordx4 v[176:179], v2, s[20:23], s79 offen sc1\n"
    "s_mov_b32 exec_hi, 0\n"
    "buffer_store_dword v180, v3, s[20:23], s79 offen offset:320 sc1\n"
    "s_mov_b64 exec, -1\n"
    "s_mov_b32 s96, 0x1ffffff\n"
    "s_mov_b32 s97, 0xfffff\n"
    "s_waitcnt vmcnt(11)\n"
    "s_barrier\n"
    "ds_read_b64_tr_b16 v[240:241], v13 offset:0\n"
    "ds_read_b64_tr_b16 v[242:243], v13 offset:288\n"
    "ds_read_b64_tr_b16 v[244:245], v14 offset:0\n"
    "ds_read_b64_tr_b16 v[246:247], v14 offset:288\n"
    "s_waitcnt lgkmcnt(2)\n"
    "v_mfma_f32_32x32x16_bf16 v[160:175], v[240:243], v[16:19], 0\n"
    "ds_read_b64_tr_b16 v[248:249], v15 offset:0\n"
    "ds_read_b64_tr_b16 v[250:251], v15 offset:288\n"
    "s_waitcnt lgkmcnt(2)\n"
    "v_mfma_f32_32x32x16_bf16 v[176:191], v[244:247], v[16:19], 0\n"
    "ds_read_b64_tr_b16 v[240:241], v13 offset:4608\n"
    "ds_read_b64_tr_b16 v[242:243], v13 offset:4896\n"
    "s_waitcnt lgkmcnt(2)\n"
    "v_mfma_f32_32x32x16_bf16 v[192:207], v[248:251], v[16:19], 0\n"
    "ds_read_b64_tr_b16 v[244:245], v14 offset:4608\n"
    "ds_read_b64_tr_b16 v[246:247], v14 offset:4896\n"
    "s_waitcnt lgkmcnt(2)\n"
    "v_mfma_f32_32x32x16_bf16 v[160:175], v[240:243], v[20:23], v[160:175]\n"
    "ds_read_b64_tr_b16 v[248:249], v15 offset:4608\n"
    "ds_read_b64_tr_b16 v[250:251], v15 offset:4896\n"
    "s_waitcnt lgkmcnt(2)\n"
    "v_mfma_f32_32x32x16_bf16 v[176:191], v[244:247], v[20:23], v[176:191]\n"
    "ds_read_b64_tr_b16 v[240:241], v13 offset:9216\n"
    "ds_read_b64_tr_b16 v[242:243], v13 offset:9504\n"
    "s_waitcnt lgkmcnt(2)\n"
    "v_mfma_f32_32x32x16_bf16 v[192:207], v[248:251], v[20:23], v[192:207]\n"
    "ds_read_b64_tr_b16 v[244:245], v14 offset:9216\n"
    "ds_read_b64_tr_b16 v[246:247], v14 offset:9504\n"
    "s_waitcnt lgkmcnt(2)\n"
    "v_mfma_f32_32x32x16_bf16 v[160:175], v[240:243], v[24:27], v[160:175]\n"
    "ds_read_b64_tr_b16 v[248:249], v15 offset:9216\n"
    "ds_read_b64_tr_b16 v[250:251], v15 offset:9504\n"
    "s_waitcnt lgkmcnt(2)\n"
    "v_mfma_f32_32x32x16_bf16 v[176:191], v[244:247], v[24:27], v[176:191]\n"
    "ds_read_b64_tr_b16 v[240:241], v13 offset:13824\n"
    "ds_read_b64_tr_b16 v[242:243], v13 offset:14112\n"
    "s_waitcnt lgkmcnt(2)\n"
    "v_mfma_f32_32x32x16_bf16 v[192:207], v[248:251], v[24:27], v[192:207]\n"
    "ds_read_b64_tr_b16 v[244:245], v14 offset:13824\n"
    "ds_read_b64_tr_b16 v[246:247], v14 offset:14112\n"
    "s_waitcnt lgkmcnt(2)\n"
    "v_mfma_f32_32x32x16_bf16 v[160:175], v[240:243], v[28:31], v[160:175]\n"
    "ds_read_b64_tr_b16 v[248:249], v15 offset:13824\n"
    "ds_read_b64_tr_b16 v[250:251], v15 offset:14112\n"
    "s_waitcnt lgkmcnt(2)\n"
    "v_mfma_f32_32x32x16_bf16 v[176:191], v[244:247], v[28:31], v[176:191]\n"
    "s_waitcnt vmcnt(8)\n"
    "s_barrier\n"
    "v_mov_b32 v228, s63\n"
    "v_mov_b32 v229, s64\n"
    "v_lshrrev_b32 v3, 1, v2\n"
    "v_and_b32 v3, 0x78, v3\n"
    "s_mov_b32 s88, 0xffff\n"
    "s_mov_b32 s89, 0\n"
    "v_add_u32 v13, 4294912000, v13\n"
    "ds_read_b64_tr_b16 v[240:241], v13 offset:0\n"
    "ds_read_b64_tr_b16 v[242:243], v13 offset:288\n"
    "s_waitcnt lgkmcnt(2)\n"
    "v_mfma_f32_32x32x16_bf16 v[192:207], v[248:251], v[28:31], v[192:207]\n"
    "v_add_u32 v14, 4294912000, v14\n"
    "ds_read_b64_tr_b16 v[244:245], v14 offset:0\n"
    "ds_read_b64_tr_b16 v[246:247], v14 offset:288\n"
    "s_waitcnt lgkmcnt(2)\n"
    "v_mfma_f32_32x32x16_bf16 v[160:175], v[240:243], v[32:35], v[160:175]\n"
    "v_mov_b32 v4, 64\n"
    "v_mov_b32 v5, 4294912000\n"
    "v_cndmask_b32 v4, v5, v4, s[98:99]\n"
    "v_add_u32 v15, v4, v15\n"
    "ds_read_b64_tr_b16 v[248:249], v15 offset:0\n"
    "ds_read_b64_tr_b16 v[250:251], v15 offset:288\n"
    "s_waitcnt lgkmcnt(2)\n"
    "v_mfma_f32_32x32x16_bf16 v[176:191], v[244:247], v[32:35], v[176:191]\n"
    "ds_read_b64_tr_b16 v[240:241], v13 offset:4608\n"
    "ds_read_b64_tr_b16 v[242:243], v13 offset:4896\n"
    "s_waitcnt lgkmcnt(2)\n"
    "v_mfma_f32_32x32x16_bf16 v[192:207], v[248:251], v[32:35], v[192:207]\n"
    "ds_read_b64_tr_b16 v[244:245], v14 offset:4608\n"
    "ds_read_b64_tr_b16 v[246:247], v14 offset:4896\n"
    "s_waitcnt lgkmcnt(2)\n"
    "v_mfma_f32_32x32x16_bf16 v[160:175], v[240:243], v[36:39], v[160:175]\n"
    "ds_read_b64_tr_b16 v[248:249], v15 offset:4608\n"
    "ds_read_b64_tr_b16 v[250:251], v15 offset:4896\n"
    "s_waitcnt lgkmcnt(2)\n"
    "v_mfma_f32_32x32x16_bf16 v[176:191], v[244:247], v[36:39], v[176:191]\n"
    "ds_read_b64_tr_b16 v[240:241], v13 offset:9216\n"
    "ds_read_b64_tr_b16 v[242:243], v13 offset:9504\n"
    "s_waitcnt lgkmcnt(2)\n"
    "v_mfma_f32_32x32x16_bf16 v[192:207], v[248:251], v[36:39], v[192:207]\n"
    "ds_read_b64_tr_b16 v[244:245], v14 offset:9216\n"
    "ds_read_b64_tr_b16 v[246:247], v14 offset:9504\n"
    "s_waitcnt lgkmcnt(2)\n"
    "v_mfma_f32_32x32x16_bf16 v[160:175], v[240:243], v[40:43], v[160:175]\n"
    "ds_read_b64_tr_b16 v[248:249], v15 offset:9216\n"
    "ds_read_b64_tr_b16 v[250:251], v15 offset:9504\n"
    "s_waitcnt lgkmcnt(2)\n"
    "v_mfma_f32_32x32x16_bf16 v[176:191], v[244:247], v[40:43], v[176:191]\n"
    "ds_read_b64_tr_b16 v[240:241], v13 offset:13824\n"
    "ds_read_b64_tr_b16 v[242:243], v13 offset:14112\n"
    "s_waitcnt lgkmcnt(2)\n"
    "v_mfma_f32_32x32x16_bf16 v[192:207], v[248:251], v[40:43], v[192:207]\n"
    "ds_read_b64_tr_b16 v[244:245], v14 offset:13824\n"
    "ds_read_b64_tr_b16 v[246:247], v14 offset:14112\n"
    "s_waitcnt lgkmcnt(2)\n"
    "v_mfma_f32_32x32x16_bf16 v[160:175], v[240:243], v[44:47], v[160:175]\n"
    "ds_read_b64_tr_b16 v[248:249], v15 offset:13824\n"
    "ds_read_b64_tr_b16 v[250:251], v15 offset:14112\n"
    "s_waitcnt lgkmcnt(2)\n"
    "v_mfma_f32_32x32x16_bf16 v[176:191], v[244:247], v[44:47], v[176:191]\n"
    "s_waitcnt vmcnt(0)\n"
    "s_barrier\n"
    "s_lshl_b32 s79, s54, 3\n"
    "s_add_u32 s79, s79, s60\n"
    "s_add_u32 s78, s79, 0\n"
    "s_mov_b64 exec, 1\n"
    "buffer_store_dwordx2 v[228:229], off, s[20:23], s78 sc1\n"
    "s_mov_b64 exec, -1\n"
    "v_add_u32 v13, 18432, v13\n"
    "ds_read_b64_tr_b16 v[240:241], v13 offset:0\n"
    "ds_read_b64_tr_b16 v[242:243], v13 offset:288\n"
    "s_add_u32 m0, s69, 55296\n"
    "s_add_u32 s78, s65, 884736\n"
    "s_bfm_b64 exec, 54, 0\n"
    "buffer_load_dwordx4 v8, s[12:15], s78 offen lds\n"
    "s_mov_b64 exec, -1\n"
    "s_waitcnt lgkmcnt(2)\n"
    "v_mfma_f32_32x32x16_bf16 v[192:207], v[248:251], v[44:47], v[192:207]\n"
    "v_add_u32 v14, 18432, v14\n"
    "ds_read_b64_tr_b16 v[244:245], v14 offset:0\n"
    "ds_read_b64_tr_b16 v[246:247], v14 offset:288\n"
    "s_waitcnt lgkmcnt(2)\n"
    "v_mfma_f32_32x32x16_bf16 v[160:175], v[240:243], v[48:51], v[160:175]\n"
    "v_mov_b32 v4, 64\n"
    "v_mov_b32 v5, 18432\n"
    "v_cndmask_b32 v4, v5, v4, s[98:99]\n"
    "v_add_u32 v15, v4, v15\n"
    "ds_read_b64_tr_b16 v[248:249], v15 offset:0\n"
    "ds_read_b64_tr_b16 v[250:251], v15 offset:288\n"
    "s_waitcnt lgkmcnt(2)\n"
    "v_mfma_f32_32x32x16_bf16 v[176:191], v[244:247], v[48:51], v[176:191]\n"
    "ds_read_b64_tr_b16 v[240:241], v13 offset:4608\n"
    "ds_read_b64_tr_b16 v[242:243], v13 offset:4896\n"
    "s_waitcnt lgkmcnt(2)\n"
    "v_mfma_f32_32x32x16_bf16 v[192:207], v[248:251], v[48:51], v[192:207]\n"
    "ds_read_b64_tr_b16 v[244:245], v14 offset:4608\n"
    "ds_read_b64_tr_b16 v[246:247], v14 offset:4896\n"
    "s_add_u32 m0, s69, 56160\n"
    "s_add_u32 s78, s65, 884736\n"
    "s_bfm_b64 exec, 54, 0\n"
    "buffer_load_dwordx4 v9, s[12:15], s78 offen lds\n"
    "s_mov_b64 exec, -1\n"
    "s_waitcnt lgkmcnt(2)\n"
    "v_mfma_f32_32x32x16_bf16 v[160:175], v[240:243], v[52:55], v[160:175]\n"
    "ds_read_b64_tr_b16 v[248:249], v15 offset:4608\n"
    "ds_read_b64_tr_b16 v[250:251], v15 offset:4896\n"
    "s_waitcnt lgkmcnt(2)\n"
    "v_mfma_f32_32x32x16_bf16 v[176:191], v[244:247], v[52:55], v[176:191]\n"
    "ds_read_b64_tr_b16 v[240:241], v13 offset:9216\n"
    "ds_read_b64_tr_b16 v[242:243], v13 offset:9504\n"
    "s_waitcnt lgkmcnt(2)\n"
    "v_mfma_f32_32x32x16_bf16 v[192:207], v[248:251], v[52:55], v[192:207]\n"
    "ds_read_b64_tr_b16 v[244:245], v14 offset:9216\n"
    "ds_read_b64_tr_b16 v[246:247], v14 offset:9504\n"
    "s_waitcnt lgkmcnt(2)\n"
    "v_mfma_f32_32x32x16_bf16 v[160:175], v[240:243], v[56:59], v[160:175]\n"
    "ds_read_b64_tr_b16 v[248:249], v15 offset:9216\n"
    "ds_read_b64_tr_b16 v[250:251], v15 offset:9504\n"
    "s_add_u32 m0, s69, 57024\n"
    "s_add_u32 s78, s65, 884736\n"
    "s_bfm_b64 exec, 36, 0\n"
    "buffer_load_dwordx4 v10, s[12:15], s78 offen lds\n"
    "s_mov_b64 exec, -1\n"
    "s_waitcnt lgkmcnt(2)\n"
    "v_mfma_f32_32x32x16_bf16 v[176:191], v[244:247], v[56:59], v[176:191]\n"
    "ds_read_b64_tr_b16 v[240:241], v13 offset:13824\n"
    "ds_read_b64_tr_b16 v[242:243], v13 offset:14112\n"
    "s_waitcnt lgkmcnt(2)\n"
    "v_mfma_f32_32x32x16_bf16 v[192:207], v[248:251], v[56:59], v[192:207]\n"
    "ds_read_b64_tr_b16 v[244:245], v14 offset:13824\n"
    "ds_read_b64_tr_b16 v[246:247], v14 offset:14112\n"
    "s_waitcnt lgkmcnt(2)\n"
    "v_mfma_f32_32x32x16_bf16 v[160:175], v[240:243], v[60:63], v[160:175]\n"
    "ds_read_b64_tr_b16 v[248:249], v15 offset:13824\n"
    "ds_read_b64_tr_b16 v[250:251], v15 offset:14112\n"
    "s_waitcnt lgkmcnt(2)\n"
    "v_mfma_f32_32x32x16_bf16 v[176:191], v[244:247], v[60:63], v[176:191]\n"
    "s_barrier\n"
    "v_add_u32 v13, 18432, v13\n"
    "ds_read_b64_tr_b16 v[240:241], v13 offset:0\n"
    "ds_read_b64_tr_b16 v[242:243], v13 offset:288\n"
    "v_and_b32 v4, 31, v1\n"
    "v_lshrrev_b32 v5, 5, v1\n"
    "v_lshlrev_b32 v5, 2, v5\n"
    "v_sub_u32 v4, v4, v5\n"
    "v_and_b32 v5, 1, v1\n"
    "v_lshlrev_b32 v5, 4, v5\n"
    "v_mov_b32 v0, 0x3f80\n"
    "v_lshlrev_b32 v5, v5, v0\n"
    "v_and_b32 v4, -2, v4\n"
    "v_cmp_eq_u32 vcc, 0, v4\n"
    "s_nop 1\n"
    "v_cndmask_b32 v232, 0, v5, vcc\n"
    "v_cmp_eq_u32 vcc, 2, v4\n"
    "s_nop 1\n"
    "v_cndmask_b32 v233, 0, v5, vcc\n"
    "v_cmp_eq_u32 vcc, 8, v4\n"
    "s_nop 1\n"
    "v_cndmask_b32 v234, 0, v5, vcc\n"
    "v_cmp_eq_u32 vcc, 10, v4\n"
    "s_nop 1\n"
    "v_cndmask_b32 v235, 0, v5, vcc\n"
    "s_waitcnt lgkmcnt(2)\n"
    "v_mfma_f32_32x32x16_bf16 v[192:207], v[248:251], v[60:63], v[192:207]\n"
    "v_add_u32 v14, 18432, v14\n"
    "ds_read_b64_tr_b16 v[244:245], v14 offset:0\n"
    "ds_read_b64_tr_b16 v[246:247], v14 offset:288\n"
    "s_waitcnt lgkmcnt(2)\n"
    "v_mfma_f32_32x32x16_bf16 v[160:175], v[240:243], v[64:67], v[160:175]\n"
    "v_mov_b32 v4, 64\n"
    "v_mov_b32 v5, 18432\n"
    "v_cndmask_b32 v4, v5, v4, s[98:99]\n"
    "v_add_u32 v15, v4, v15\n"
    "ds_read_b64_tr_b16 v[248:249], v15 offset:0\n"
    "ds_read_b64_tr_b16 v[250:251], v15 offset:288\n"
    "s_lshl_b32 s79, s50, 11\n"
    "s_add_u32 s79, s79, 73728\n"
    "v_add_u32 v0, s79, v2\n"
    "v_mov_b32 v216, 0\n"
    "v_mov_b32 v217, 0\n"
    "v_mov_b32 v218, 0\n"
    "v_mov_b32 v219, 0\n"
    "ds_write_b128 v0, v[216:219]\n"
    "ds_write_b128 v0, v[216:219] offset:1024\n"
    "s_and_b32 s80, s50, 3\n"
    "s_add_u32 s81, s80, 1\n"
    "s_and_b32 s81, s81, 6\n"
    "s_and_b32 s82, s50, 2\n"
    "s_bitcmp1_b32 s50, 0\n"
    "s_cselect_b32 s83, 1, 4\n"
    "s_cselect_b32 s84, 512, 0\n"
    "s_add_u32 s83, s83, s82\n"
    "s_lshr_b32 s82, s83, 2\n"
    "s_lshl_b32 s82, s82, 10\n"
    "s_and_b32 s83, s83, 3\n"
    "s_lshl_b32 s83, s83, 2\n"
    "s_add_u32 s82, s82, s83\n"
    "s_add_u32 s82, s82, s84\n"
    "s_add_u32 s82, s82, s79\n"
    "s_sub_u32 s82, s82, 1280\n"
    "v_lshl_add_u32 v4, v2, 2, s82\n"
    "s_mov_b32 vcc_lo, 0x1f00000\n"
    "s_mov_b32 vcc_hi, 0\n"
    "s_add_u32 s83, s81, 0\n"
    "s_lshr_b32 s84, s83, 2\n"
    "s_lshl_b32 s84, s84, 10\n"
    "s_and_b32 s83, s83, 3\n"
    "s_lshl_b32 s83, s83, 2\n"
    "s_add_u32 s83, s83, s84\n"
    "v_add_u32 v5, s83, v0\n"
    "v_add_u32 v220, 0, v4\n"
    "v_cndmask_b32 v216, v5, v220, vcc\n"
    "s_add_u32 s83, s81, 1\n"
    "s_lshr_b32 s84, s83, 2\n"
    "s_lshl_b32 s84, s84, 10\n"
    "s_and_b32 s83, s83, 3\n"
    "s_lshl_b32 s83, s83, 2\n"
    "s_add_u32 s83, s83, s84\n"
    "v_add_u32 v5, s83, v0\n"
    "v_add_u32 v220, 16, v4\n"
    "v_cndmask_b32 v217, v5, v220, vcc\n"
    "s_add_u32 s83, s81, 2\n"
    "s_lshr_b32 s84, s83, 2\n"
    "s_lshl_b32 s84, s84, 10\n"
    "s_and_b32 s83, s83, 3\n"
    "s_lshl_b32 s83, s83, 2\n"
    "s_add_u32 s83, s83, s84\n"
    "v_add_u32 v5, s83, v0\n"
    "v_add_u32 v220, 32, v4\n"
    "v_cndmask_b32 v218, v5, v220, vcc\n"
    "s_add_u32 s83, s81, 3\n"
    "s_lshr_b32 s84, s83, 2\n"
    "s_lshl_b32 s84, s84, 10\n"
    "s_and_b32 s83, s83, 3\n"
    "s_lshl_b32 s83, s83, 2\n"
    "s_add_u32 s83, s83, s84\n"
    "v_add_u32 v5, s83, v0\n"
    "v_add_u32 v220, 48, v4\n"
    "v_cndmask_b32 v219, v5, v220, vcc\n"
    "s_waitcnt lgkmcnt(4)\n"
    "v_mfma_f32_32x32x16_bf16 v[176:191], v[244:247], v[64:67], v[176:191]\n"
    "ds_read_b64_tr_b16 v[240:241], v13 offset:4608\n"
    "ds_read_b64_tr_b16 v[242:243], v13 offset:4896\n"
    "s_waitcnt lgkmcnt(4)\n"
    "v_mfma_f32_32x32x16_bf16 v[192:207], v[248:251], v[64:67], v[192:207]\n"
    "ds_read_b64_tr_b16 v[244:245], v14 offset:4608\n"
    "ds_read_b64_tr_b16 v[246:247], v14 offset:4896\n"
    "s_add_u32 m0, s69, 0\n"
    "s_add_u32 s78, s65, 1105920\n"
    "s_bfm_b64 exec, 54, 0\n"
    "buffer_load_dwordx4 v8, s[12:15], s78 offen lds\n"
    "s_mov_b64 exec, -1\n"
    "s_waitcnt lgkmcnt(2)\n"
    "v_mfma_f32_32x32x16_bf16 v[160:175], v[240:243], v[68:71], v[160:175]\n"
    "ds_read_b64_tr_b16 v[248:249], v15 offset:4608\n"
    "ds_read_b64_tr_b16 v[250:251], v15 offset:4896\n"
    "s_waitcnt lgkmcnt(2)\n"
    "v_mfma_f32_32x32x16_bf16 v[176:191], v[244:247], v[68:71], v[176:191]\n"
    "ds_read_b64_tr_b16 v[240:241], v13 offset:9216\n"
    "ds_read_b64_tr_b16 v[242:243], v13 offset:9504\n"
    "s_waitcnt lgkmcnt(2)\n"
    "v_mfma_f32_32x32x16_bf16 v[192:207], v[248:251], v[68:71], v[192:207]\n"
    "ds_read_b64_tr_b16 v[244:245], v14 offset:9216\n"
    "ds_read_b64_tr_b16 v[246:247], v14 offset:9504\n"
    "s_add_u32 m0, s69, 864\n"
    "s_add_u32 s78, s65, 1105920\n"
    "s_bfm_b64 exec, 54, 0\n"
    "buffer_load_dwordx4 v9, s[12:15], s78 offen lds\n"
    "s_mov_b64 exec, -1\n"
    "s_waitcnt lgkmcnt(2)\n"
    "v_mfma_f32_32x32x16_bf16 v[160:175], v[240:243], v[72:75], v[160:175]\n"
    "ds_read_b64_tr_b16 v[248:249], v15 offset:9216\n"
    "ds_read_b64_tr_b16 v[250:251], v15 offset:9504\n"
    "s_waitcnt lgkmcnt(2)\n"
    "v_mfma_f32_32x32x16_bf16 v[176:191], v[244:247], v[72:75], v[176:191]\n"
    "ds_read_b64_tr_b16 v[240:241], v13 offset:13824\n"
    "ds_read_b64_tr_b16 v[242:243], v13 offset:14112\n"
    "s_add_u32 m0, s69, 1728\n"
    "s_add_u32 s78, s65, 1105920\n"
    "s_bfm_b64 exec, 36, 0\n"
    "buffer_load_dwordx4 v10, s[12:15], s78 offen lds\n"
    "s_mov_b64 exec, -1\n"
    "s_waitcnt lgkmcnt(2)\n"
    "v_mfma_f32_32x32x16_bf16 v[192:207], v[248:251], v[72:75], v[192:207]\n"
    "ds_read_b64_tr_b16 v[244:245], v14 offset:13824\n"
    "ds_read_b64_tr_b16 v[246:247], v14 offset:14112\n"
    "s_waitcnt lgkmcnt(2)\n"
    "v_mfma_f32_32x32x16_bf16 v[160:175], v[240:243], v[76:79], v[160:175]\n"
    "ds_read_b64_tr_b16 v[248:249], v15 offset:13824\n"
    "ds_read_b64_tr_b16 v[250:251], v15 offset:14112\n"
    "s_waitcnt lgkmcnt(2)\n"
    "v_mfma_f32_32x32x16_bf16 v[176:191], v[244:247], v[76:79], v[176:191]\n"
    "s_waitcnt vmcnt(3)\n"
    "s_barrier\n"
    "s_add_u32 s78, s60, 0\n"
    "buffer_load_dwordx2 v[6:7], v3, s[20:23], s78 offen sc1\n"
    "v_add_u32 v13, 18432, v13\n"
    "ds_read_b64_tr_b16 v[240:241], v13 offset:0\n"
    "ds_read_b64_tr_b16 v[242:243], v13 offset:288\n"
    "s_add_u32 m0, s69, 18432\n"
    "s_add_u32 s78, s65, 1327104\n"
    "s_bfm_b64 exec, 54, 0\n"
    "buffer_load_dwordx4 v8, s[12:15], s78 offen lds\n"
    "s_mov_b64 exec, -1\n"
    "s_waitcnt lgkmcnt(2)\n"
    "v_mfma_f32_32x32x16_bf16 v[192:207], v[248:251], v[76:79], v[192:207]\n"
    "v_add_u32 v14, 18432, v14\n"
    "ds_read_b64_tr_b16 v[244:245], v14 offset:0\n"
    "ds_read_b64_tr_b16 v[246:247], v14 offset:288\n"
    "s_waitcnt lgkmcnt(2)\n"
    "v_mfma_f32_32x32x16_bf16 v[160:175], v[240:243], v[80:83], v[160:175]\n"
    "v_mov_b32 v4, 384\n"
    "v_mov_b32 v5, 18432\n"
    "v_cndmask_b32 v4, v5, v4, s[98:99]\n"
    "v_add_u32 v15, v4, v15\n"
    "ds_read_b64_tr_b16 v[248:249], v15 offset:0\n"
    "ds_read_b64_tr_b16 v[250:251], v15 offset:288\n"
    "s_waitcnt lgkmcnt(2)\n"
    "v_mfma_f32_32x32x16_bf16 v[176:191], v[244:247], v[80:83], v[176:191]\n"
    "ds_read_b64_tr_b16 v[240:241], v13 offset:4608\n"
    "ds_read_b64_tr_b16 v[242:243], v13 offset:4896\n"
    "s_waitcnt lgkmcnt(2)\n"
    "v_mfma_f32_32x32x16_bf16 v[192:207], v[248:251], v[80:83], v[192:207]\n"
    "ds_read_b64_tr_b16 v[244:245], v14 offset:4608\n"
    "ds_read_b64_tr_b16 v[246:247], v14 offset:4896\n"
    "s_add_u32 m0, s69, 19296\n"
    "s_add_u32 s78, s65, 1327104\n"
    "s_bfm_b64 exec, 54, 0\n"
    "buffer_load_dwordx4 v9, s[12:15], s78 offen lds\n"
    "s_mov_b64 exec, -1\n"
    "s_waitcnt lgkmcnt(2)\n"
    "v_mfma_f32_32x32x16_bf16 v[160:175], v[240:243], v[84:87], v[160:175]\n"
    "ds_read_b64_tr_b16 v[248:249], v15 offset:4608\n"
    "ds_read_b64_tr_b16 v[250:251], v15 offset:4896\n"
    "s_waitcnt lgkmcnt(2)\n"
    "v_mfma_f32_32x32x16_bf16 v[176:191], v[244:247], v[84:87], v[176:191]\n"
    "ds_read_b64_tr_b16 v[240:241], v13 offset:9216\n"
    "ds_read_b64_tr_b16 v[242:243], v13 offset:9504\n"
    "s_waitcnt lgkmcnt(2)\n"
    "v_mfma_f32_32x32x16_bf16 v[192:207], v[248:251], v[84:87], v[192:207]\n"
    "ds_read_b64_tr_b16 v[244:245], v14 offset:9216\n"
    "ds_read_b64_tr_b16 v[246:247], v14 offset:9504\n"
    "s_waitcnt lgkmcnt(2)\n"
    "v_mfma_f32_32x32x16_bf16 v[160:175], v[240:243], v[88:91], v[160:175]\n"
    "ds_read_b64_tr_b16 v[248:249], v15 offset:9216\n"
    "ds_read_b64_tr_b16 v[250:251], v15 offset:9504\n"
    "s_add_u32 m0, s69, 20160\n"
    "s_add_u32 s78, s65, 1327104\n"
    "s_bfm_b64 exec, 36, 0\n"
    "buffer_load_dwordx4 v10, s[12:15], s78 offen lds\n"
    "s_mov_b64 exec, -1\n"
    "s_waitcnt lgkmcnt(2)\n"
    "v_mfma_f32_32x32x16_bf16 v[176:191], v[244:247], v[88:91], v[176:191]\n"
    "ds_read_b64_tr_b16 v[240:241], v13 offset:13824\n"
    "ds_read_b64_tr_b16 v[242:243], v13 offset:14112\n"
    "s_waitcnt lgkmcnt(2)\n"
    "v_mfma_f32_32x32x16_bf16 v[192:207], v[248:251], v[88:91], v[192:207]\n"
    "ds_read_b64_tr_b16 v[244:245], v14 offset:13824\n"
    "ds_read_b64_tr_b16 v[246:247], v14 offset:14112\n"
    "s_waitcnt lgkmcnt(2)\n"
    "v_mfma_f32_32x32x16_bf16 v[160:175], v[240:243], v[92:95], v[160:175]\n"
    "ds_read_b64_tr_b16 v[248:249], v15 offset:13824\n"
    "ds_read_b64_tr_b16 v[250:251], v15 offset:14112\n"
    "s_waitcnt lgkmcnt(2)\n"
    "v_mfma_f32_32x32x16_bf16 v[176:191], v[244:247], v[92:95], v[176:191]\n"
    "s_waitcnt vmcnt(4)\n"
    "s_barrier\n"
    "v_add_u32 v13, 4294912000, v13\n"
    "ds_read_b64_tr_b16 v[240:241], v13 offset:0\n"
    "ds_read_b64_tr_b16 v[242:243], v13 offset:288\n"
    "s_add_u32 m0, s69, 36864\n"
    "s_add_u32 s78, s65, 1548288\n"
    "s_bfm_b64 exec, 54, 0\n"
    "buffer_load_dwordx4 v8, s[12:15], s78 offen lds\n"
    "s_mov_b64 exec, -1\n"
    "s_waitcnt lgkmcnt(2)\n"
    "v_mfma_f32_32x32x16_bf16 v[192:207], v[248:251], v[92:95], v[192:207]\n"
    "v_add_u32 v14, 4294912000, v14\n"
    "ds_read_b64_tr_b16 v[244:245], v14 offset:0\n"
    "ds_read_b64_tr_b16 v[246:247], v14 offset:288\n"
    "s_waitcnt lgkmcnt(2)\n"
    "v_mfma_f32_32x32x16_bf16 v[160:175], v[240:243], v[96:99], v[160:175]\n"
    "v_mov_b32 v4, 64\n"
    "v_mov_b32 v5, 4294912000\n"
    "v_cndmask_b32 v4, v5, v4, s[98:99]\n"
    "v_add_u32 v15, v4, v15\n"
    "ds_read_b64_tr_b16 v[248:249], v15 offset:0\n"
    "ds_read_b64_tr_b16 v[250:251], v15 offset:288\n"
    "s_waitcnt lgkmcnt(2)\n"
    "v_mfma_f32_32x32x16_bf16 v[176:191], v[244:247], v[96:99], v[176:191]\n"
    "ds_read_b64_tr_b16 v[240:241], v13 offset:4608\n"
    "ds_read_b64_tr_b16 v[242:243], v13 offset:4896\n"
    "s_waitcnt lgkmcnt(2)\n"
    "v_mfma_f32_32x32x16_bf16 v[192:207], v[248:251], v[96:99], v[192:207]\n"
    "ds_read_b64_tr_b16 v[244:245], v14 offset:4608\n"
    "ds_read_b64_tr_b16 v[246:247], v14 offset:4896\n"
    "s_add_u32 m0, s69, 37728\n"
    "s_add_u32 s78, s65, 1548288\n"
    "s_bfm_b64 exec, 54, 0\n"
    "buffer_load_dwordx4 v9, s[12:15], s78 offen lds\n"
    "s_mov_b64 exec, -1\n"
    "s_waitcnt lgkmcnt(2)\n"
    "v_mfma_f32_32x32x16_bf16 v[160:175], v[240:243], v[100:103], v[160:175]\n"
    "ds_read_b64_tr_b16 v[248:249], v15 offset:4608\n"
    "ds_read_b64_tr_b16 v[250:251], v15 offset:4896\n"
    "s_waitcnt lgkmcnt(2)\n"
    "v_mfma_f32_32x32x16_bf16 v[176:191], v[244:247], v[100:103], v[176:191]\n"
    "ds_read_b64_tr_b16 v[240:241], v13 offset:9216\n"
    "ds_read_b64_tr_b16 v[242:243], v13 offset:9504\n"
    "s_waitcnt lgkmcnt(2)\n"
    "v_mfma_f32_32x32x16_bf16 v[192:207], v[248:251], v[100:103], v[192:207]\n"
    "ds_read_b64_tr_b16 v[244:245], v14 offset:9216\n"
    "ds_read_b64_tr_b16 v[246:247], v14 offset:9504\n"
    "s_waitcnt lgkmcnt(2)\n"
    "v_mfma_f32_32x32x16_bf16 v[160:175], v[240:243], v[104:107], v[160:175]\n"
    "ds_read_b64_tr_b16 v[248:249], v15 offset:9216\n"
    "ds_read_b64_tr_b16 v[250:251], v15 offset:9504\n"
    "s_add_u32 m0, s69, 38592\n"
    "s_add_u32 s78, s65, 1548288\n"
    "s_bfm_b64 exec, 36, 0\n"
    "buffer_load_dwordx4 v10, s[12:15], s78 offen lds\n"
    "s_mov_b64 exec, -1\n"
    "s_waitcnt lgkmcnt(2)\n"
    "v_mfma_f32_32x32x16_bf16 v[176:191], v[244:247], v[104:107], v[176:191]\n"
    "ds_read_b64_tr_b16 v[240:241], v13 offset:13824\n"
    "ds_read_b64_tr_b16 v[242:243], v13 offset:14112\n"
    "s_waitcnt lgkmcnt(2)\n"
    "v_mfma_f32_32x32x16_bf16 v[192:207], v[248:251], v[104:107], v[192:207]\n"
    "ds_read_b64_tr_b16 v[244:245], v14 offset:13824\n"
    "ds_read_b64_tr_b16 v[246:247], v14 offset:14112\n"
    "s_waitcnt lgkmcnt(2)\n"
    "v_mfma_f32_32x32x16_bf16 v[160:175], v[240:243], v[108:111], v[160:175]\n"
    "ds_read_b64_tr_b16 v[248:249], v15 offset:13824\n"
    "ds_read_b64_tr_b16 v[250:251], v15 offset:14112\n"
    "s_waitcnt lgkmcnt(2)\n"
    "v_mfma_f32_32x32x16_bf16 v[176:191], v[244:247], v[108:111], v[176:191]\n"
    "s_waitcnt vmcnt(3)\n"
    "s_barrier\n"
    "v_cmp_ne_u64 vcc, v[6:7], v[228:229]\n"
    "s_nop 0\n"
    "s_and_b64 vcc, vcc, s[88:89]\n"
    "s_cbranch_vccz Lok_28\n"
    "s_mov_b32 s86, 0x40000\n"
    "s_add_u32 s78, s60, 0\n"
    "Lspin_27:\n"
    "s_sleep 1\n"
    "buffer_load_dwordx2 v[6:7], v3, s[20:23], s78 offen sc1\n"
    "s_waitcnt vmcnt(0)\n"
    "s_sub_u32 s86, s86, 1\n"
    "s_cbranch_scc1 Lok_28\n"
    "v_cmp_ne_u64 vcc, v[6:7], v[228:229]\n"
    "s_nop 1\n"
    "s_and_b64 vcc, vcc, s[88:89]\n"
    "s_cbranch_vccnz Lspin_27\n"
    "Lok_28:\n"
    "s_mov_b64 vcc, s[90:91]\n"
    "s_or_b32 s79, s54, s50\n"
    "s_cmp_eq_u32 s79, 0\n"
    "s_cbranch_scc0 Lek_29\n"
    "s_lshl_b32 s79, s53, 8\n"
    "s_add_u32 s78, s79, 4096\n"
    "s_mov_b64 exec, 1\n"
    "buffer_store_dwordx2 v[228:229], off, s[20:23], s78 sc1\n"
    "s_mov_b64 exec, -1\n"
    "Lek_29:\n"
    "v_add_u32 v13, 18432, v13\n"
    "ds_read_b64_tr_b16 v[240:241], v13 offset:0\n"
    "ds_read_b64_tr_b16 v[242:243], v13 offset:288\n"
    "s_add_u32 m0, s69, 55296\n"
    "s_add_u32 s78, s65, 1769472\n"
    "s_bfm_b64 exec, 54, 0\n"
    "buffer_load_dwordx4 v8, s[12:15], s78 offen lds\n"
    "s_mov_b64 exec, -1\n"
    "s_waitcnt lgkmcnt(2)\n"
    "v_mfma_f32_32x32x16_bf16 v[192:207], v[248:251], v[108:111], v[192:207]\n"
    "v_add_u32 v14, 18432, v14\n"
    "ds_read_b64_tr_b16 v[244:245], v14 offset:0\n"
    "ds_read_b64_tr_b16 v[246:247], v14 offset:288\n"
    "s_add_u32 m0, s69, 56160\n"
    "s_add_u32 s78, s65, 1769472\n"
    "s_bfm_b64 exec, 54, 0\n"
    "buffer_load_dwordx4 v9, s[12:15], s78 offen lds\n"
    "s_mov_b64 exec, -1\n"
    "s_waitcnt lgkmcnt(2)\n"
    "v_mfma_f32_32x32x16_bf16 v[160:175], v[240:243], v[112:115], v[160:175]\n"
    "v_mov_b32 v4, 64\n"
    "v_mov_b32 v5, 18432\n"
    "v_cndmask_b32 v4, v5, v4, s[98:99]\n"
    "v_add_u32 v15, v4, v15\n"
    "ds_read_b64_tr_b16 v[248:249], v15 offset:0\n"
    "ds_read_b64_tr_b16 v[250:251], v15 offset:288\n"
    "s_waitcnt lgkmcnt(2)\n"
    "v_mfma_f32_32x32x16_bf16 v[176:191], v[244:247], v[112:115], v[176:191]\n"
    "ds_read_b64_tr_b16 v[240:241], v13 offset:4608\n"
    "ds_read_b64_tr_b16 v[242:243], v13 offset:4896\n"
    "s_add_u32 m0, s69, 57024\n"
    "s_add_u32 s78, s65, 1769472\n"
    "s_bfm_b64 exec, 36, 0\n"
    "buffer_load_dwordx4 v10, s[12:15], s78 offen lds\n"
    "s_mov_b64 exec, -1\n"
    "s_waitcnt lgkmcnt(2)\n"
    "v_mfma_f32_32x32x16_bf16 v[192:207], v[248:251], v[112:115], v[192:207]\n"
    "ds_read_b64_tr_b16 v[244:245], v14 offset:4608\n"
    "ds_read_b64_tr_b16 v[246:247], v14 offset:4896\n"
    "s_waitcnt lgkmcnt(2)\n"
    "v_mfma_f32_32x32x16_bf16 v[160:175], v[240:243], v[116:119], v[160:175]\n"
    "ds_read_b64_tr_b16 v[248:249], v15 offset:4608\n"
    "ds_read_b64_tr_b16 v[250:251], v15 offset:4896\n"
    "s_add_u32 s78, s68, 0\n"
    "s_mov_b64 exec, s[96:97]\n"
    "buffer_load_dwordx4 v[16:19], v2, s[20:23], s78 offen sc1\n"
    "s_mov_b64 exec, -1\n"
    "s_waitcnt lgkmcnt(2)\n"
    "v_mfma_f32_32x32x16_bf16 v[176:191], v[244:247], v[116:119], v[176:191]\n"
    "ds_read_b64_tr_b16 v[240:241], v13 offset:9216\n"
    "ds_read_b64_tr_b16 v[242:243], v13 offset:9504\n"
    "s_add_u32 s78, s68, 1024\n"
    "s_mov_b64 exec, s[96:97]\n"
    "buffer_load_dwordx4 v[20:23], v2, s[20:23], s78 offen sc1\n"
    "s_mov_b64 exec, -1\n"
    "s_waitcnt lgkmcnt(2)\n"
    "v_mfma_f32_32x32x16_bf16 v[192:207], v[248:251], v[116:119], v[192:207]\n"
    "ds_read_b64_tr_b16 v[244:245], v14 offset:9216\n"
    "ds_read_b64_tr_b16 v[246:247], v14 offset:9504\n"
    "s_waitcnt lgkmcnt(2)\n"
    "v_mfma_f32_32x32x16_bf16 v[160:175], v[240:243], v[120:123], v[160:175]\n"
    "ds_read_b64_tr_b16 v[248:249], v15 offset:9216\n"
    "ds_read_b64_tr_b16 v[250:251], v15 offset:9504\n"
    "s_add_u32 s78, s68, 2048\n"
    "s_mov_b64 exec, s[96:97]\n"
    "buffer_load_dwordx4 v[24:27], v2, s[20:23], s78 offen sc1\n"
    "s_mov_b64 exec, -1\n"
    "s_waitcnt lgkmcnt(2)\n"
    "v_mfma_f32_32x32x16_bf16 v[176:191], v[244:247], v[120:123], v[176:191]\n"
    "ds_read_b64_tr_b16 v[240:241], v13 offset:13824\n"
    "ds_read_b64_tr_b16 v[242:243], v13 offset:14112\n"
    "s_waitcnt lgkmcnt(2)\n"
    "v_mfma_f32_32x32x16_bf16 v[192:207], v[248:251], v[120:123], v[192:207]\n"
    "ds_read_b64_tr_b16 v[244:245], v14 offset:13824\n"
    "ds_read_b64_tr_b16 v[246:247], v14 offset:14112\n"
    "s_add_u32 s78, s68, 3072\n"
    "s_mov_b64 exec, s[96:97]\n"
    "buffer_load_dwordx4 v[28:31], v2, s[20:23], s78 offen sc1\n"
    "s_mov_b64 exec, -1\n"
    "s_waitcnt lgkmcnt(2)\n"
    "v_mfma_f32_32x32x16_bf16 v[160:175], v[240:243], v[124:127], v[160:175]\n"
    "ds_read_b64_tr_b16 v[248:249], v15 offset:13824\n"
    "ds_read_b64_tr_b16 v[250:251], v15 offset:14112\n"
    "s_waitcnt lgkmcnt(2)\n"
    "v_mfma_f32_32x32x16_bf16 v[176:191], v[244:247], v[124:127], v[176:191]\n"
    "s_waitcnt vmcnt(7)\n"
    "s_barrier\n"
    "v_add_u32 v13, 18432, v13\n"
    "ds_read_b64_tr_b16 v[240:241], v13 offset:0\n"
    "ds_read_b64_tr_b16 v[242:243], v13 offset:288\n"
    "s_add_u32 s78, s68, 4096\n"
    "s_mov_b64 exec, s[96:97]\n"
    "buffer_load_dwordx4 v[32:35], v2, s[20:23], s78 offen sc1\n"
    "s_mov_b64 exec, -1\n"
    "s_waitcnt lgkmcnt(2)\n"
    "v_mfma_f32_32x32x16_bf16 v[192:207], v[248:251], v[124:127], v[192:207]\n"
    "v_add_u32 v14, 18432, v14\n"
    "ds_read_b64_tr_b16 v[244:245], v14 offset:0\n"
    "ds_read_b64_tr_b16 v[246:247], v14 offset:288\n"
    "s_waitcnt lgkmcnt(2)\n"
    "v_mfma_f32_32x32x16_bf16 v[160:175], v[240:243], v[128:131], v[160:175]\n"
    "v_mov_b32 v4, 64\n"
    "v_mov_b32 v5, 18432\n"
    "v_cndmask_b32 v4, v5, v4, s[98:99]\n"
    "v_add_u32 v15, v4, v15\n"
    "ds_read_b64_tr_b16 v[248:249], v15 offset:0\n"
    "ds_read_b64_tr_b16 v[250:251], v15 offset:288\n"
    "s_add_u32 s78, s68, 5120\n"
    "s_mov_b64 exec, s[96:97]\n"
    "buffer_load_dwordx4 v[36:39], v2, s[20:23], s78 offen sc1\n"
    "s_mov_b64 exec, -1\n"
    "s_waitcnt lgkmcnt(2)\n"
    "v_mfma_f32_32x32x16_bf16 v[176:191], v[244:247], v[128:131], v[176:191]\n"
    "ds_read_b64_tr_b16 v[240:241], v13 offset:4608\n"
    "ds_read_b64_tr_b16 v[242:243], v13 offset:4896\n"
    "s_waitcnt lgkmcnt(2)\n"
    "v_mfma_f32_32x32x16_bf16 v[192:207], v[248:251], v[128:131], v[192:207]\n"
    "ds_read_b64_tr_b16 v[244:245], v14 offset:4608\n"
    "ds_read_b64_tr_b16 v[246:247], v14 offset:4896\n"
    "s_add_u32 s78, s68, 6144\n"
    "s_mov_b64 exec, s[96:97]\n"
    "buffer_load_dwordx4 v[40:43], v2, s[20:23], s78 offen sc1\n"
    "s_mov_b64 exec, -1\n"
    "s_waitcnt lgkmcnt(2)\n"
    "v_mfma_f32_32x32x16_bf16 v[160:175], v[240:243], v[132:135], v[160:175]\n"
    "ds_read_b64_tr_b16 v[248:249], v15 offset:4608\n"
    "ds_read_b64_tr_b16 v[250:251], v15 offset:4896\n"
    "s_waitcnt lgkmcnt(2)\n"
    "v_mfma_f32_32x32x16_bf16 v[176:191], v[244:247], v[132:135], v[176:191]\n"
    "ds_read_b64_tr_b16 v[240:241], v13 offset:9216\n"
    "ds_read_b64_tr_b16 v[242:243], v13 offset:9504\n"
    "s_add_u32 s78, s68, 7168\n"
    "s_mov_b64 exec, s[96:97]\n"
    "buffer_load_dwordx4 v[44:47], v2, s[20:23], s78 offen sc1\n"
    "s_mov_b64 exec, -1\n"
    "s_waitcnt lgkmcnt(2)\n"
    "v_mfma_f32_32x32x16_bf16 v[192:207], v[248:251], v[132:135], v[192:207]\n"
    "ds_read_b64_tr_b16 v[244:245], v14 offset:9216\n"
    "ds_read_b64_tr_b16 v[246:247], v14 offset:9504\n"
    "s_waitcnt lgkmcnt(2)\n"
    "v_mfma_f32_32x32x16_bf16 v[160:175], v[240:243], v[136:139], v[160:175]\n"
    "ds_read_b64_tr_b16 v[248:249], v15 offset:9216\n"
    "ds_read_b64_tr_b16 v[250:251], v15 offset:9504\n"
    "s_add_u32 s78, s68, 8192\n"
    "s_mov_b64 exec, s[96:97]\n"
    "buffer_load_dwordx4 v[48:51], v2, s[20:23], s78 offen sc1\n"
    "s_mov_b64 exec, -1\n"
    "s_waitcnt lgkmcnt(2)\n"
    "v_mfma_f32_32x32x16_bf16 v[176:191], v[244:247], v[136:139], v[176:191]\n"
    "ds_read_b64_tr_b16 v[240:241], v13 offset:13824\n"
    "ds_read_b64_tr_b16 v[242:243], v13 offset:14112\n"
    "s_waitcnt lgkmcnt(2)\n"
    "v_mfma_f32_32x32x16_bf16 v[192:207], v[248:251], v[136:139], v[192:207]\n"
    "ds_read_b64_tr_b16 v[244:245], v14 offset:13824\n"
    "ds_read_b64_tr_b16 v[246:247], v14 offset:14112\n"
    "s_add_u32 s78, s68, 9216\n"
    "s_mov_b64 exec, s[96:97]\n"
    "buffer_load_dwordx4 v[52:55], v2, s[20:23], s78 offen sc1\n"
    "s_mov_b64 exec, -1\n"
    "s_waitcnt lgkmcnt(2)\n"
    "v_mfma_f32_32x32x16_bf16 v[160:175], v[240:243], v[140:143], v[160:175]\n"
    "ds_read_b64_tr_b16 v[248:249], v15 offset:13824\n"
    "ds_read_b64_tr_b16 v[250:251], v15 offset:14112\n"
    "s_waitcnt lgkmcnt(2)\n"
    "v_mfma_f32_32x32x16_bf16 v[176:191], v[244:247], v[140:143], v[176:191]\n"
    "s_waitcnt vmcnt(10)\n"
    "s_barrier\n"
    "v_add_u32 v13, 18432, v13\n"
    "ds_read_b64_tr_b16 v[240:241], v13 offset:0\n"
    "ds_read_b64_tr_b16 v[242:243], v13 offset:288\n"
    "s_add_u32 s78, s68, 10240\n"
    "s_mov_b64 exec, s[96:97]\n"
    "buffer_load_dwordx4 v[56:59], v2, s[20:23], s78 offen sc1\n"
    "s_mov_b64 exec, -1\n"
    "s_waitcnt lgkmcnt(2)\n"
    "v_mfma_f32_32x32x16_bf16 v[192:207], v[248:251], v[140:143], v[192:207]\n"
    "v_add_u32 v14, 18432, v14\n"
    "ds_read_b64_tr_b16 v[244:245], v14 offset:0\n"
    "ds_read_b64_tr_b16 v[246:247], v14 offset:288\n"
    "s_waitcnt lgkmcnt(2)\n"
    "v_mfma_f32_32x32x16_bf16 v[160:175], v[240:243], v[144:147], v[160:175]\n"
    "v_mov_b32 v4, 384\n"
    "v_mov_b32 v5, 18432\n"
    "v_cndmask_b32 v4, v5, v4, s[98:99]\n"
    "v_add_u32 v15, v4, v15\n"
    "ds_read_b64_tr_b16 v[248:249], v15 offset:0\n"
    "ds_read_b64_tr_b16 v[250:251], v15 offset:288\n"
    "s_add_u32 s78, s68, 11264\n"
    "s_mov_b64 exec, s[96:97]\n"
    "buffer_load_dwordx4 v[60:63], v2, s[20:23], s78 offen sc1\n"
    "s_mov_b64 exec, -1\n"
    "s_waitcnt lgkmcnt(2)\n"
    "v_mfma_f32_32x32x16_bf16 v[176:191], v[244:247], v[144:147], v[176:191]\n"
    "ds_read_b64_tr_b16 v[240:241], v13 offset:4608\n"
    "ds_read_b64_tr_b16 v[242:243], v13 offset:4896\n"
    "s_waitcnt lgkmcnt(2)\n"
    "v_mfma_f32_32x32x16_bf16 v[192:207], v[248:251], v[144:147], v[192:207]\n"
    "ds_read_b64_tr_b16 v[244:245], v14 offset:4608\n"
    "ds_read_b64_tr_b16 v[246:247], v14 offset:4896\n"
    "s_add_u32 s78, s68, 12288\n"
    "s_mov_b64 exec, s[96:97]\n"
    "buffer_load_dwordx4 v[64:67], v2, s[20:23], s78 offen sc1\n"
    "s_mov_b64 exec, -1\n"
    "s_waitcnt lgkmcnt(2)\n"
    "v_mfma_f32_32x32x16_bf16 v[160:175], v[240:243], v[148:151], v[160:175]\n"
    "ds_read_b64_tr_b16 v[248:249], v15 offset:4608\n"
    "ds_read_b64_tr_b16 v[250:251], v15 offset:4896\n"
    "s_waitcnt lgkmcnt(2)\n"
    "v_mfma_f32_32x32x16_bf16 v[176:191], v[244:247], v[148:151], v[176:191]\n"
    "ds_read_b64_tr_b16 v[240:241], v13 offset:9216\n"
    "ds_read_b64_tr_b16 v[242:243], v13 offset:9504\n"
    "s_add_u32 s78, s68, 13312\n"
    "s_mov_b64 exec, s[96:97]\n"
    "buffer_load_dwordx4 v[68:71], v2, s[20:23], s78 offen sc1\n"
    "s_mov_b64 exec, -1\n"
    "s_waitcnt lgkmcnt(2)\n"
    "v_mfma_f32_32x32x16_bf16 v[192:207], v[248:251], v[148:151], v[192:207]\n"
    "ds_read_b64_tr_b16 v[244:245], v14 offset:9216\n"
    "ds_read_b64_tr_b16 v[246:247], v14 offset:9504\n"
    "s_waitcnt lgkmcnt(2)\n"
    "v_mfma_f32_32x32x16_bf16 v[160:175], v[240:243], v[152:155], v[160:175]\n"
    "ds_read_b64_tr_b16 v[248:249], v15 offset:9216\n"
    "ds_read_b64_tr_b16 v[250:251], v15 offset:9504\n"
    "s_add_u32 s78, s68, 14336\n"
    "s_mov_b64 exec, s[96:97]\n"
    "buffer_load_dwordx4 v[72:75], v2, s[20:23], s78 offen sc1\n"
    "s_mov_b64 exec, -1\n"
    "s_waitcnt lgkmcnt(2)\n"
    "v_mfma_f32_32x32x16_bf16 v[176:191], v[244:247], v[152:155], v[176:191]\n"
    "ds_read_b64_tr_b16 v[240:241], v13 offset:13824\n"
    "ds_read_b64_tr_b16 v[242:243], v13 offset:14112\n"
    "s_waitcnt lgkmcnt(2)\n"
    "v_mfma_f32_32x32x16_bf16 v[192:207], v[248:251], v[152:155], v[192:207]\n"
    "ds_read_b64_tr_b16 v[244:245], v14 offset:13824\n"
    "ds_read_b64_tr_b16 v[246:247], v14 offset:14112\n"
    "s_add_u32 s78, s68, 15360\n"
    "s_mov_b64 exec, s[96:97]\n"
    "buffer_load_dwordx4 v[76:79], v2, s[20:23], s78 offen sc1\n"
    "s_mov_b64 exec, -1\n"
    "s_waitcnt lgkmcnt(2)\n"
    "v_mfma_f32_32x32x16_bf16 v[160:175], v[240:243], v[156:159], v[160:175]\n"
    "ds_read_b64_tr_b16 v[248:249], v15 offset:13824\n"
    "ds_read_b64_tr_b16 v[250:251], v15 offset:14112\n"
    "s_waitcnt lgkmcnt(2)\n"
    "v_mfma_f32_32x32x16_bf16 v[176:191], v[244:247], v[156:159], v[176:191]\n"
    "s_waitcnt lgkmcnt(0)\n"
    "v_mfma_f32_32x32x16_bf16 v[192:207], v[248:251], v[156:159], v[192:207]\n"
    "s_nop 5\n"
    "v_mul_f32 v4, s70, v160\n"
    "v_mul_f32 v6, s70, v161\n"
    "v_exp_f32_e64 v4, v4 clamp\n"
    "v_exp_f32_e64 v6, v6 clamp\n"
    "v_max_f32 v5, s59, v160\n"
    "v_max_f32 v7, s59, v161\n"
    "v_add_f32 v160, v4, v5\n"
    "v_add_f32 v161, v6, v7\n"
    "v_mul_f32 v4, s70, v162\n"
    "v_mul_f32 v6, s70, v163\n"
    "v_exp_f32_e64 v4, v4 clamp\n"
    "v_exp_f32_e64 v6, v6 clamp\n"
    "v_max_f32 v5, s59, v162\n"
    "v_max_f32 v7, s59, v163\n"
    "v_add_f32 v162, v4, v5\n"
    "v_add_f32 v163, v6, v7\n"
    "v_mul_f32 v4, s70, v164\n"
    "v_mul_f32 v6, s70, v165\n"
    "v_exp_f32_e64 v4, v4 clamp\n"
    "v_exp_f32_e64 v6, v6 clamp\n"
    "v_max_f32 v5, s59, v164\n"
    "v_max_f32 v7, s59, v165\n"
    "v_add_f32 v164, v4, v5\n"
    "v_add_f32 v165, v6, v7\n"
    "s_waitcnt vmcnt(15)\n"
    "v_mfma_f32_32x32x16_bf16 v[96:111], v[232:235], v[16:19], 0\n"
    "v_mul_f32 v4, s70, v166\n"
    "v_mul_f32 v6, s70, v167\n"
    "v_exp_f32_e64 v4, v4 clamp\n"
    "v_exp_f32_e64 v6, v6 clamp\n"
    "v_max_f32 v5, s59, v166\n"
    "v_max_f32 v7, s59, v167\n"
    "v_add_f32 v166, v4, v5\n"
    "v_add_f32 v167, v6, v7\n"
    "s_waitcnt vmcnt(14)\n"
    "v_mfma_f32_32x32x16_bf16 v[96:111], v[232:235], v[20:23], v[96:111]\n"
    "v_mul_f32 v4, s70, v168\n"
    "v_mul_f32 v6, s70, v169\n"
    "v_exp_f32_e64 v4, v4 clamp\n"
    "v_exp_f32_e64 v6, v6 clamp\n"
    "v_max_f32 v5, s59, v168\n"
    "v_max_f32 v7, s59, v169\n"
    "v_add_f32 v168, v4, v5\n"
    "v_add_f32 v169, v6, v7\n"
    "s_waitcnt vmcnt(13)\n"
    "v_mfma_f32_32x32x16_bf16 v[96:111], v[232:235], v[24:27], v[96:111]\n"
    "v_mul_f32 v4, s70, v170\n"
    "v_mul_f32 v6, s70, v171\n"
    "v_exp_f32_e64 v4, v4 clamp\n"
    "v_exp_f32_e64 v6, v6 clamp\n"
    "v_max_f32 v5, s59, v170\n"
    "v_max_f32 v7, s59, v171\n"
    "v_add_f32 v170, v4, v5\n"
    "v_add_f32 v171, v6, v7\n"
    "s_waitcnt vmcnt(12)\n"
    "v_mfma_f32_32x32x16_bf16 v[96:111], v[232:235], v[28:31], v[96:111]\n"
    "v_mul_f32 v4, s70, v172\n"
    "v_mul_f32 v6, s70, v173\n"
    "v_exp_f32_e64 v4, v4 clamp\n"
    "v_exp_f32_e64 v6, v6 clamp\n"
    "v_max_f32 v5, s59, v172\n"
    "v_max_f32 v7, s59, v173\n"
    "v_add_f32 v172, v4, v5\n"
    "v_add_f32 v173, v6, v7\n"
    "s_waitcnt vmcnt(11)\n"
    "v_mfma_f32_32x32x16_bf16 v[96:111], v[232:235], v[32:35], v[96:111]\n"
    "v_mul_f32 v4, s70, v174\n"
    "v_mul_f32 v6, s70, v175\n"
    "v_exp_f32_e64 v4, v4 clamp\n"
    "v_exp_f32_e64 v6, v6 clamp\n"
    "v_max_f32 v5, s59, v174\n"
    "v_max_f32 v7, s59, v175\n"
    "v_add_f32 v174, v4, v5\n"
    "v_add_f32 v175, v6, v7\n"
    "s_waitcnt vmcnt(10)\n"
    "v_mfma_f32_32x32x16_bf16 v[96:111], v[232:235], v[36:39], v[96:111]\n"
    "v_mul_f32 v4, s70, v176\n"
    "v_mul_f32 v6, s70, v177\n"
    "v_exp_f32_e64 v4, v4 clamp\n"
    "v_exp_f32_e64 v6, v6 clamp\n"
    "v_max_f32 v5, s59, v176\n"
    "v_max_f32 v7, s59, v177\n"
    "v_add_f32 v176, v4, v5\n"
    "v_add_f32 v177, v6, v7\n"
    "s_waitcnt vmcnt(9)\n"
    "v_mfma_f32_32x32x16_bf16 v[96:111], v[232:235], v[40:43], v[96:111]\n"
    "v_mul_f32 v4, s70, v178\n"
    "v_mul_f32 v6, s70, v179\n"
    "v_exp_f32_e64 v4, v4 clamp\n"
    "v_exp_f32_e64 v6, v6 clamp\n"
    "v_max_f32 v5, s59, v178\n"
    "v_max_f32 v7, s59, v179\n"
    "v_add_f32 v178, v4, v5\n"
    "v_add_f32 v179, v6, v7\n"
    "s_waitcnt vmcnt(8)\n"
    "v_mfma_f32_32x32x16_bf16 v[96:111], v[232:235], v[44:47], v[96:111]\n"
    "v_mul_f32 v4, s70, v180\n"
    "v_mul_f32 v6, s70, v181\n"
    "v_exp_f32_e64 v4, v4 clamp\n"
    "v_exp_f32_e64 v6, v6 clamp\n"
    "v_max_f32 v5, s59, v180\n"
    "v_max_f32 v7, s59, v181\n"
    "v_add_f32 v180, v4, v5\n"
    "v_add_f32 v181, v6, v7\n"
    "s_waitcnt vmcnt(7)\n"
    "v_mfma_f32_32x32x16_bf16 v[96:111], v[232:235], v[48:51], v[96:111]\n"
    "v_mul_f32 v4, s70, v182\n"
    "v_mul_f32 v6, s70, v183\n"
    "v_exp_f32_e64 v4, v4 clamp\n"
    "v_exp_f32_e64 v6, v6 clamp\n"
    "v_max_f32 v5, s59, v182\n"
    "v_max_f32 v7, s59, v183\n"
    "v_add_f32 v182, v4, v5\n"
    "v_add_f32 v183, v6, v7\n"
    "s_waitcnt vmcnt(6)\n"
    "v_mfma_f32_32x32x16_bf16 v[96:111], v[232:235], v[52:55], v[96:111]\n"
    "v_mul_f32 v4, s70, v184\n"
    "v_mul_f32 v6, s70, v185\n"
    "v_exp_f32_e64 v4, v4 clamp\n"
    "v_exp_f32_e64 v6, v6 clamp\n"
    "v_max_f32 v5, s59, v184\n"
    "v_max_f32 v7, s59, v185\n"
    "v_add_f32 v184, v4, v5\n"
    "v_add_f32 v185, v6, v7\n"
    "s_waitcnt vmcnt(5)\n"
    "v_mfma_f32_32x32x16_bf16 v[96:111], v[232:235], v[56:59], v[96:111]\n"
    "v_mul_f32 v4, s70, v186\n"
    "v_mul_f32 v6, s70, v187\n"
    "v_exp_f32_e64 v4, v4 clamp\n"
    "v_exp_f32_e64 v6, v6 clamp\n"
    "v_max_f32 v5, s59, v186\n"
    "v_max_f32 v7, s59, v187\n"
    "v_add_f32 v186, v4, v5\n"
    "v_add_f32 v187, v6, v7\n"
    "s_waitcnt vmcnt(4)\n"
    "v_mfma_f32_32x32x16_bf16 v[96:111], v[232:235], v[60:63], v[96:111]\n"
    "v_mul_f32 v4, s70, v188\n"
    "v_mul_f32 v6, s70, v189\n"
    "v_exp_f32_e64 v4, v4 clamp\n"
    "v_exp_f32_e64 v6, v6 clamp\n"
    "v_max_f32 v5, s59, v188\n"
    "v_max_f32 v7, s59, v189\n"
    "v_add_f32 v188, v4, v5\n"
    "v_add_f32 v189, v6, v7\n"
    "s_waitcnt vmcnt(3)\n"
    "v_mfma_f32_32x32x16_bf16 v[96:111], v[232:235], v[64:67], v[96:111]\n"
    "v_mul_f32 v4, s70, v190\n"
    "v_mul_f32 v6, s70, v191\n"
    "v_exp_f32_e64 v4, v4 clamp\n"
    "v_exp_f32_e64 v6, v6 clamp\n"
    "v_max_f32 v5, s59, v190\n"
    "v_max_f32 v7, s59, v191\n"
    "v_add_f32 v190, v4, v5\n"
    "v_add_f32 v191, v6, v7\n"
    "s_waitcnt vmcnt(2)\n"
    "v_mfma_f32_32x32x16_bf16 v[96:111], v[232:235], v[68:71], v[96:111]\n"
    "v_mul_f32 v4, s70, v192\n"
    "v_mul_f32 v6, s70, v193\n"
    "v_exp_f32_e64 v4, v4 clamp\n"
    "v_exp_f32_e64 v6, v6 clamp\n"
    "v_max_f32 v5, s59, v192\n"
    "v_max_f32 v7, s59, v193\n"
    "v_add_f32 v192, v4, v5\n"
    "v_add_f32 v193, v6, v7\n"
    "s_waitcnt vmcnt(1)\n"
    "v_mfma_f32_32x32x16_bf16 v[96:111], v[232:235], v[72:75], v[96:111]\n"
    "v_mul_f32 v4, s70, v194\n"
    "v_mul_f32 v6, s70, v195\n"
    "v_exp_f32_e64 v4, v4 clamp\n"
    "v_exp_f32_e64 v6, v6 clamp\n"
    "v_max_f32 v5, s59, v194\n"
    "v_max_f32 v7, s59, v195\n"
    "v_add_f32 v194, v4, v5\n"
    "v_add_f32 v195, v6, v7\n"
    "s_waitcnt vmcnt(0)\n"
    "v_mfma_f32_32x32x16_bf16 v[96:111], v[232:235], v[76:79], v[96:111]\n"
    "v_mul_f32 v4, s70, v196\n"
    "v_mul_f32 v6, s70, v197\n"
    "v_exp_f32_e64 v4, v4 clamp\n"
    "v_exp_f32_e64 v6, v6 clamp\n"
    "v_max_f32 v5, s59, v196\n"
    "v_max_f32 v7, s59, v197\n"
    "v_add_f32 v196, v4, v5\n"
    "v_add_f32 v197, v6, v7\n"
    "v_mul_f32 v4, s70, v198\n"
    "v_mul_f32 v6, s70, v199\n"
    "v_exp_f32_e64 v4, v4 clamp\n"
    "v_exp_f32_e64 v6, v6 clamp\n"
    "v_max_f32 v5, s59, v198\n"
    "v_max_f32 v7, s59, v199\n"
    "v_add_f32 v198, v4, v5\n"
    "v_add_f32 v199, v6, v7\n"
    "v_cvt_pk_bf16_f32 v128, v160, v161\n"
    "v_cvt_pk_bf16_f32 v129, v162, v163\n"
    "v_cvt_pk_bf16_f32 v130, v164, v165\n"
    "v_cvt_pk_bf16_f32 v131, v166, v167\n"
    "v_cvt_pk_bf16_f32 v132, v168, v169\n"
    "v_cvt_pk_bf16_f32 v133, v170, v171\n"
    "v_cvt_pk_bf16_f32 v134, v172, v173\n"
    "v_cvt_pk_bf16_f32 v135, v174, v175\n"
    "v_cvt_pk_bf16_f32 v136, v176, v177\n"
    "v_cvt_pk_bf16_f32 v137, v178, v179\n"
    "v_cvt_pk_bf16_f32 v138, v180, v181\n"
    "v_cvt_pk_bf16_f32 v139, v182, v183\n"
    "v_cvt_pk_bf16_f32 v140, v184, v185\n"
    "v_cvt_pk_bf16_f32 v141, v186, v187\n"
    "v_cvt_pk_bf16_f32 v142, v188, v189\n"
    "v_cvt_pk_bf16_f32 v143, v190, v191\n"
    "v_cvt_pk_bf16_f32 v144, v192, v193\n"
    "v_cvt_pk_bf16_f32 v145, v194, v195\n"
    "v_cvt_pk_bf16_f32 v146, v196, v197\n"
    "v_cvt_pk_bf16_f32 v147, v198, v199\n"
    "v_cvt_pk_bf16_f32 v96, v96, v97\n"
    "v_cvt_pk_bf16_f32 v97, v98, v99\n"
    "v_cvt_pk_bf16_f32 v98, v100, v101\n"
    "v_cvt_pk_bf16_f32 v99, v102, v103\n"
    "s_mov_b64 exec, s[96:97]\n"
    "ds_write_b32 v216, v96\n"
    "ds_write_b32 v217, v97\n"
    "ds_write_b32 v218, v98\n"
    "ds_write_b32 v219, v99\n"
    "s_mov_b64 exec, -1\n"
    "v_mul_f32 v4, s70, v200\n"
    "v_exp_f32_e64 v4, -v4\n"
    "s_nop 0\n"
    "v_add_f32 v4, 1.0, v4\n"
    "v_rcp_f32 v15, v4\n"
    "s_lshl_b32 s79, s52, 13\n"
    "s_add_u32 s79, s79, 73728\n"
    "v_add_u32 v4, s79, v2\n"
    "s_lshl_b32 s79, s54, 2\n"
    "s_add_u32 s79, s79, s53\n"
    "s_lshl_b32 s80, s79, 2\n"
    "s_add_u32 s80, s80, s51\n"
    "s_lshl_b32 s83, s80, 7\n"
    "s_mul_i32 s79, s79, 36864\n"
    "s_lshl_b32 s80, s51, 10\n"
    "s_add_u32 s79, s79, s80\n"
    "s_add_u32 s79, s79, 2097152\n"
    "s_lshl_b32 s80, s52, 14\n"
    "s_add_u32 s84, s79, s80\n"
    "s_lshl_b32 s80, s52, 9\n"
    "s_add_u32 s85, s79, s80\n"
    "s_add_u32 s85, s85, 32768\n"
    "v_mov_b32 v8, s61\n"
    "v_mov_b32 v9, s62\n"
    "v_lshrrev_b32 v10, 2, v2\n"
    "s_add_u32 s79, s83, 143360\n"
    "s_bfm_b64 exec, 32, 0\n"
    "buffer_store_dword v15, v10, s[20:23], s79 offen sc1\n"
    "s_mov_b64 exec, -1\n"
    "s_waitcnt lgkmcnt(0)\n"
    "s_barrier\n"
    "ds_read_b128 v[16:19], v4 offset:0\n"
    "ds_read_b128 v[20:23], v4 offset:1024\n"
    "ds_read_b128 v[24:27], v4 offset:2048\n"
    "ds_read_b128 v[28:31], v4 offset:3072\n"
    "ds_read_b128 v[32:35], v4 offset:4096\n"
    "ds_read_b128 v[36:39], v4 offset:5120\n"
    "ds_read_b128 v[40:43], v4 offset:6144\n"
    "ds_read_b128 v[44:47], v4 offset:7168\n"
    "s_waitcnt lgkmcnt(7)\n"
    "v_mfma_f32_32x32x16_bf16 v[160:175], v[16:19], v[128:131], 0\n"
    "s_waitcnt lgkmcnt(6)\n"
    "v_mfma_f32_32x32x16_bf16 v[160:175], v[20:23], v[132:135], v[160:175]\n"
    "s_waitcnt lgkmcnt(5)\n"
    "v_mfma_f32_32x32x16_bf16 v[176:191], v[24:27], v[132:135], 0\n"
    "s_waitcnt lgkmcnt(4)\n"
    "v_mfma_f32_32x32x16_bf16 v[176:191], v[28:31], v[136:139], v[176:191]\n"
    "s_waitcnt lgkmcnt(3)\n"
    "v_mfma_f32_32x32x16_bf16 v[192:207], v[32:35], v[136:139], 0\n"
    "s_waitcnt lgkmcnt(2)\n"
    "v_mfma_f32_32x32x16_bf16 v[192:207], v[36:39], v[140:143], v[192:207]\n"
    "s_waitcnt lgkmcnt(1)\n"
    "v_mfma_f32_32x32x16_bf16 v[208:223], v[40:43], v[140:143], 0\n"
    "s_waitcnt lgkmcnt(0)\n"
    "v_mfma_f32_32x32x16_bf16 v[208:223], v[44:47], v[144:147], v[208:223]\n"
    "v_add_f32 v4, s59, v170\n"
    "v_mov_b32 v5, v4\n"
    "s_nop 1\n"
    "v_permlane32_swap_b32 v4, v5\n"
    "v_rcp_f32 v4, v4\n"
    "s_nop 0\n"
    "v_mul_f32 v4, v15, v4\n"
    "v_mul_f32 v160, v4, v160\n"
    "v_mul_f32 v161, v4, v161\n"
    "v_mul_f32 v162, v4, v162\n"
    "v_mul_f32 v163, v4, v163\n"
    "v_mul_f32 v164, v4, v164\n"
    "v_mul_f32 v165, v4, v165\n"
    "v_mul_f32 v166, v4, v166\n"
    "v_mul_f32 v167, v4, v167\n"
    "v_mul_f32 v168, v4, v168\n"
    "v_mul_f32 v169, v4, v169\n"
    "v_cvt_pk_bf16_f32 v160, v160, v161\n"
    "v_cvt_pk_bf16_f32 v161, v162, v163\n"
    "v_cvt_pk_bf16_f32 v162, v164, v165\n"
    "v_cvt_pk_bf16_f32 v163, v166, v167\n"
    "s_add_u32 s79, s84, 0\n"
    "buffer_store_dwordx4 v[160:163], v2, s[20:23], s79 offen sc1\n"
    "v_cvt_pk_bf16_f32 v48, v168, v169\n"
    "v_add_f32 v4, s59, v186\n"
    "v_mov_b32 v5, v4\n"
    "s_nop 1\n"
    "v_permlane32_swap_b32 v4, v5\n"
    "v_rcp_f32 v4, v4\n"
    "s_nop 0\n"
    "v_mul_f32 v4, v15, v4\n"
    "v_mul_f32 v176, v4, v176\n"
    "v_mul_f32 v177, v4, v177\n"
    "v_mul_f32 v178, v4, v178\n"
    "v_mul_f32 v179, v4, v179\n"
    "v_mul_f32 v180, v4, v180\n"
    "v_mul_f32 v181, v4, v181\n"
    "v_mul_f32 v182, v4, v182\n"
    "v_mul_f32 v183, v4, v183\n"
    "v_mul_f32 v184, v4, v184\n"
    "v_mul_f32 v185, v4, v185\n"
    "v_cvt_pk_bf16_f32 v176, v176, v177\n"
    "v_cvt_pk_bf16_f32 v177, v178, v179\n"
    "v_cvt_pk_bf16_f32 v178, v180, v181\n"
    "v_cvt_pk_bf16_f32 v179, v182, v183\n"
    "s_add_u32 s79, s84, 4096\n"
    "buffer_store_dwordx4 v[176:179], v2, s[20:23], s79 offen sc1\n"
    "v_cvt_pk_bf16_f32 v49, v184, v185\n"
    "v_add_f32 v4, s59, v202\n"
    "v_mov_b32 v5, v4\n"
    "s_nop 1\n"
    "v_permlane32_swap_b32 v4, v5\n"
    "v_rcp_f32 v4, v4\n"
    "s_nop 0\n"
    "v_mul_f32 v4, v15, v4\n"
    "v_mul_f32 v192, v4, v192\n"
    "v_mul_f32 v193, v4, v193\n"
    "v_mul_f32 v194, v4, v194\n"
    "v_mul_f32 v195, v4, v195\n"
    "v_mul_f32 v196, v4, v196\n"
    "v_mul_f32 v197, v4, v197\n"
    "v_mul_f32 v198, v4, v198\n"
    "v_mul_f32 v199, v4, v199\n"
    "v_mul_f32 v200, v4, v200\n"
    "v_mul_f32 v201, v4, v201\n"
    "v_cvt_pk_bf16_f32 v192, v192, v193\n"
    "v_cvt_pk_bf16_f32 v193, v194, v195\n"
    "v_cvt_pk_bf16_f32 v194, v196, v197\n"
    "v_cvt_pk_bf16_f32 v195, v198, v199\n"
    "s_add_u32 s79, s84, 8192\n"
    "buffer_store_dwordx4 v[192:195], v2, s[20:23], s79 offen sc1\n"
    "v_cvt_pk_bf16_f32 v50, v200, v201\n"
    "v_add_f32 v4, s59, v218\n"
    "v_mov_b32 v5, v4\n"
    "s_nop 1\n"
    "v_permlane32_swap_b32 v4, v5\n"
    "v_rcp_f32 v4, v4\n"
    "s_nop 0\n"
    "v_mul_f32 v4, v15, v4\n"
    "v_mul_f32 v208, v4, v208\n"
    "v_mul_f32 v209, v4, v209\n"
    "v_mul_f32 v210, v4, v210\n"
    "v_mul_f32 v211, v4, v211\n"
    "v_mul_f32 v212, v4, v212\n"
    "v_mul_f32 v213, v4, v213\n"
    "v_mul_f32 v214, v4, v214\n"
    "v_mul_f32 v215, v4, v215\n"
    "v_mul_f32 v216, v4, v216\n"
    "v_mul_f32 v217, v4, v217\n"
    "v_cvt_pk_bf16_f32 v208, v208, v209\n"
    "v_cvt_pk_bf16_f32 v209, v210, v211\n"
    "v_cvt_pk_bf16_f32 v210, v212, v213\n"
    "v_cvt_pk_bf16_f32 v211, v214, v215\n"
    "s_add_u32 s79, s84, 12288\n"
    "buffer_store_dwordx4 v[208:211], v2, s[20:23], s79 offen sc1\n"
    "v_cvt_pk_bf16_f32 v51, v216, v217\n"
    "s_bfm_b64 exec, 32, 0\n"
    "buffer_store_dwordx4 v[48:51], v2, s[20:23], s85 offen sc1\n"
    "s_mov_b64 exec, -1\n"
    "s_lshl_b32 s79, s52, 3\n"
    "s_add_u32 s83, s83, s79\n"
    "s_waitcnt vmcnt(0)\n"
    "s_add_u32 s78, s83, 176128\n"
    "s_mov_b64 exec, 1\n"
    "buffer_store_dwordx2 v[8:9], off, s[20:23], s78 sc1\n"
    "s_mov_b64 exec, -1\n"
    "Lend_2:\n"
    "Lfinish:\n"
    : "+{v0}"(tid)
    : "s"(__builtin_amdgcn_kernarg_segment_ptr()), "s"(blockIdx.x)
    : "memory", "vcc", "scc", "v1", "v2", "v3", "v4", "v5", "v6", "v7", "v8", "v9", "v10", "v11", "v12", "v13", "v14", "v15", "v16", "v17", "v18", "v19", "v20", "v21", "v22", "v23", "v24", "v25", "v26", "v27", "v28", "v29", "v30", "v31", "v32", "v33", "v34", "v35", "v36", "v37", "v38", "v39", "v40", "v41", "v42", "v43", "v44", "v45", "v46", "v47", "v48", "v49", "v50", "v51", "v52", "v53", "v54", "v55", "v56", "v57", "v58", "v59", "v60", "v61", "v62", "v63", "v64", "v65", "v66", "v67", "v68", "v69", "v70", "v71", "v72", "v73", "v74", "v75", "v76", "v77", "v78", "v79", "v80", "v81", "v82", "v83", "v84", "v85", "v86", "v87", "v88", "v89", "v90", "v91", "v92", "v93", "v94", "v95", "v96", "v97", "v98", "v99", "v100", "v101", "v102", "v103", "v104", "v105", "v106", "v107", "v108", "v109", "v110", "v111", "v112", "v113", "v114", "v115", "v116", "v117", "v118", "v119", "v120", "v121", "v122", "v123", "v124", "v125", "v126", "v127", "v128", "v129", "v130", "v131", "v132", "v133", "v134", "v135", "v136", "v137", "v138", "v139", "v140", "v141", "v142", "v143", "v144", "v145", "v146", "v147", "v148", "v149", "v150", "v151", "v152", "v153", "v154", "v155", "v156", "v157", "v158", "v159", "v160", "v161", "v162", "v163", "v164", "v165", "v166", "v167", "v168", "v169", "v170", "v171", "v172", "v173", "v174", "v175", "v176", "v177", "v178", "v179", "v180", "v181", "v182", "v183", "v184", "v185", "v186", "v187", "v188", "v189", "v190", "v191", "v192", "v193", "v194", "v195", "v196", "v197", "v198", "v199", "v200", "v201", "v202", "v203", "v204", "v205", "v206", "v207", "v208", "v209", "v210", "v211", "v212", "v213", "v214", "v215", "v216", "v217", "v218", "v219", "v220", "v221", "v222", "v223", "v224", "v225", "v226", "v227", "v228", "v229", "v230", "v231", "v232", "v233", "v234", "v235", "v236", "v237", "v238", "v239", "v240", "v241", "v242", "v243", "v244", "v245", "v246", "v247", "v248", "v249", "v250", "v251", "v252", "v253", "v254", "v255", "s8", "s9", "s10", "s11", "s12", "s13", "s14", "s15", "s16", "s17", "s18", "s19", "s20", "s21", "s22", "s23", "s24", "s25", "s26", "s27", "s28", "s29", "s30", "s31", "s32", "s33", "s34", "s35", "s36", "s37", "s38", "s39", "s40", "s41", "s42", "s43", "s44", "s45", "s46", "s47", "s48", "s49", "s50", "s51", "s52", "s53", "s54", "s55", "s56", "s57", "s58", "s59", "s60", "s61", "s62", "s63", "s64", "s65", "s66", "s67", "s68", "s69", "s70", "s71", "s72", "s73", "s74", "s75", "s76", "s77", "s78", "s79", "s80", "s81", "s82", "s83", "s84", "s85", "s86", "s87", "s88", "s89", "s90", "s91", "s92", "s93", "s94", "s95", "s96", "s97", "s98", "s99");
}

extern "C" void kernel_launch(void* const* d_in, const int* in_sizes, int n_in, void* d_out, int out_size, void* d_ws, size_t ws_size, hipStream_t stream) {
  Args a;
  a.x = d_in[0]; a.wqkv = d_in[1]; a.wo = d_in[2]; a.w1 = d_in[4]; a.b1 = d_in[5]; a.w2 = d_in[6]; a.gw = d_in[7]; a.out = d_out; a.ws = d_ws;
  (void)hipFuncSetAttribute((const void*)fused_kernel, hipFuncAttributeMaxDynamicSharedMemorySize, 163840);
  fused_kernel<<<256, 512, 163840, stream>>>(a);
}
```
